```python
import torch

def _softmax(x, axis=-1):
    return torch.softmax(x, dim=axis)
def _gelu(x):
    return torch.nn.functional.gelu(x, approximate="tanh")
def _silu(x):
    return torch.nn.functional.silu(x)
def _sigmoid(x):
    return torch.sigmoid(x)
def _layernorm(x, axis=-1, eps=1e-5):
    m = x.mean(dim=axis, keepdim=True)
    v = x.var(dim=axis, keepdim=True, unbiased=False)
    return (x - m) / torch.sqrt(v + eps)
def _scatter_add(tgt, idx, src, axis=0):
    return tgt.clone().index_add_(axis, idx, src)
def _segment_sum(src, seg, n):
    out = torch.zeros((n,) + src.shape[1:], dtype=src.dtype)
    return out.index_add_(0, seg, src)
def _pad(x, pw):
    flat = tuple(v for pair in reversed(pw) for v in pair)
    return torch.nn.functional.pad(x, flat)

kernel_name = 'sequential_preLN__mha_block__mixer_s_mlp__prompt'

def setup_inputs():

    _w = (lambda a: (lambda t: t.to(torch.bfloat16) if t.is_floating_point() else t)(torch.as_tensor(a)))
    def _A_setup():
        B, S, D, H = 1, 2048, 512, 32
        return {
            "x": _w((lambda *s: torch.randn(*s, dtype=torch.bfloat16) * 0.05)(B, S, D)),
            "wqkv": _w((lambda *s: torch.randn(*s, dtype=torch.bfloat16) * 0.05)(D, 3 * D)),
            "wo": _w((lambda *s: torch.randn(*s, dtype=torch.bfloat16) * 0.05)(D, D)),
        }
    _A_in = _A_setup()

    def _B_setup():
        B, S, D, F = 1, 2048, 512, 1792
        return {
            "x":  _w((lambda *s: torch.randn(*s, dtype=torch.bfloat16) * 0.05)(B, S, D)),
            "w1": _w((lambda *s: torch.randn(*s, dtype=torch.bfloat16) * 3.2)(D, D) * (D ** -0.5)),
            "w2": _w((lambda *s: torch.randn(*s, dtype=torch.bfloat16) * 3.2)(D, D) * (D ** -0.5)),
            "w3": _w((lambda *s: torch.randn(*s, dtype=torch.bfloat16) * 3.2)(D, F) * (D ** -0.5)),
            "w4": _w((lambda *s: torch.randn(*s, dtype=torch.bfloat16) * 3.2)(F, D) * (F ** -0.5)),
        }
    _B_in = _B_setup()

    return {
        "A_x": _A_in["x"],
        "A_wqkv": _A_in["wqkv"],
        "A_wo": _A_in["wo"],
        "B_x": _B_in["x"],
        "B_w1": _B_in["w1"],
        "B_w2": _B_in["w2"],
        "B_w3": _B_in["w3"],
        "B_w4": _B_in["w4"],
    }

def reference(A_x, A_wqkv, A_wo, B_x, B_w1, B_w2, B_w3, B_w4):
    def _A_ref(x, wqkv, wo):
        B, S, D = x.shape
        H = 32
        Dh = D // H
        qkv = (lambda x, *d: x.permute(*d))((x @ wqkv).reshape(B, S, 3, H, Dh), 2, 0, 3, 1, 4)
        q, k, v = qkv[0], qkv[1], qkv[2]
        scores = torch.einsum("bhqd,bhkd->bhqk", q, k) * (Dh ** -0.5)
        p = _softmax(scores, axis=-1)
        o = torch.einsum("bhqk,bhkd->bhqd", p, v)
        return (lambda x, *d: x.permute(*d))(o, 0, 2, 1, 3).reshape(B, S, D) @ wo

    def _B_ref(x, w1, w2, w3, w4):
        B, S, D = x.shape
        h = _gelu(_layernorm(x, axis=-1) @ w1)
        c = D // 4
        g0 = _pad(h[:, :, 0*c:1*c], ((0, 0), (1, 0), (0, 0)))[:, :S, :]
        g1 = _pad(h[:, :, 1*c:2*c], ((0, 0), (0, 1), (0, 0)))[:, 1:, :]
        g2 = _pad(h[:, :, 2*c:3*c], ((0, 0), (2, 0), (0, 0)))[:, :S, :]
        g3 = _pad(h[:, :, 3*c:   ], ((0, 0), (0, 2), (0, 0)))[:, 2:, :]
        hs = torch.concatenate([g0, g1, g2, g3], axis=-1) @ w2
        x1 = x + hs
        h2 = _gelu(_layernorm(x1, axis=-1) @ w3) @ w4
        return x1 + h2

    x = A_x
    h = x + _A_ref(_layernorm(x, axis=-1), A_wqkv, A_wo)
    h2 = h + _B_ref(_layernorm(h, axis=-1), B_w1, B_w2, B_w3, B_w4)
    return h2
```

```cpp
#include <hip/hip_runtime.h>
#include <hip/hip_bf16.h>
typedef __attribute__((ext_vector_type(8))) __bf16 bf16x8;
typedef __attribute__((ext_vector_type(4))) float f32x4;
typedef unsigned short u16;
__device__ __forceinline__ float bf2f(u16 x){ return __uint_as_float((unsigned)x<<16); }
__device__ __forceinline__ u16 f2bf(float f){ __bf16 b=(__bf16)f; return *(u16*)&b; }
__device__ __forceinline__ float rb(float f){ return bf2f(f2bf(f)); }
__device__ __forceinline__ float gelu(float x){ float u=0.7978845608f*(x+0.044715f*x*x*x); return x/(1.f+__expf(-2.f*u)); }

__global__ void ln_k(const u16* in, u16* out1, u16* out2) {
  int row = blockIdx.x*4 + (threadIdx.x>>6), l = threadIdx.x&63;
  float v[8]; for (int i=0;i<8;i++) v[i]=bf2f(in[row*512+l*8+i]);
  for (int p=0;p<2;p++){
    float s=0; for (int i=0;i<8;i++) s+=v[i];
    for (int o=32;o;o>>=1) s+=__shfl_xor(s,o);
    float m=s/512; float q=0; for (int i=0;i<8;i++) q+=(v[i]-m)*(v[i]-m);
    for (int o=32;o;o>>=1) q+=__shfl_xor(q,o);
    float r=rsqrtf(q/512+1e-5f);
    for (int i=0;i<8;i++) v[i]=rb((v[i]-m)*r);
    u16* o = p==0?out1:out2;
    if (!o) break;
    for (int i=0;i<8;i++) o[row*512+l*8+i]=f2bf(v[i]);
  }
}
template<int MODE,int SHIFT>
__global__ void __launch_bounds__(256) gemm_k(const u16* A,const u16* W,u16* C,int N,int K,const u16* r1,const u16* r2){
  __shared__ u16 sA[64][40], sB[64][40];
  int bm=blockIdx.y*64,bn=blockIdx.x*64,t=threadIdx.x,w=t>>6,l=t&63;
  f32x4 acc[4]={};
  for (int k0=0;k0<K;k0+=32){
    { int r=t>>2,c=(t&3)*8; int row=bm+r;
      if (SHIFT){ int g=(k0+c)>>7; int sh = g==0?-1:g==1?1:g==2?-2:2; row+=sh; }
      uint4 v=make_uint4(0,0,0,0); if (row>=0&&row<2048) v=*(const uint4*)(A+(size_t)row*K+k0+c);
      *(uint4*)&sA[r][c]=v; }
    { int kk=t>>3,c=(t&7)*8; uint4 v=*(const uint4*)(W+(size_t)(k0+kk)*N+bn+c); u16* p=(u16*)&v; for (int i=0;i<8;i++) sB[c+i][kk]=p[i]; }
    __syncthreads();
    bf16x8 a=*(bf16x8*)&sA[w*16+(l&15)][(l>>4)*8];
    for (int f=0;f<4;f++){ bf16x8 b=*(bf16x8*)&sB[f*16+(l&15)][(l>>4)*8]; acc[f]=__builtin_amdgcn_mfma_f32_16x16x32_bf16(a,b,acc[f],0,0,0);}
    __syncthreads();
  }
  for (int f=0;f<4;f++) for (int r=0;r<4;r++){ int row=bm+w*16+(l>>4)*4+r,col=bn+f*16+(l&15); float v=rb(acc[f][r]);
    if (MODE==1) v=gelu(v);
    if (MODE==2) v=v+bf2f(r1[row*N+col]);
    if (MODE==3) v=rb(v+bf2f(r1[row*N+col]))+bf2f(r2[row*N+col]);
    C[(size_t)row*N+col]=f2bf(v);}
}
__global__ void __launch_bounds__(256) attn_k(const u16* qkv,u16* O){
  __shared__ float sK[128][16], sV[128][16];
  int h=blockIdx.y,q=blockIdx.x*256+threadIdx.x; float qv[16],o[16]={},m=-1e30f,lsum=0;
  for (int d=0;d<16;d++) qv[d]=bf2f(qkv[q*1536+h*16+d])*0.25f;
  for (int k0=0;k0<2048;k0+=128){
    __syncthreads();
    for (int i=threadIdx.x;i<2048;i+=256){ int kk=i>>4,d=i&15; sK[kk][d]=bf2f(qkv[(k0+kk)*1536+512+h*16+d]); sV[kk][d]=bf2f(qkv[(k0+kk)*1536+1024+h*16+d]); }
    __syncthreads();
    for (int kk=0;kk<128;kk++){ float s=0; for (int d=0;d<16;d++) s+=qv[d]*sK[kk][d];
      float mn=fmaxf(m,s); float al=__expf(m-mn),p=__expf(s-mn); lsum=lsum*al+p; for (int d=0;d<16;d++) o[d]=o[d]*al+p*sV[kk][d]; m=mn; }
  }
  for (int d=0;d<16;d++) O[q*512+h*16+d]=f2bf(o[d]/lsum);
}
extern "C" void kernel_launch(void* const* d_in,const int* in_sizes,int n_in,void* d_out,int out_size,void* d_ws,size_t ws_size,hipStream_t s){
  const u16 *x=(const u16*)d_in[0],*wqkv=(const u16*)d_in[1],*wo=(const u16*)d_in[2],*w1=(const u16*)d_in[4],*w2=(const u16*)d_in[5],*w3=(const u16*)d_in[6],*w4=(const u16*)d_in[7];
  u16* ws=(u16*)d_ws; const size_t M=2048*512;
  u16 *xn=ws,*qkv=ws+M,*O=ws+4*M,*h=ws+5*M,*y=ws+6*M,*z=ws+7*M,*hh=ws+8*M,*x1=ws+9*M,*x1n=ws+10*M,*G=ws+11*M;
  ln_k<<<512,256,0,s>>>(x,xn,nullptr);
  gemm_k<0,0><<<dim3(24,32),256,0,s>>>(xn,wqkv,qkv,1536,512,nullptr,nullptr);
  attn_k<<<dim3(8,32),256,0,s>>>(qkv,O);
  gemm_k<2,0><<<dim3(8,32),256,0,s>>>(O,wo,h,512,512,x,nullptr);
  ln_k<<<512,256,0,s>>>(h,y,z);
  gemm_k<1,0><<<dim3(8,32),256,0,s>>>(z,w1,hh,512,512,nullptr,nullptr);
  gemm_k<2,1><<<dim3(8,32),256,0,s>>>(hh,w2,x1,512,512,y,nullptr);
  ln_k<<<512,256,0,s>>>(x1,x1n,nullptr);
  gemm_k<1,0><<<dim3(28,32),256,0,s>>>(x1n,w3,G,1792,512,nullptr,nullptr);
  gemm_k<3,0><<<dim3(8,32),256,0,s>>>(G,w4,(u16*)d_out,512,1792,x1,h);
}
```

```cpp
#include <hip/hip_runtime.h>
#include <type_traits>

typedef unsigned u32;
typedef unsigned long long u64;
typedef __attribute__((ext_vector_type(4))) u32 u32x4;
typedef __attribute__((ext_vector_type(2))) u32 u32x2;
typedef __attribute__((ext_vector_type(4))) short s16x4;
typedef __attribute__((ext_vector_type(2))) __bf16 bf16x2;
typedef __attribute__((ext_vector_type(8))) __bf16 bf16x8;
typedef __attribute__((ext_vector_type(16))) __bf16 bf16x16;
typedef __attribute__((ext_vector_type(2))) float f32x2;
typedef __attribute__((ext_vector_type(4))) float f32x4;
typedef __attribute__((ext_vector_type(8))) float f32x8;
typedef __attribute__((ext_vector_type(16))) float f32x16;
typedef __attribute__((ext_vector_type(32))) float f32x32;
typedef __amdgpu_buffer_rsrc_t srd_t;
#define LAS __attribute__((address_space(3)))
#define GAS __attribute__((address_space(1)))
#define RLX_AGENT __ATOMIC_RELAXED, __HIP_MEMORY_SCOPE_AGENT
#define DI __device__ __forceinline__

constexpr u32 WS_EPOCH = 0;
constexpr u32 WS_KVFLAG = 4096;
constexpr u32 WS_WGFLAG = 16384;
constexpr u32 WS_STAMP = 32768;
constexpr u32 WS_K = 1u << 20;
constexpr u32 WS_V = 3u << 20;
constexpr u32 WS_WOT = 5u << 20;
constexpr u32 WS_W1T = WS_WOT + (512u << 10);
constexpr u32 WS_W2T = WS_WOT + (1024u << 10);
constexpr u32 WS_W3T = WS_WOT + (1536u << 10);
constexpr u32 WS_W4T = WS_W3T + (1792u << 10);
constexpr u32 WS_O = 10u << 20;
constexpr u32 WS_H = 12u << 20;
constexpr u32 WS_HH = 14u << 20;
constexpr u32 WS_X1 = 16u << 20;
constexpr u32 WS_G = 18u << 20;
constexpr u32 WS_HST = 25u << 20;
constexpr u32 WS_XST = WS_HST + (256u << 10);
constexpr u32 WS_END = WS_XST + (256u << 10);
constexpr u32 SENT = 0xFFFFFFFFu;
constexpr int SPIN_CAP = 200000;

constexpr u32 L_RX = 0, L_RK = 32768, L_RV = 65536;
constexpr u32 L_WQ = 98304;
constexpr u32 L_RST = 0;
constexpr u32 L_TSC = L_RK;
constexpr u32 L_QX = L_RV;
constexpr u32 L_CNT = 131072;
constexpr u32 L_XCH = 131072 + 1024;

DI u32 opaque(u32 v) { asm volatile("" : "+v"(v)); return v; }
DI u32 rfl(u32 v) { return __builtin_amdgcn_readfirstlane(v); }
DI bf16x8 as_bf8(u32x4 v) { return __builtin_bit_cast(bf16x8, v); }
DI u32 pk_bf16(float a, float b) { f32x2 p = {a, b}; return __builtin_bit_cast(u32, __builtin_convertvector(p, bf16x2)); }
DI float dot2(u32 a, u32 b, float c) { return __builtin_amdgcn_fdot2_f32_bf16(__builtin_bit_cast(bf16x2, a), __builtin_bit_cast(bf16x2, b), c, false); }
DI float swap32_add(float v) { auto r = __builtin_amdgcn_permlane32_swap(__float_as_uint(v), __float_as_uint(v), false, false); return __uint_as_float(r[0]) + __uint_as_float(r[1]); }
DI float swap16_add(float v) { auto r = __builtin_amdgcn_permlane16_swap(__float_as_uint(v), __float_as_uint(v), false, false); return __uint_as_float(r[0]) + __uint_as_float(r[1]); }
DI float swap32_max(float v) { auto r = __builtin_amdgcn_permlane32_swap(__float_as_uint(v), __float_as_uint(v), false, false); return fmaxf(__uint_as_float(r[0]), __uint_as_float(r[1])); }
DI f32x16 mfma32(u32x4 a, u32x4 b, f32x16 c) { return __builtin_amdgcn_mfma_f32_32x32x16_bf16(as_bf8(a), as_bf8(b), c, 0, 0, 0); }
DI f32x4 mfma16(u32x4 a, u32x4 b, f32x4 c) { return __builtin_amdgcn_mfma_f32_16x16x32_bf16(as_bf8(a), as_bf8(b), c, 0, 0, 0); }
DI f32x16 smf(u32x4 a, f32x8 p, f32x16 c) { return __builtin_amdgcn_smfmac_f32_32x32x32_bf16(as_bf8(a), __builtin_bit_cast(bf16x16, p), c, 0xDDDD, 0, 0); }
DI u32x4 lds_r128(LAS char* l, u32 off) { return *(LAS u32x4*)(l + off); }
DI void lds_w128(LAS char* l, u32 off, u32x4 v) { *(LAS u32x4*)(l + off) = v; }
DI u32x2 lds_tr(LAS char* l, u32 off) { return __builtin_bit_cast(u32x2, __builtin_amdgcn_ds_read_tr16_b64_v4i16((LAS s16x4*)(l + off))); }
DI u32x4 lds_tr2(LAS char* l, u32 off0, u32 off1) { u32x2 a = lds_tr(l, off0), b = lds_tr(l, off1); return u32x4{a.x, a.y, b.x, b.y}; }
DI void dma16(srd_t srd, u32 voff, u32 soff, u32 ldsaddr) {
  asm volatile("s_mov_b32 m0, %0\n\ts_nop 0\n\tbuffer_load_dwordx4 %1, %2, %3 offen lds" ::"s"(ldsaddr), "v"(voff), "s"(srd), "s"(soff) : "memory");
}
DI void dma16c(srd_t srd, u32 voff, u32 soff, u32 ldsaddr) {
  asm volatile("s_mov_b32 m0, %0\n\ts_nop 0\n\tbuffer_load_dwordx4 %1, %2, %3 offen sc1 lds" ::"s"(ldsaddr), "v"(voff), "s"(srd), "s"(soff) : "memory");
}
template <int N> DI void wait_vm() { asm volatile("s_waitcnt vmcnt(%0)" ::"n"(N) : "memory"); }
DI void wait_lgkm0() { asm volatile("s_waitcnt lgkmcnt(0)" ::: "memory"); }
DI void bar() { asm volatile("s_waitcnt lgkmcnt(0)\n\ts_barrier" ::: "memory"); }
DI u32x4 ld_c(srd_t s, u32 voff) { return __builtin_amdgcn_raw_buffer_load_b128(s, voff, 0, 16); }
DI void st_c(srd_t s, u32 voff, u32x4 v) { __builtin_amdgcn_raw_buffer_store_b128(v, s, voff, 0, 16); }
DI u32x4 clampS(u32x4 v) { return u32x4{min(v.x, SENT - 1), min(v.y, SENT - 1), min(v.z, SENT - 1), min(v.w, SENT - 1)}; }

#define STAMP(i) do {} while (0)
#define STAMPR(i) do {} while (0)

DI void ring_issue(u32x4& kf, u32x4& va, u32x4& vb, srd_t srd, u32 kvoff, u32 vvoff, u32 soff, u64 vmask) {
  asm volatile(
      "buffer_load_dwordx4 %0, %3, %5, %6 offen sc1\n\t"
      "s_mov_b64 exec, %7\n\t"
      "buffer_load_dwordx4 %1, %4, %5, %6 offen sc1\n\t"
      "buffer_load_dwordx4 %2, %4, %5, %6 offen offset:512 sc1\n\t"
      "s_mov_b64 exec, -1"
      : "+v"(kf), "+v"(va), "+v"(vb)
      : "v"(kvoff), "v"(vvoff), "s"(srd), "s"(soff), "s"(vmask)
      : "memory");
}
#define RING_SC " sc1"
#define RING_PRE ""
#define A_EXP(r) "v_exp_f32 v[" #r "], v[" #r "]\n\t"
#define A_EXP2(r) A_EXP(r) A_EXP(r + 1)
#define A_EXP3(r) A_EXP2(r) A_EXP(r + 2)
#define A_EXP5(r) A_EXP3(r) A_EXP2(r + 3)
#define A_EXP6(r) A_EXP3(r) A_EXP3(r + 3)
#define A_R4(r) "v[" #r ":" #r "+3]"
#define A_SMF(o, a, p) "v_smfmac_f32_32x32x32_bf16 v[" #o ":" #o "+15], " A_R4(a) ", v[" #p ":" #p "+7], %[idx]\n\t"
#define A_MFMA(d, k, q) "v_mfma_f32_32x32x16_bf16 v[" #d ":" #d "+15], " A_R4(k) ", %[" q "], 0\n\t"
#define A_RING(ip)                                                                   \
  "s_add_u32 %[T], %[T], 2048\n\t"                                                    \
  "s_and_b32 %[t], %[T], 0xfc00\n\t"                                                      \
  "s_or_b32 %[t], %[t], %[hb]\n\t"                                                        \
  RING_PRE "buffer_load_dwordx4 " A_R4(64 + 12 * ip) ", %[kv], %[srd], %[t] offen" RING_SC "\n\t"         \
  EXECW "s_mov_b64 exec, %[vm]\n\t"                                                             \
  RING_PRE "buffer_load_dwordx4 " A_R4(68 + 12 * ip) ", %[vv], %[srd], %[t] offen" RING_SC "\n\t"         \
  RING_PRE "buffer_load_dwordx4 " A_R4(72 + 12 * ip) ", %[vv], %[srd], %[t] offen offset:512" RING_SC "\n\t" \
  EXECW "s_mov_b64 exec, -1\n\t"
#define A_SENT(i)                                                                         \
  "s_cmp_eq_u32 %[outer], 0\n\t"                                                          \
  "s_cbranch_scc0 2f\n\t"                                                                 \
  "s_add_u32 %[t], %[sso], 1024*" #i "\n\t"                                               \
  "s_min_u32 %[t], %[t], %[ssm]\n\t"                                                      \
  "buffer_store_dwordx4 %[sent], %[sv], %[srd], %[t] offen sc1\n\t"                       \
  "s_branch 2f\n"
#define A_DMA(i)                                                                          \
  "1:\n\t"                                                                                \
  "s_add_u32 m0, %[dl], 1024*" #i "\n\t"                                                  \
  "s_add_u32 %[t], %[dso], 1024*" #i "\n\t"                                               \
  "buffer_load_dwordx4 %[sv], %[srd], %[t] offen sc1 lds\n\t"                             \
  "s_add_u32 m0, %[dl], 0x10000+1024*" #i "\n\t"                                          \
  "s_add_u32 %[t], %[dso], 0x80000+1024*" #i "\n\t"                                       \
  "buffer_load_dwordx4 %[sv], %[srd], %[t] offen sc1 lds\n"                               \
  "2:\n\t"
#define A_MEM0 A_RING(7) "s_cmp_eq_u32 %[outer], 3\n\ts_cbranch_scc1 1f\n\t" A_SENT(0) A_DMA(0)
#define A_MEM(i, ip) "s_cmp_eq_u32 %[outer], 3\n\ts_cbranch_scc1 1f\n\t" A_RING(ip) A_SENT(i) A_DMA(i)
#define EXECW ""
#define A_FLAGS                                                                           \
  "s_cmp_eq_u32 %[outer], 2\n\ts_cbranch_scc0 3f\n\t"                                     \
  "s_mov_b32 m0, %[fl]\n\ts_movk_i32 %[t], 0x4000\n\t"                                    \
  "buffer_load_dwordx4 %[sv], %[srd], %[t] offen sc1 lds\n\t"                             \
  "buffer_load_dwordx4 %[sv], %[srd], %[t] offen offset:1024 sc1 lds\n"                   \
  "3:\n\t"
#define A_STEP(i, ip, in, N, MEM, X)                                                      \
  PRIO_PRE "s_setprio 3-(" #i "/2)\n\t" A_EXP2(32) A_SMF(16, 72 + 12 * ip, 56) A_EXP3(34) A_MFMA(48, 64 + 12 * i, "qb") MEM     \
  A_EXP5(37) A_SMF(0, 68 + 12 * i, 32) A_EXP6(42)                                         \
  A_EXP2(48) A_SMF(0, 72 + 12 * i, 40) A_EXP3(50) "s_waitcnt vmcnt(" #N ")\n\t"           \
  A_MFMA(32, 64 + 12 * in, "qa") X A_EXP5(53) A_SMF(16, 68 + 12 * i, 48) A_EXP6(58)
#define PRIO_PRE ""
#define ROUND_BAR "s_barrier"
#define A_MV(r, x) "v_mov_b64 v[" #r ":" #r "+1], " x "\n\t"
#define A_INIT1(r) A_MV(r, "%[one]") A_MV(r + 2, "%[one]") A_MV(r + 4, "%[one]") A_MV(r + 6, "%[one]")
#define A_INIT0(r) A_MV(r, "0") A_MV(r + 2, "0") A_MV(r + 4, "0") A_MV(r + 6, "0")
static_assert(WS_WGFLAG == 0x4000 && WS_W1T - WS_WOT == 0x80000, "asm constants");
DI void ring_wait(int n, u32x4& kf, u32x4& va, u32x4& vb) {
  asm volatile("s_waitcnt vmcnt(%3)" : "+v"(kf), "+v"(va), "+v"(vb) : "n"(n) : "memory");
}
DI f32x8 exp_lo(f32x16 s) { f32x8 p; for (int i = 0; i < 8; i++) p[i] = __builtin_amdgcn_exp2f(s[i]); return p; }
DI f32x8 exp_hi(f32x16 s) { f32x8 p; for (int i = 0; i < 8; i++) p[i] = __builtin_amdgcn_exp2f(s[8 + i]); return p; }

template <int IMM> DI void ald_c(u32x4& d, srd_t s, u32 voff, u32 soff) {
  asm volatile("buffer_load_dwordx4 %0, %1, %2, %3 offen offset:%4 sc1" : "=v"(d) : "v"(voff), "s"(s), "s"(soff), "n"(IMM));
}
DI void ald_if(u64 m, u32x4& d, srd_t s, u32 voff, u32 soff) {
  asm volatile("s_cmp_eq_u64 %4, 0\n\ts_cbranch_scc1 1f\n\tbuffer_load_dwordx4 %0, %1, %2, %3 offen sc1 ; RF\n1:" : "+v"(d) : "v"(voff), "s"(s), "s"(soff), "s"(m) : "scc");
}
template <int N> DI void vm_wait(u32x4& a) { asm volatile("s_waitcnt vmcnt(%1)" : "+v"(a) : "n"(N)); }
template <int N> DI void vm_wait(u32x4& a, u32x4& b) { asm volatile("s_waitcnt vmcnt(%2)" : "+v"(a), "+v"(b) : "n"(N)); }
DI void rf_wait(u32x4& a) { asm volatile("s_waitcnt vmcnt(0) ; RF" : "+v"(a)); }
DI void opaque_s(u32& v) { asm volatile("" : "+s"(v)); }
DI u32 clamp1(u32 v) { return min(v, SENT - 1); }
DI u32 umax3(u32 a, u32 b, u32 c) { return max(max(a, b), c); }
DI u32 smax(u32 m, u32x4 v) { return umax3(umax3(m, v.x, v.y), v.z, v.w); }
template <int I, int N> struct Unr {
  template <class F> static DI void go(F&& f) { f(std::integral_constant<int, I>{}); Unr<I + 1, N>::go(f); }
};
template <int N> struct Unr<N, N> { template <class F> static DI void go(F&&) {} };
template <int NF> DI void refetch(u32x4* a, srd_t s, const u32* voff, const u32* soff) {
#pragma unroll 1
  for (int spin = 0; spin < SPIN_CAP; spin++) {
    __builtin_amdgcn_s_sleep(2);
    u64 bad = 0;
    Unr<0, NF>::go([&](auto k) {
      const u64 m = __ballot(smax(0, a[k]) == SENT);
      ald_if(m, a[k], s, voff[k], soff[k]);
      bad |= m;
    });
    if (!bad) break;
    Unr<0, NF>::go([&](auto k) { rf_wait(a[k]); });
  }
}
DI float gelu(float x) {
  const float z = x * fmaf(x * x, -0.10294324f, -2.3022082f);
  return x * __builtin_amdgcn_rcpf(1.f + __builtin_amdgcn_exp2f(z));
}
DI u32 split_hl(float t) { const float hi = __uint_as_float(__float_as_uint(t) & 0xFFFF0000u); return pk_bf16(hi, t - hi); }
DI float bf_lo(u32 v) { return __uint_as_float(v << 16); }
DI float bf_hi(u32 v) { return __uint_as_float(v & 0xFFFF0000u); }
#define D1 2
#define D2 2
#define D3 2
#define D4 2
#define D5 2

__global__ void __launch_bounds__(512, 2)
fused_block(const char* __restrict__ px, const char* __restrict__ pwqkv, const char* __restrict__ pwo, const char* __restrict__ pw1,
            const char* __restrict__ pw2, const char* __restrict__ pw3, const char* __restrict__ pw4, char* __restrict__ pout, char* __restrict__ pws) {
  __shared__ __attribute__((aligned(16))) char lds_[163840];
  LAS char* lds = (LAS char*)lds_;
  const u32 lane = __builtin_amdgcn_mbcnt_hi(~0u, __builtin_amdgcn_mbcnt_lo(~0u, 0u));
  const u32 w = rfl(threadIdx.x >> 6);
  const u32 b = blockIdx.x, xcd = b & 7, jj = b >> 3;
  const u32 rb = 8 * (xcd >> 1) + (jj >> 2), hq = 4 * (xcd & 1) + (jj & 3);
  STAMP(0);
  STAMPR(62);
  asm volatile("" ::"s"(px), "s"(pwqkv), "s"(pwo), "s"(pw1), "s"(pw2), "s"(pw3), "s"(pw4), "s"(pout), "s"(pws));
  STAMP(20);
  const srd_t sws = __builtin_amdgcn_make_buffer_rsrc(pws, 0, WS_END, 0x00020000);
  const u64 ep = __hip_atomic_load((GAS u64*)(pws + WS_EPOCH), RLX_AGENT);
  const u32 h = lane >> 5, r31 = lane & 31;
  const u32 rot = (rb + 2 * hq) & 7;
  u64 tag;
  f32x32 acc, sp, rg0, rg1, rg2;
  const u32 kh = w & 1, hl = w >> 1, hd = 4 * hq + hl;
  const u32 kvoff = WS_K + lane * 16, vvoff = WS_V + h * 256 + (lane & 15) * 16;
  const u64 vmask = 0x0000FFFF0000FFFFull;
  const u32 hbase = hd * 65536;
  const u32 T0 = (2 * rb + kh) * 1024 - 2048;
  u32 tmp, tmp2;
#define ATT_INIT()                                                                                                          \
  asm volatile(A_INIT1(68) A_INIT1(80) A_INIT1(92) A_INIT1(104) A_INIT1(116) A_INIT1(128) A_INIT1(140) A_INIT1(152)         \
               A_INIT0(0) A_INIT0(8) A_INIT0(16) A_INIT0(24) A_INIT0(56)                                                    \
               : "={v[0:31]}"(acc), "={v[32:63]}"(sp), "={v[64:95]}"(rg0), "={v[96:127]}"(rg1), "={v[128:159]}"(rg2)        \
               : [one] "s"(0x3F803F803F803F80ull))
#define ATT_FILL(CMP)                                                                                                       \
  asm volatile(CMP " %[w], 4\n\ts_cbranch_scc0 1f\n\ts_mov_b32 %[T], %[T0]\n\t"                                              \
               A_RING(0) A_RING(1) A_RING(2) A_RING(3) A_RING(4) A_RING(5) A_RING(6) "1:"                                   \
               : "+{v[64:95]}"(rg0), "+{v[96:127]}"(rg1), "+{v[128:159]}"(rg2), [T] "=&s"(tmp2), [t] "=&s"(tmp)             \
               : [kv] "v"(kvoff), [vv] "v"(vvoff), [srd] "s"(sws), [vm] "s"(vmask), [hb] "s"(hbase), [w] "s"(w), [T0] "s"(T0) \
               : "memory", "scc")

  if (w < 4) {
    const u32 rt = w & 1, ch = w >> 1;
    u32 touch = 0;
    if (w == 0) {
      u64 pc;
      u32 len;
      asm volatile("s_getpc_b64 %0\n.Lfb_here:\n\ts_mov_b32 %1, .Lfb_last-.Lfb_here" : "=s"(pc), "=s"(len));
      touch = *(const GAS u32*)(pc + min(((jj & 3) * 64 + lane) * 128, len));
    } else if (w == 1) {
      touch = __builtin_amdgcn_raw_buffer_load_b32(sws, min(lane, 25u) << 20, 0, 0);
    }
    u32x4 xf[32];
    f32x16 accK = {}, accV = {}, accQ = {};
    float s0 = 0, s1 = 0, q0 = 0, q1 = 0, ck = 0, cv = 0, cq = 0;
    const u32 xl = (32 * rt + r31) * 128 + ((h ^ ((r31 >> 1) & 7)) * 16);
    const u32 xl4[4] = {opaque(xl), opaque(xl ^ 32), opaque(xl ^ 64), opaque(xl ^ 96)};
    const u32 wl = (8 * h + ((lane >> 2) & 3)) * 128 + ((ch ^ ((lane >> 3) & 1)) * 64) + ((lane >> 4) & 1) * 32 + (lane & 3) * 8;
    const u32 wlk = opaque(wl + L_RK), wlq = opaque(wl + L_WQ);
    auto csum = [&](float c, u32x4 f) { return dot2(f.w, 0x3F803F80u, dot2(f.z, 0x3F803F80u, dot2(f.y, 0x3F803F80u, dot2(f.x, 0x3F803F80u, c)))); };
#pragma unroll
    for (int blk = 0; blk < 8; blk++) {
      bar();
      if (blk == 0) STAMP(2);
      if (blk == 4) STAMP(3);
      if (blk == 7) STAMP(4);
      const u32 so = (blk & 3) * 8192;
#pragma unroll
      for (int s4 = 0; s4 < 4; s4++) xf[4 * blk + s4] = lds_r128(lds, xl4[s4] + so);
#pragma unroll
      for (int s4 = 0; s4 < 4; s4++) {
        const int s = 4 * blk + s4;
        const u32x4 wk = lds_tr2(lds, wlk + so + s4 * 2048, wlk + so + s4 * 2048 + 512);
        const u32x4 wv = lds_tr2(lds, wlk + so + 32768 + s4 * 2048, wlk + so + 32768 + s4 * 2048 + 512);
        accK = mfma32(wk, xf[s], accK);
        accV = mfma32(xf[s], wv, accV);
        s0 = dot2(xf[s].x, 0x3F803F80u, s0); q0 = dot2(xf[s].x, xf[s].x, q0);
        s1 = dot2(xf[s].y, 0x3F803F80u, s1); q1 = dot2(xf[s].y, xf[s].y, q1);
        s0 = dot2(xf[s].z, 0x3F803F80u, s0); q0 = dot2(xf[s].z, xf[s].z, q0);
        s1 = dot2(xf[s].w, 0x3F803F80u, s1); q1 = dot2(xf[s].w, xf[s].w, q1);
        ck = csum(ck, wk);
        cv = csum(cv, wv);
      }
      asm volatile("" : "+v"(s0), "+v"(s1), "+v"(q0), "+v"(q1), "+v"(ck), "+v"(cv));
    }
    const float sum = swap32_add(s0 + s1), sq = swap32_add(q0 + q1);
    const float mean = sum * (1.f / 512.f);
    const float rstd = __builtin_amdgcn_rsqf(fmaxf(sq * (1.f / 512.f) - mean * mean, 0.f) + 1e-5f);
    u32x4 mf;
    {
      const float nm = -mean, hi = __uint_as_float(__float_as_uint(nm) & 0xFFFF0000u), sel = h ? nm - hi : hi;
      mf = u32x4{pk_bf16(sel, sel), 0, 0, 0};
    }
    auto csfrag = [&](float c) {
      const float t = swap32_add(c), hi = __uint_as_float(__float_as_uint(t) & 0xFFFF0000u);
      return u32x4{pk_bf16(hi, t - hi), 0, 0, 0};
    };
    *(LAS float*)(lds + L_RST + w * 128 + r31 * 4) = rstd;
    asm volatile("" : "+v"(touch));
    tag = ep + 1;
    const u32 tile = 2 * rb + rt, hp = 2 * hq + ch;
    {
      accK = mfma32(csfrag(ck), mf, accK);
      accV = mfma32(mf, csfrag(cv), accV);
      const u32 pi = (r31 & 19) | ((r31 & 4) << 1) | ((r31 & 8) >> 1);
      const u32 ko = WS_K + (2 * hp * 64 + tile) * 1024 + h * 512 + pi * 16;
      u32x4 a, c;
      for (int i = 0; i < 4; i++) { a[i] = pk_bf16(accK[2 * i] * rstd, accK[2 * i + 1] * rstd); c[i] = pk_bf16(accK[8 + 2 * i] * rstd, accK[9 + 2 * i] * rstd); }
      st_c(sws, ko, a);
      st_c(sws, ko + 65536, c);
    }
    {
      f32x4 rs[4];
      for (int i = 0; i < 4; i++) rs[i] = *(LAS f32x4*)(lds + L_RST + w * 128 + i * 32 + h * 16);
      const u32 vo = WS_V + ((2 * hp + (r31 >> 4)) * 64 + tile) * 1024 + h * 256 + (r31 & 15) * 16;
      u32x4 a, c;
      for (int i = 0; i < 4; i++) {
        a[i] = pk_bf16(accV[2 * i] * rs[i >> 1][(2 * i) & 3], accV[2 * i + 1] * rs[i >> 1][(2 * i + 1) & 3]);
        c[i] = pk_bf16(accV[8 + 2 * i] * rs[2 + (i >> 1)][(2 * i) & 3], accV[9 + 2 * i] * rs[2 + (i >> 1)][(2 * i + 1) & 3]);
      }
      st_c(sws, vo, a);
      st_c(sws, vo + 512, c);
    }
    STAMP(5);
#pragma unroll
    for (int qc = 0; qc < 4; qc++) {
      bar();
      if (qc == 0) STAMP(6);
      if (qc == 3) {
        STAMP(7);
        wait_vm<0>();
        if (lane == 0) __hip_atomic_store((GAS u64*)(pws + WS_KVFLAG + (hp * 64 + tile) * 8), tag, RLX_AGENT);
      }
#pragma unroll
      for (int s = 8 * qc; s < 8 * qc + 8; s++) {
        const u32x4 wq = lds_tr2(lds, wlq + s * 2048, wlq + s * 2048 + 512);
        accQ = mfma32(wq, xf[s], accQ);
        cq = csum(cq, wq);
      }
      asm volatile("" : "+v"(cq));
    }
    accQ = mfma32(csfrag(cq), mf, accQ);
    {
      const float qs = rstd * (0.25f * 1.4426950408889634f);
      u32x4 a, c;
      for (int i = 0; i < 4; i++) { a[i] = pk_bf16(accQ[2 * i] * qs, accQ[2 * i + 1] * qs); c[i] = pk_bf16(accQ[8 + 2 * i] * qs, accQ[9 + 2 * i] * qs); }
      lds_w128(lds, L_QX + w * 2048 + lane * 16, a);
      lds_w128(lds, L_QX + w * 2048 + 1024 + lane * 16, c);
    }
    STAMP(8);
  } else {
    const u32 hw = w - 4;
    const srd_t sx = __builtin_amdgcn_make_buffer_rsrc((void*)px, 0, 2048u * 1024u, 0x00020000);
    const srd_t sw = __builtin_amdgcn_make_buffer_rsrc((void*)pwqkv, 0, 512u * 3072u, 0x00020000);
    const u32 xv0 = (lane >> 3) * 1024 + (((lane & 7) ^ (lane >> 4)) * 16), xv1 = (xv0 ^ 64) + 8192;
    const u32 wv0 = (lane >> 3) * 3072 + (((lane & 7) ^ ((lane >> 2) & 4)) * 16), wv1 = wv0 + 8 * 3072;
    const u32 xs = (64 * rb + 16 * hw) * 1024, ws_ = 16 * hw * 3072 + hq * 128;
    auto blk_issue = [&](u32 blk) {
      const u32 kb = (blk + rot) & 7, l = (blk & 3) * 8192 + hw * 2048, wo = ws_ + kb * (64 * 3072);
      dma16(sx, xv0, xs + kb * 128, l);
      dma16(sx, xv1, xs + kb * 128, l + 1024);
      dma16(sw, wv0, wo + 1024, l + L_RK);
      dma16(sw, wv1, wo + 1024, l + L_RK + 1024);
      dma16(sw, wv0, wo + 2048, l + L_RV);
      dma16(sw, wv1, wo + 2048, l + L_RV + 1024);
    };
    auto q_issue = [&](u32 blk) {
      const u32 kb = (blk + rot) & 7, l = L_WQ + blk * 8192 + hw * 2048, wo = ws_ + kb * (64 * 3072);
      dma16(sw, wv0, wo, l);
      dma16(sw, wv1, wo, l + 1024);
    };
    blk_issue(0);
    blk_issue(1);
    STAMP(1);
#pragma unroll 1
    for (u32 blk = 0; blk < 6; blk++) {
      blk_issue(blk + 2);
      wait_vm<12>();
      bar();
    }
    STAMP(3);
    q_issue(0); q_issue(1); q_issue(2);
    wait_vm<12>();
    bar();
    q_issue(3); q_issue(4); q_issue(5);
    wait_vm<12>();
    bar();
    STAMP(4);
    q_issue(6); q_issue(7);
    __builtin_amdgcn_sched_barrier(0);
    u32x4 tu[5];
    u32 tdst[5];
    const u32 wid = (b * 4 + hw) & 1023;
#pragma unroll
    for (int n = 0; n < 5; n++) {
      const u32 U = n * 1024 + wid, g = U & 3;
      const char* src;
      u32 rlo, rhi, pitch = 1024, fstr = 1024, col, dst;
      if (U < 1536) {
        const u32 m = U >> 9, kb = (U >> 2) & 15, cs = (U >> 6) & 7;
        rlo = m == 0 ? 32 * kb + 16 * (g >> 1) + 4 * (g & 1) : 64 * (kb >> 1) + 16 * g + 8 * (kb & 1);
        rhi = rlo + (m == 0 ? 8 : 4);
        src = m == 0 ? pwo : m == 1 ? pw1 : pw2;
        col = cs * 128;
        dst = WS_WOT + m * (512u << 10) + (cs * 16 + kb) * 4096;
      } else if (U < 1536 + 1792) {
        const u32 t = (U - 1536) >> 2, kb = t & 15, sl = t >> 4;
        rlo = 64 * (kb >> 1) + 16 * g + 8 * (kb & 1);
        rhi = rlo + 4;
        src = pw3; pitch = 3584; fstr = 16384;
        col = sl * 128;
        dst = WS_W3T + (64 * sl + kb) * 1024;
      } else {
        const u32 t = (U - 1536 - 1792) >> 2, cs = t & 7, kb = t >> 3;
        const bool sg = kb >= 48;
        const u32 Tt = sg ? 7 * (2 * (kb - 48) + (g >> 1)) + 6 : kb < 32 ? 7 * (kb >> 1) + 2 * (kb & 1) : 7 * (kb - 32) + 4;
        const u32 T1 = sg ? Tt : Tt + 1, g0 = sg ? 2 * (g & 1) : g, g1 = sg ? g0 + 1 : g;
        rlo = 64 * (Tt >> 2) + 16 * g0 + 4 * (Tt & 3);
        rhi = 64 * (T1 >> 2) + 16 * g1 + 4 * (T1 & 3);
        src = pw4;
        col = cs * 128;
        dst = WS_W4T + (cs * 56 + kb) * 4096;
      }
      tdst[n] = dst + g * 256 + (lane >> 4) * fstr + (lane & 15) * 16;
      tu[n] = *(const u32x4*)(src + (((h ? rhi : rlo) + ((lane >> 3) & 3)) * pitch + col + (lane & 7) * 16));
    }
    __builtin_amdgcn_sched_barrier(0);
    STAMP(5);
    wait_vm<17>(); bar();
    STAMP(6);
    wait_vm<13>(); bar();
    wait_vm<9>(); bar();
    wait_vm<5>(); bar();
    STAMP(7);
    tag = ep + 1;
    const u32 sc = L_TSC + hw * 5120;
#pragma unroll
    for (int n = 0; n < 5; n++) lds_w128(lds, sc + n * 1024 + lane * 16, tu[n]);
    const u32 tl = sc + ((lane >> 2) & 3) * 128 + (lane & 3) * 32 + (lane >> 4) * 8;
#pragma unroll
    for (int n = 0; n < 5; n++) st_c(sws, tdst[n], lds_tr2(lds, tl + n * 1024, tl + n * 1024 + 512));
    STAMP(8);
  }
  ATT_INIT();
  if (w >= 4) {
    const u32 tl_ = (u32)tag, th_ = (u32)(tag >> 32);
#pragma unroll 1
    for (int spin = 0; spin < SPIN_CAP; spin++) {
      asm volatile("" ::: "memory");
      const u32x4 f = ld_c(sws, WS_KVFLAG + hq * 1024 + lane * 16);
      if (__all(f.x == tl_ && f.y == th_ && f.z == tl_ && f.w == th_)) break;
      __builtin_amdgcn_s_sleep(4);
    }
  }
  STAMP(10);
  ATT_FILL("s_cmp_ge_u32");
  bar();
  STAMP(9);
  ATT_FILL("s_cmp_lt_u32");
  u32 T = T0 + 7 * 2048;

  const u32x4 qa = lds_r128(lds, L_QX + (2 * (hl >> 1)) * 2048 + (hl & 1) * 1024 + lane * 16);
  const u32x4 qb = lds_r128(lds, L_QX + (2 * (hl >> 1) + 1) * 2048 + (hl & 1) * 1024 + lane * 16);
  if (threadIdx.x == 0) *(LAS u32*)(lds + L_CNT) = 0;
  const f32x16 zero16 = {};
  const u32x4 sent = {SENT, SENT, SENT, SENT};
  const u32 svoff = lane * 16;
  const u32 flds = L_XCH + w * 2304;
  asm volatile("s_waitcnt vmcnt(18)\n\t" A_MFMA(32, 64, "qa") "s_nop 11" : "+{v[32:63]}"(sp) : "{v[64:95]}"(rg0), [qa] "v"(qa) : "memory");
  STAMP(11);
#pragma unroll 1
  for (u32 outer = 0; outer < 4; outer++) {
    asm volatile(A_STEP(0, 7, 1, 18, A_MEM0, "") A_STEP(1, 0, 2, 17, A_MEM(1, 0), "") A_STEP(2, 1, 3, 16, A_MEM(2, 1), "")
                 A_STEP(3, 2, 4, 15, A_MEM(3, 2), A_FLAGS) A_STEP(4, 3, 5, 14, A_MEM(4, 3), "") A_STEP(5, 4, 6, 13, A_MEM(5, 4), "")
                 A_STEP(6, 5, 7, 12, A_MEM(6, 5), "") A_STEP(7, 6, 0, 18, A_MEM(7, 6), "") ROUND_BAR
                 : "+{v[0:31]}"(acc), "+{v[32:63]}"(sp), "+{v[64:95]}"(rg0), "+{v[96:127]}"(rg1), "+{v[128:159]}"(rg2), [T] "+s"(T), [t] "=&s"(tmp)
                 : [qa] "v"(qa), [qb] "v"(qb), [kv] "v"(kvoff), [vv] "v"(vvoff), [sv] "v"(svoff), [sent] "v"(sent), [idx] "v"(0xDDDDu), [srd] "s"(sws),
                   [vm] "s"(vmask), [hb] "s"(hbase), [outer] "s"(outer), [sso] "s"(WS_O + (b * 62 + w * 8) * 1024), [ssm] "s"(WS_O + (b * 62 + 61) * 1024),
                   [dso] "s"(WS_WOT + ((jj & 7) * 64 + w * 8) * 1024), [dl] "s"(w * 8192), [fl] "s"(flds)
                 : "memory", "scc");
    if (outer == 1) {
      if (lane == 0) {
        const u32 c = __hip_atomic_fetch_add((LAS u32*)(lds + L_CNT), 1u, __ATOMIC_RELAXED, __HIP_MEMORY_SCOPE_WORKGROUP);
        if (c == 7) __hip_atomic_store((GAS u64*)(pws + WS_WGFLAG + b * 8), tag, RLX_AGENT);
      }
    }
    if (outer == 2) {
      wait_vm<12>();
      u32x4 fl0 = lds_r128(lds, flds + svoff), fl1 = lds_r128(lds, flds + 1024 + svoff);
      const u32 tl = (u32)tag, th = (u32)(tag >> 32);
#pragma unroll 1
      for (int spin = 0; spin < SPIN_CAP; spin++) {
        const bool ok = fl0.x == tl && fl0.y == th && fl0.z == tl && fl0.w == th && fl1.x == tl && fl1.y == th && fl1.z == tl && fl1.w == th;
        if (__all(ok)) break;
        __builtin_amdgcn_s_sleep(8);
        asm volatile("" ::: "memory");
        fl0 = ld_c(sws, WS_WGFLAG + svoff);
        fl1 = ld_c(sws, WS_WGFLAG + 1024 + svoff);
      }
      if (b == 0 && w == 0 && lane == 0) __hip_atomic_store((GAS u64*)(pws + WS_EPOCH), tag, RLX_AGENT);
      STAMP(33);
    }
  }
  asm volatile("s_nop 1\n\t" A_SMF(16, 72 + 12 * 7, 56) "s_nop 15\n\ts_nop 3"
               : "+{v[0:31]}"(acc) : "{v[32:63]}"(sp), "{v[128:159]}"(rg2), [idx] "v"(0xDDDDu));
  const f32x16 oa = __builtin_shufflevector(acc, acc, 0, 1, 2, 3, 4, 5, 6, 7, 8, 9, 10, 11, 12, 13, 14, 15);
  const f32x16 ob = __builtin_shufflevector(acc, acc, 16, 17, 18, 19, 20, 21, 22, 23, 24, 25, 26, 27, 28, 29, 30, 31);
  STAMP(12);
  {
    const f32x16 snd = kh ? oa : ob;
    f32x16 o = kh ? ob : oa;
    const u32 xo = L_XCH + w * 2304;
    *(LAS f32x4*)(lds + xo + lane * 16) = f32x4{snd[0], snd[1], snd[2], snd[3]};
    *(LAS f32x4*)(lds + xo + 1024 + lane * 16) = f32x4{snd[4], snd[5], snd[6], snd[7]};
    *(LAS float*)(lds + xo + 2048 + lane * 4) = snd[8];
    bar();
    const u32 xi = L_XCH + (w ^ 1) * 2304;
    const f32x4 r0 = *(LAS f32x4*)(lds + xi + lane * 16), r1 = *(LAS f32x4*)(lds + xi + 1024 + lane * 16);
    float l = o[8] + *(LAS float*)(lds + xi + 2048 + lane * 4);
    for (int i = 0; i < 4; i++) { o[i] += r0[i]; o[4 + i] += r1[i]; }
    float z = 0.f;
    for (int i = 0; i < 8; i++) z = fmaf(o[i], 0.f, z);
    const bool safe = l > 0x1p-40f && l < 0x1p40f && z == 0.f;
    if (!__all(safe)) {
      const u32x4 q = kh ? qb : qa;
      u32x4 k1 = {0, 0, 0, 0}, v1 = {0x3F803F80u, 0x3F803F80u, 0x3F803F80u, 0x3F803F80u}, v2 = v1;
      float m = -3.0e38f;
      o = zero16;
#pragma unroll 1
      for (u32 t = 0; t < 64; t++) {
        ring_issue(k1, v1, v2, sws, kvoff, vvoff, hbase + t * 1024, vmask);
        ring_wait(0, k1, v1, v2);
        f32x16 s = mfma32(k1, q, zero16);
        float mx = m;
        for (int i = 0; i < 16; i++) mx = fmaxf(mx, s[i]);
        mx = swap32_max(mx);
        const float al = __builtin_amdgcn_exp2f(m - mx);
        m = mx;
        for (int i = 0; i < 16; i++) { s[i] -= mx; o[i] *= al; }
        o = smf(v1, exp_lo(s), o);
        o = smf(v2, exp_hi(s), o);
      }
      l = o[8];
    }
    const float il = __builtin_amdgcn_rcpf(l);
    u32x4 ov;
    for (int i = 0; i < 4; i++) ov[i] = pk_bf16(o[2 * i] * il, o[2 * i + 1] * il);
    const u32 rb16 = 4 * rb + 2 * kh + (r31 >> 4);
    st_c(sws, WS_O + (rb16 * 16 + (hd >> 1)) * 1024 + (2 * (hl & 1) + h) * 256 + (lane & 15) * 16, clampS(ov));
  }

  STAMP(13);
  const u32 rb2 = 2 * xcd + (jj >> 4), u = jj & 15, prb = 2 * rb2 + (u >> 3), cs = u & 7;
  const u32 st = w & 3, e = w >> 2, g = lane >> 4, i15 = lane & 15;
  const u32 s16 = 4 * prb + st;
  constexpr u32 L_A = 0, L_B = 65536, L_X = 131072, L_C = 147456;
  const u32 lx = L_X + w * 2048 + svoff;
  const u32x4 ones = {0x3F803F80u, 0x3F803F80u, 0x3F803F80u, 0x3F803F80u};
  const f32x4 zero4 = {};
  u32x4 xr;
  {
    const srd_t sx = __builtin_amdgcn_make_buffer_rsrc((void*)px, 0, 2048u * 1024u, 0x00020000);
    asm volatile("buffer_load_dwordx4 %0, %1, %2, %3 offen" : "=v"(xr) : "v"(i15 * 1024 + g * 32), "s"(sx), "s"(s16 * 16384 + cs * 128 + e * 16));
  }
  u32x4 wr[32];
  const u32 wro = opaque(e * 32768 + svoff), wro2 = opaque(e * 32768 + (svoff ^ 0));
  auto load_w = [&](u32 base) {
    const u32 b0 = base + wro + e * 2048, b1 = base + wro2 + (1 - e) * 2048;
#pragma unroll
    for (int k = 0; k < 8; k++) {
      wr[4 * k] = lds_r128(lds, b0 + k * 4096); wr[4 * k + 1] = lds_r128(lds, b0 + k * 4096 + 1024);
      wr[4 * k + 2] = lds_r128(lds, b1 + k * 4096); wr[4 * k + 3] = lds_r128(lds, b1 + k * 4096 + 1024);
    }
  };
  auto dma_n = [&](int n, u32 src, u32 dst) {
    for (int q = 0; q < n; q++) dma16c(sws, svoff, src + (w * n + q) * 1024, dst + (w * n + q) * 1024);
  };
  const u32 w4src = WS_W4T + cs * 56 * 4096;
  wait_vm<2>();
  bar();
  STAMP(40);
  {
    f32x4 c = zero4;
#pragma unroll
    for (int k = 0; k < 8; k++) c = mfma16(ones, lds_r128(lds, L_B + ((8 * e + k) * 4 + st) * 1024 + svoff), c);
    *(LAS float*)(lds + L_X + (g + 4 * (st >> 1)) * 2048 + i15 * 16 + (2 * (st & 1) + e) * 4) = c[0];
  }
  load_w(L_A);
  bar();
  u32 cs1a, cs1b;
  {
    const f32x4 c = *(LAS f32x4*)(lds + L_X + w * 2048 + i15 * 16);
    cs1a = split_hl(c[0] + c[1]);
    cs1b = split_hl(c[2] + c[3]);
  }
  STAMP(41);
  f32x4 ac[4];
  u32x4 af[18];
  auto gemm8 = [&](auto ns_, auto ny_, u32 abase, u32 sbase, auto&& behind) {
    constexpr int NS = ns_, NY = ny_;
    const u32 so = abase + (s16 * 16 + 8 * e) * 1024, sv = g * 512 + i15 * 16, sso = rfl(sbase + s16 * 2048);
    if (NS) { ald_c<0>(af[8], sws, sv, sso); ald_c<256>(af[9], sws, sv, sso); }
    Unr<0, 8>::go([&](auto k) { ald_c<(k & 3) * 1024>(af[k], sws, svoff, so + (k >> 2) * 4096); });
    behind();
    u32 redo = 0;
#pragma unroll 1
    do {
      u32 mx = 0;
      if (NS) { vm_wait<8 + NY>(af[8], af[9]); mx = smax(smax(mx, af[8]), af[9]); }
      for (int f = 0; f < 4; f++) ac[f] = zero4;
      Unr<0, 8>::go([&](auto k) {
        vm_wait<7 - k + NY>(af[k]);
        for (int f = 0; f < 4; f++) ac[f] = mfma16(wr[4 * k + f], af[k], ac[f]);
        mx = smax(mx, af[k]);
      });
      redo = rfl(!redo && __any(mx == SENT));
      if (redo) {
        u32 vo[10], sf[10];
        for (int k = 0; k < 8; k++) { vo[k] = svoff; sf[k] = so + k * 1024; }
        vo[8] = vo[9] = sv; sf[8] = sso; sf[9] = sso + 256;
        refetch<8 + NS>(af, sws, vo, sf);
      }
      opaque_s(redo);
    } while (redo);
  };
  auto row_stats = [&](u32x4 a, u32x4 c, float& mean, float& var) {
    const f32x4 p = __builtin_bit_cast(f32x4, a) + __builtin_bit_cast(f32x4, c);
    const float s = swap32_add(swap16_add(p[0] + p[2])), q = swap32_add(swap16_add(p[1] + p[3]));
    mean = s * (1.f / 512.f);
    var = fmaxf(q * (1.f / 512.f) - mean * mean, 0.f);
  };
  auto mean_frag = [&](float mean) {
    const float nm = -mean, hi = __uint_as_float(__float_as_uint(nm) & 0xFFFF0000u), sel = g == 0 ? hi : g == 1 ? nm - hi : 0.f;
    return u32x4{pk_bf16(sel, sel), 0, 0, 0};
  };
  auto exchange = [&](float* v) {
    *(LAS f32x4*)(lds + lx) = ac[2];
    *(LAS f32x4*)(lds + lx + 1024) = ac[3];
    bar();
    const f32x4 r0 = *(LAS f32x4*)(lds + (lx ^ 8192)), r1 = *(LAS f32x4*)(lds + (lx ^ 8192) + 1024);
    for (int i = 0; i < 4; i++) { v[i] = ac[0][i] + r0[i]; v[4 + i] = ac[1][i] + r1[i]; }
  };
  auto pack8 = [&](const float* v) { return clampS(u32x4{pk_bf16(v[0], v[1]), pk_bf16(v[2], v[3]), pk_bf16(v[4], v[5]), pk_bf16(v[6], v[7])}); };
  auto put_stats = [&](const float* v, u32 sbase) {
    float s = 0, q = 0;
    for (int i = 0; i < 8; i++) { s += v[i]; q = fmaf(v[i], v[i], q); }
    s = swap32_add(swap16_add(s));
    q = swap32_add(swap16_add(q));
    if (g == 0)
      __builtin_amdgcn_raw_buffer_store_b64(u32x2{clamp1(__float_as_uint(s)), clamp1(__float_as_uint(q))}, sws,
                                            sbase + s16 * 2048 + cs * 256 + i15 * 16 + e * 8, 0, 16);
  };
  using I0 = std::integral_constant<int, 0>; using I2 = std::integral_constant<int, 2>;
  using I8 = std::integral_constant<int, 8>; using I10 = std::integral_constant<int, 10>;
  const u32 fo = (s16 * 16 + 2 * cs + e) * 1024 + svoff;
  float hv[8], yv[8];
  __builtin_amdgcn_s_sleep(D1);
  gemm8(I0{}, I10{}, WS_O, 0, [&] { dma_n(8, WS_W2T + cs * 65536, L_A); dma_n(2, w4src, L_C); });
  STAMP(42);
  exchange(hv);
  vm_wait<10>(xr);
  for (int i = 0; i < 4; i++) { hv[2 * i] += bf_lo(xr[i]); hv[2 * i + 1] += bf_hi(xr[i]); }
  st_c(sws, WS_H + fo, pack8(hv));
  put_stats(hv, WS_HST);
  STAMP(43);
  load_w(L_B);
  bar();
  STAMP(44);
  __builtin_amdgcn_s_sleep(D2);
  gemm8(I2{}, I8{}, WS_H, WS_HST, [&] { dma_n(8, WS_W3T + u * 7 * 16384, L_B); });
  STAMP(45);
  {
    float mean, var, t[8];
    row_stats(af[8], af[9], mean, var);
    const u32x4 mf = mean_frag(mean);
    ac[0] = mfma16(u32x4{cs1a, 0, 0, 0}, mf, ac[0]);
    ac[1] = mfma16(u32x4{cs1b, 0, 0, 0}, mf, ac[1]);
    const float r1 = __builtin_amdgcn_rsqf(var + 1e-5f);
    const float r12 = r1 * __builtin_amdgcn_rsqf(var * r1 * r1 + 1e-5f);
    exchange(t);
    for (int i = 0; i < 8; i++) { t[i] = gelu(t[i] * r12); yv[i] = (hv[i] - mean) * r1; }
    const int sh = (cs & 2 ? -1 : 1) * (cs & 4 ? 2 : 1);
    const u32 rd = s16 * 16 + i15 + sh, rw = rd & 2047;
    u32x4 o = pack8(t);
    if (rd != rw) o = u32x4{0, 0, 0, 0};
    st_c(sws, WS_HH + (rw >> 4) * 16384 + (2 * cs + e) * 1024 + (g * 16 + (rw & 15)) * 16, o);
  }
  STAMP(46);
  wait_vm<9>();
  bar();
  load_w(L_A);
  bar();
  STAMP(47);
  __builtin_amdgcn_s_sleep(D3);
  gemm8(I0{}, I8{}, WS_HH, 0, [&] { dma_n(6, WS_W3T + (u * 7 + 4) * 16384, L_A); dma_n(2, w4src + 16384, L_A + 49152); });
  STAMP(48);
  {
    float t[8];
    exchange(t);
    for (int i = 0; i < 8; i++) t[i] += yv[i];
    st_c(sws, WS_X1 + fo, pack8(t));
    put_stats(t, WS_XST);
    for (int i = 0; i < 8; i++) hv[i] += t[i];
  }
  STAMP(49);
  wait_vm<2>();
  bar();
  if (w < 7) {
    f32x4 c = zero4;
    const u32 tb = (w < 4 ? L_B + w * 16384 : L_A + (w - 4) * 16384) + svoff;
#pragma unroll
    for (int k = 0; k < 16; k++) c = mfma16(ones, lds_r128(lds, tb + k * 1024), c);
    const u32 cw = split_hl(c[0]);
    *(LAS u32*)(lds + L_X + g * 2048 + i15 * 32 + w * 4) = cw;
    *(LAS u32*)(lds + L_X + (g + 4) * 2048 + i15 * 32 + w * 4) = cw;
  }
  bar();
  const u32x4 c3a = lds_r128(lds, L_X + w * 2048 + i15 * 32), c3b = lds_r128(lds, L_X + w * 2048 + i15 * 32 + 16);
  wait_lgkm0();
  STAMP(50);
  u32x4 w4p[14];
  {
    const u32 t16 = 8 * rb2 + w, so = WS_X1 + t16 * 16384, sv = g * 512 + i15 * 16, sso = WS_XST + t16 * 2048;
    __builtin_amdgcn_s_sleep(D4);
    ald_c<0>(af[16], sws, sv, sso);
    ald_c<256>(af[17], sws, sv, sso);
    Unr<0, 16>::go([&](auto k) { ald_c<(k & 3) * 1024>(af[k], sws, svoff, so + (k >> 2) * 4096); });
    dma_n(2, w4src + 2 * 16384, L_X);
    Unr<0, 14>::go([&](auto k) { ald_c<(k & 1) * 1024>(w4p[k], sws, svoff, w4src + (7 + (k >> 1)) * 16384 + w * 2048); });
    f32x4 ga[7];
    const u32 lb = opaque(L_B + svoff), la = opaque(L_A + svoff);
    u32 redo = 0;
#pragma unroll 1
    do {
      vm_wait<32>(af[16], af[17]);
      u32 mx = smax(smax(0, af[16]), af[17]);
      for (int f = 0; f < 4; f++) ga[f] = zero4;
      Unr<0, 16>::go([&](auto k) {
        vm_wait<31 - k>(af[k]);
        for (int f = 0; f < 4; f++) ga[f] = mfma16(lds_r128(lds, lb + f * 16384 + k * 1024), af[k], ga[f]);
        mx = smax(mx, af[k]);
      });
      redo = rfl(!redo && __any(mx == SENT));
      if (redo) {
        u32 vo[18], sf[18];
        for (int k = 0; k < 16; k++) { vo[k] = svoff; sf[k] = so + k * 1024; }
        vo[16] = vo[17] = sv; sf[16] = sso; sf[17] = sso + 256;
        refetch<18>(af, sws, vo, sf);
      }
      opaque_s(redo);
    } while (redo);
    STAMP(51);
    bar();
    dma_n(8, w4src + 3 * 16384, L_B);
    float mean, var;
    row_stats(af[16], af[17], mean, var);
    const u32x4 mf = mean_frag(mean);
    const float r = __builtin_amdgcn_rsqf(var + 1e-5f);
    const u32 go = WS_G + t16 * 57344 + svoff;
    auto fin = [&](int t) {
      const u32 c = t < 4 ? c3a[t] : c3b[t - 4];
      const f32x4 v = mfma16(u32x4{c, 0, 0, 0}, mf, ga[t]);
      return u32x2{clamp1(pk_bf16(gelu(v[0] * r), gelu(v[1] * r))), clamp1(pk_bf16(gelu(v[2] * r), gelu(v[3] * r)))};
    };
    auto tile = [&](int t) {
      ga[t] = zero4;
#pragma unroll
      for (int k = 0; k < 16; k++) ga[t] = mfma16(lds_r128(lds, la + (t - 4) * 16384 + k * 1024), af[k], ga[t]);
    };
    auto put2 = [&](int t, u32 kb) { const u32x2 p = fin(t), q = fin(t + 1); st_c(sws, go + kb * 1024, u32x4{p.x, p.y, q.x, q.y}); };
    tile(4);
    put2(0, 2 * u);
    tile(5);
    put2(2, 2 * u + 1);
    tile(6);
    put2(4, 32 + u);
    __builtin_amdgcn_raw_buffer_store_b64(fin(6), sws, WS_G + t16 * 57344 + (48 + (u >> 1)) * 1024 + ((2 * (u & 1) + (g >> 1)) * 16 + i15) * 16 + (g & 1) * 8, 0, 16);
  }
  STAMP(52);
  {
    Unr<0, 14>::go([&](auto k) { vm_wait<4>(w4p[k]); });
    bar();
    const u32 kq = 2 * e + (st & 1), rh = st >> 1;
    for (int k = 0; k < 6; k++) lds_w128(lds, L_A + (k >> 1) * 16384 + w * 2048 + (k & 1) * 1024 + svoff, w4p[k]);
    u32x4 gf[28];
    const u32 so0 = WS_G + (4 * prb + 2 * rh + (kq & 1)) * 57344 + kq * 1024, so1 = WS_G + (4 * prb + 2 * rh + 1 - (kq & 1)) * 57344 + kq * 1024;
    Unr<0, 8>::go([&](auto n) { ald_c<0>(gf[2 * n], sws, svoff, so0 + n * 4096); ald_c<0>(gf[2 * n + 1], sws, svoff, so1 + n * 4096); });
    f32x4 oc[2][4];
    const u32 wl0 = opaque(kq * 4096 + e * 2048 + svoff), wl1 = opaque(kq * 4096 + (1 - e) * 2048 + svoff);
    auto step = [&](int n, u32 slot) {
      const u32x4 w0 = lds_r128(lds, wl0 + slot), w1 = lds_r128(lds, wl0 + slot + 1024), w2 = lds_r128(lds, wl1 + slot), w3 = lds_r128(lds, wl1 + slot + 1024);
      for (int s = 0; s < 2; s++) {
        oc[s][0] = mfma16(w0, gf[2 * n + s], oc[s][0]); oc[s][1] = mfma16(w1, gf[2 * n + s], oc[s][1]);
        oc[s][2] = mfma16(w2, gf[2 * n + s], oc[s][2]); oc[s][3] = mfma16(w3, gf[2 * n + s], oc[s][3]);
      }
    };
    auto check = [&](auto n0_, auto n1_) {
      constexpr int N0 = n0_, N1 = n1_;
      u32 mx = 0;
      for (int k = 2 * N0; k < 2 * N1; k++) mx = smax(mx, gf[k]);
      if (__any(mx == SENT)) {
        u32 vo[2 * (N1 - N0)], sf[2 * (N1 - N0)];
        for (int k = 2 * N0; k < 2 * N1; k++) { vo[k - 2 * N0] = svoff; sf[k - 2 * N0] = (k & 1 ? so1 : so0) + (k >> 1) * 4096; }
        refetch<2 * (N1 - N0)>(gf + 2 * N0, sws, vo, sf);
      }
    };
    for (int s = 0; s < 2; s++) for (int f = 0; f < 4; f++) oc[s][f] = zero4;
    using std::integral_constant;
    Unr<0, 4>::go([&](auto n) {
      vm_wait<14 - 2 * n>(gf[2 * n], gf[2 * n + 1]);
      check(n, integral_constant<int, n + 1>{});
      step(n, n == 0 ? L_C : n == 1 ? L_A + 49152 : n == 2 ? L_X : L_B);
    });
    STAMP(53);
    __builtin_amdgcn_s_sleep(D5);
    Unr<8, 14>::go([&](auto n) { ald_c<0>(gf[2 * n], sws, svoff, so0 + n * 4096); ald_c<0>(gf[2 * n + 1], sws, svoff, so1 + n * 4096); });
    bar();
    for (int k = 6; k < 14; k++) {
      const int n = (k >> 1) - 3;
      lds_w128(lds, (n == 0 ? L_C : n == 1 ? L_A + 49152 : n == 2 ? L_X : L_B) + w * 2048 + (k & 1) * 1024 + svoff, w4p[k]);
    }
    Unr<4, 7>::go([&](auto n) {
      vm_wait<26 - 2 * n>(gf[2 * n], gf[2 * n + 1]);
      check(n, integral_constant<int, n + 1>{});
      step(n, L_B + (n - 3) * 16384);
    });
    bar();
    STAMP(54);
    Unr<7, 14>::go([&](auto n) {
      vm_wait<26 - 2 * n>(gf[2 * n], gf[2 * n + 1]);
      check(n, integral_constant<int, n + 1>{});
      step(n, n < 10 ? L_A + (n - 7) * 16384 : n == 10 ? L_C : n == 11 ? L_A + 49152 : n == 12 ? L_X : L_B);
    });
    STAMP(55);
    const u32 ro = L_B + 16384 + w * 6144 + svoff;
    *(LAS f32x4*)(lds + ro) = oc[1][0]; *(LAS f32x4*)(lds + ro + 1024) = oc[1][1];
    *(LAS f32x4*)(lds + ro + 2048) = oc[0][2]; *(LAS f32x4*)(lds + ro + 3072) = oc[0][3];
    *(LAS f32x4*)(lds + ro + 4096) = oc[1][2]; *(LAS f32x4*)(lds + ro + 5120) = oc[1][3];
    bar();
    for (int d = 1; d < 4; d++) {
      const u32 ri = L_B + 16384 + (w ^ ((d & 1) | ((d & 2) << 1))) * 6144 + (d - 1) * 2048 + svoff;
      oc[0][0] += *(LAS f32x4*)(lds + ri);
      oc[0][1] += *(LAS f32x4*)(lds + ri + 1024);
    }
    u32x4 o;
    for (int i = 0; i < 2; i++) {
      o[i] = pk_bf16(hv[2 * i] + oc[0][0][2 * i], hv[2 * i + 1] + oc[0][0][2 * i + 1]);
      o[2 + i] = pk_bf16(hv[4 + 2 * i] + oc[0][1][2 * i], hv[5 + 2 * i] + oc[0][1][2 * i + 1]);
    }
    asm volatile(".Lfb_last:");
    *(u32x4*)(pout + (s16 * 16 + i15) * 1024 + cs * 128 + g * 32 + e * 16) = o;
  }
  STAMP(56);
  wait_vm<0>();
  STAMP(14);
  STAMPR(63);
}

extern "C" void kernel_launch(void* const* d_in, const int* in_sizes, int n_in, void* d_out, int out_size, void* d_ws, size_t ws_size,
                              hipStream_t stream) {
  fused_block<<<256, 512, 0, stream>>>((const char*)d_in[0], (const char*)d_in[1], (const char*)d_in[2], (const char*)d_in[4], (const char*)d_in[5],
                                       (const char*)d_in[6], (const char*)d_in[7], (char*)d_out, (char*)d_ws);
}
```

```cpp
#include <hip/hip_runtime.h>
#include <type_traits>

typedef unsigned u32;
typedef unsigned long long u64;
typedef __attribute__((ext_vector_type(4))) u32 u32x4;
typedef __attribute__((ext_vector_type(2))) u32 u32x2;
typedef __attribute__((ext_vector_type(4))) short s16x4;
typedef __attribute__((ext_vector_type(2))) __bf16 bf16x2;
typedef __attribute__((ext_vector_type(8))) __bf16 bf16x8;
typedef __attribute__((ext_vector_type(16))) __bf16 bf16x16;
typedef __attribute__((ext_vector_type(2))) float f32x2;
typedef __attribute__((ext_vector_type(4))) float f32x4;
typedef __attribute__((ext_vector_type(8))) float f32x8;
typedef __attribute__((ext_vector_type(16))) float f32x16;
typedef __attribute__((ext_vector_type(32))) float f32x32;
typedef __amdgpu_buffer_rsrc_t srd_t;
#define LAS __attribute__((address_space(3)))
#define GAS __attribute__((address_space(1)))
#define RLX_AGENT __ATOMIC_RELAXED, __HIP_MEMORY_SCOPE_AGENT
#define DI __device__ __forceinline__

constexpr u32 WS_EPOCH = 0;
constexpr u32 WS_KVFLAG = 4096;
constexpr u32 WS_WGFLAG = 16384;
constexpr u32 WS_STAMP = 32768;
constexpr u32 WS_K = 1u << 20;
constexpr u32 WS_V = 3u << 20;
constexpr u32 WS_WOT = 5u << 20;
constexpr u32 WS_W1T = WS_WOT + (512u << 10);
constexpr u32 WS_W2T = WS_WOT + (1024u << 10);
constexpr u32 WS_W3T = WS_WOT + (1536u << 10);
constexpr u32 WS_W4T = WS_W3T + (1792u << 10);
constexpr u32 WS_O = 10u << 20;
constexpr u32 WS_H = 12u << 20;
constexpr u32 WS_HH = 14u << 20;
constexpr u32 WS_X1 = 16u << 20;
constexpr u32 WS_G = 18u << 20;
constexpr u32 WS_HST = 25u << 20;
constexpr u32 WS_XST = WS_HST + (128u << 10);
constexpr u32 WS_END = WS_XST + (128u << 10);
constexpr u32 SENT = 0xFFFFFFFFu;
constexpr int SPIN_CAP = 200000;

constexpr u32 L_RX = 0, L_RK = 32768, L_RV = 65536;
constexpr u32 L_WQ = 98304;
constexpr u32 L_RST = 0;
constexpr u32 L_TSC = L_RK;
constexpr u32 L_QX = L_RV;
constexpr u32 L_CNT = 131072;
constexpr u32 L_XCH = 131072 + 1024;

DI u32 opaque(u32 v) { asm volatile("" : "+v"(v)); return v; }
DI u32 rfl(u32 v) { return __builtin_amdgcn_readfirstlane(v); }
DI bf16x8 as_bf8(u32x4 v) { return __builtin_bit_cast(bf16x8, v); }
DI u32 pk_bf16(float a, float b) { f32x2 p = {a, b}; return __builtin_bit_cast(u32, __builtin_convertvector(p, bf16x2)); }
DI float dot2(u32 a, u32 b, float c) { return __builtin_amdgcn_fdot2_f32_bf16(__builtin_bit_cast(bf16x2, a), __builtin_bit_cast(bf16x2, b), c, false); }
DI float swap32_add(float v) { auto r = __builtin_amdgcn_permlane32_swap(__float_as_uint(v), __float_as_uint(v), false, false); return __uint_as_float(r[0]) + __uint_as_float(r[1]); }
DI float swap16_add(float v) { auto r = __builtin_amdgcn_permlane16_swap(__float_as_uint(v), __float_as_uint(v), false, false); return __uint_as_float(r[0]) + __uint_as_float(r[1]); }
DI float swap32_max(float v) { auto r = __builtin_amdgcn_permlane32_swap(__float_as_uint(v), __float_as_uint(v), false, false); return fmaxf(__uint_as_float(r[0]), __uint_as_float(r[1])); }
DI f32x16 mfma32(u32x4 a, u32x4 b, f32x16 c) { return __builtin_amdgcn_mfma_f32_32x32x16_bf16(as_bf8(a), as_bf8(b), c, 0, 0, 0); }
DI f32x4 mfma16(u32x4 a, u32x4 b, f32x4 c) { return __builtin_amdgcn_mfma_f32_16x16x32_bf16(as_bf8(a), as_bf8(b), c, 0, 0, 0); }
DI f32x16 smf(u32x4 a, f32x8 p, f32x16 c) { return __builtin_amdgcn_smfmac_f32_32x32x32_bf16(as_bf8(a), __builtin_bit_cast(bf16x16, p), c, 0xDDDD, 0, 0); }
DI u32x4 lds_r128(LAS char* l, u32 off) { return *(LAS u32x4*)(l + off); }
DI void lds_w128(LAS char* l, u32 off, u32x4 v) { *(LAS u32x4*)(l + off) = v; }
DI u32x2 lds_tr(LAS char* l, u32 off) { return __builtin_bit_cast(u32x2, __builtin_amdgcn_ds_read_tr16_b64_v4i16((LAS s16x4*)(l + off))); }
DI u32x4 lds_tr2(LAS char* l, u32 off0, u32 off1) { u32x2 a = lds_tr(l, off0), b = lds_tr(l, off1); return u32x4{a.x, a.y, b.x, b.y}; }
DI void dma16(srd_t srd, u32 voff, u32 soff, u32 ldsaddr) {
  asm volatile("s_mov_b32 m0, %0\n\ts_nop 0\n\tbuffer_load_dwordx4 %1, %2, %3 offen lds" ::"s"(ldsaddr), "v"(voff), "s"(srd), "s"(soff) : "memory");
}
DI void dma16c(srd_t srd, u32 voff, u32 soff, u32 ldsaddr) {
  asm volatile("s_mov_b32 m0, %0\n\ts_nop 0\n\tbuffer_load_dwordx4 %1, %2, %3 offen sc1 lds" ::"s"(ldsaddr), "v"(voff), "s"(srd), "s"(soff) : "memory");
}
template <int N> DI void wait_vm() { asm volatile("s_waitcnt vmcnt(%0)" ::"n"(N) : "memory"); }
DI void wait_lgkm0() { asm volatile("s_waitcnt lgkmcnt(0)" ::: "memory"); }
DI void bar() { asm volatile("s_waitcnt lgkmcnt(0)\n\ts_barrier" ::: "memory"); }
DI u32x4 ld_c(srd_t s, u32 voff) { return __builtin_amdgcn_raw_buffer_load_b128(s, voff, 0, 16); }
DI void st_c(srd_t s, u32 voff, u32x4 v) { __builtin_amdgcn_raw_buffer_store_b128(v, s, voff, 0, 16); }
DI u32x4 clampS(u32x4 v) { return u32x4{min(v.x, SENT - 1), min(v.y, SENT - 1), min(v.z, SENT - 1), min(v.w, SENT - 1)}; }

#define STAMP(i) do {} while (0)
#define CNT(i, v) do {} while (0)
#define CNTS(i) do {} while (0)
#define STAMPR(i) do {} while (0)

DI void ring_issue(u32x4& kf, u32x4& va, u32x4& vb, srd_t srd, u32 kvoff, u32 vvoff, u32 soff, u64 vmask) {
  asm volatile(
      "buffer_load_dwordx4 %0, %3, %5, %6 offen sc1\n\t"
      "s_mov_b64 exec, %7\n\t"
      "buffer_load_dwordx4 %1, %4, %5, %6 offen sc1\n\t"
      "buffer_load_dwordx4 %2, %4, %5, %6 offen offset:512 sc1\n\t"
      "s_mov_b64 exec, -1"
      : "+v"(kf), "+v"(va), "+v"(vb)
      : "v"(kvoff), "v"(vvoff), "s"(srd), "s"(soff), "s"(vmask)
      : "memory");
}
#define RING_SC " sc1"
#define RING_PRE ""
#define A_EXP(r) "v_exp_f32 v[" #r "], v[" #r "]\n\t"
#define A_EXP2(r) A_EXP(r) A_EXP(r + 1)
#define A_EXP3(r) A_EXP2(r) A_EXP(r + 2)
#define A_EXP5(r) A_EXP3(r) A_EXP2(r + 3)
#define A_EXP6(r) A_EXP3(r) A_EXP3(r + 3)
#define A_R4(r) "v[" #r ":" #r "+3]"
#define A_SMF(o, a, p) "v_smfmac_f32_32x32x32_bf16 v[" #o ":" #o "+15], " A_R4(a) ", v[" #p ":" #p "+7], %[idx]\n\t"
#define A_MFMA(d, k, q) "v_mfma_f32_32x32x16_bf16 v[" #d ":" #d "+15], " A_R4(k) ", %[" q "], 0\n\t"
#define A_RING(ip)                                                                   \
  "s_add_u32 %[T], %[T], 2048\n\t"                                                    \
  "s_and_b32 %[t], %[T], 0xfc00\n\t"                                                      \
  "s_or_b32 %[t], %[t], %[hb]\n\t"                                                        \
  RING_PRE "buffer_load_dwordx4 " A_R4(64 + 12 * ip) ", %[kv], %[srd], %[t] offen" RING_SC "\n\t"         \
  EXECW "s_mov_b64 exec, %[vm]\n\t"                                                             \
  RING_PRE "buffer_load_dwordx4 " A_R4(68 + 12 * ip) ", %[vv], %[srd], %[t] offen" RING_SC "\n\t"         \
  RING_PRE "buffer_load_dwordx4 " A_R4(72 + 12 * ip) ", %[vv], %[srd], %[t] offen offset:512" RING_SC "\n\t" \
  EXECW "s_mov_b64 exec, -1\n\t"
#define A_SENT(i)                                                                         \
  "s_cmp_eq_u32 %[outer], 0\n\t"                                                          \
  "s_cbranch_scc0 2f\n\t"                                                                 \
  "s_add_u32 %[t], %[sso], 1024*" #i "\n\t"                                               \
  "s_min_u32 %[t], %[t], %[ssm]\n\t"                                                      \
  "buffer_store_dwordx4 %[sent], %[sv], %[srd], %[t] offen sc1\n\t"                       \
  "s_branch 2f\n"
#define A_DMA(i)                                                                          \
  "1:\n\t"                                                                                \
  "s_add_u32 m0, %[dl], 1024*" #i "\n\t"                                                  \
  "s_add_u32 %[t], %[dso], 1024*" #i "\n\t"                                               \
  "buffer_load_dwordx4 %[sv], %[srd], %[t] offen sc1 lds\n\t"                             \
  "s_add_u32 m0, %[dl], 0x10000+1024*" #i "\n\t"                                          \
  "s_add_u32 %[t], %[dso], 0x80000+1024*" #i "\n\t"                                       \
  "buffer_load_dwordx4 %[sv], %[srd], %[t] offen sc1 lds\n"                               \
  "2:\n\t"
#define A_MEM0 A_RING(7) "s_cmp_eq_u32 %[outer], 3\n\ts_cbranch_scc1 1f\n\t" A_SENT(0) A_DMA(0)
#define A_MEM(i, ip) "s_cmp_eq_u32 %[outer], 3\n\ts_cbranch_scc1 1f\n\t" A_RING(ip) A_SENT(i) A_DMA(i)
#define EXECW ""
#define A_FLAGS                                                                           \
  "s_cmp_eq_u32 %[outer], 2\n\ts_cbranch_scc0 3f\n\t"                                     \
  "s_mov_b32 m0, %[fl]\n\ts_movk_i32 %[t], 0x4000\n\t"                                    \
  "buffer_load_dwordx4 %[sv], %[srd], %[t] offen sc1 lds\n\t"                             \
  "buffer_load_dwordx4 %[sv], %[srd], %[t] offen offset:1024 sc1 lds\n"                   \
  "3:\n\t"
#define A_STEP(i, ip, in, N, MEM, X)                                                      \
  PRIO_PRE "s_setprio 3-(" #i "/2)\n\t" A_EXP2(32) A_SMF(16, 72 + 12 * ip, 56) A_EXP3(34) A_MFMA(48, 64 + 12 * i, "qb") MEM     \
  A_EXP5(37) A_SMF(0, 68 + 12 * i, 32) A_EXP6(42)                                         \
  A_EXP2(48) A_SMF(0, 72 + 12 * i, 40) A_EXP3(50) "s_waitcnt vmcnt(" #N ")\n\t"           \
  A_MFMA(32, 64 + 12 * in, "qa") X A_EXP5(53) A_SMF(16, 68 + 12 * i, 48) A_EXP6(58)
#define PRIO_PRE ""
#define ROUND_BAR "s_barrier"
#define A_MV(r, x) "v_mov_b64 v[" #r ":" #r "+1], " x "\n\t"
#define A_INIT1(r) A_MV(r, "%[one]") A_MV(r + 2, "%[one]") A_MV(r + 4, "%[one]") A_MV(r + 6, "%[one]")
#define A_INIT0(r) A_MV(r, "0") A_MV(r + 2, "0") A_MV(r + 4, "0") A_MV(r + 6, "0")
static_assert(WS_WGFLAG == 0x4000 && WS_W1T - WS_WOT == 0x80000, "asm constants");
DI void ring_wait(int n, u32x4& kf, u32x4& va, u32x4& vb) {
  asm volatile("s_waitcnt vmcnt(%3)" : "+v"(kf), "+v"(va), "+v"(vb) : "n"(n) : "memory");
}
DI f32x8 exp_lo(f32x16 s) { f32x8 p; for (int i = 0; i < 8; i++) p[i] = __builtin_amdgcn_exp2f(s[i]); return p; }
DI f32x8 exp_hi(f32x16 s) { f32x8 p; for (int i = 0; i < 8; i++) p[i] = __builtin_amdgcn_exp2f(s[8 + i]); return p; }

template <int IMM> DI void ald_c(u32x4& d, srd_t s, u32 voff, u32 soff) {
  asm volatile("buffer_load_dwordx4 %0, %1, %2, %3 offen offset:%4 sc1" : "=v"(d) : "v"(voff), "s"(s), "s"(soff), "n"(IMM));
}
DI void ald_if(u64 m, u32x4& d, srd_t s, u32 voff, u32 soff) {
  asm volatile("s_cmp_eq_u64 %4, 0\n\ts_cbranch_scc1 1f\n\tbuffer_load_dwordx4 %0, %1, %2, %3 offen sc1 ; RF\n1:" : "+v"(d) : "v"(voff), "s"(s), "s"(soff), "s"(m) : "scc");
}
template <int N> DI void vm_wait(u32x4& a) { asm volatile("s_waitcnt vmcnt(%1)" : "+v"(a) : "n"(N)); }
template <int N> DI void vm_wait(u32x4& a, u32x4& b) { asm volatile("s_waitcnt vmcnt(%2)" : "+v"(a), "+v"(b) : "n"(N)); }
DI void rf_wait(u32x4& a) { asm volatile("s_waitcnt vmcnt(0) ; RF" : "+v"(a)); }
DI void opaque_s(u32& v) { asm volatile("" : "+s"(v)); }
template <class A> DI void pin(A& a) { asm volatile("" : "+v"(a)); }
template <class A, class B> DI void pin(A& a, B& b) { asm volatile("" : "+v"(a), "+v"(b)); }
template <class A, class B, class C> DI void pin(A& a, B& b, C& c) { asm volatile("" : "+v"(a), "+v"(b), "+v"(c)); }
template <class A, class B, class C, class D> DI void pin(A& a, B& b, C& c, D& d) { asm volatile("" : "+v"(a), "+v"(b), "+v"(c), "+v"(d)); }
template <int IMM> DI void ald_p(u32x4& d, srd_t s, u32 voff, u32 soff) {
  asm volatile("buffer_load_dwordx4 %0, %1, %2, %3 offen offset:%4" : "=v"(d) : "v"(voff), "s"(s), "s"(soff), "n"(IMM));
}
template <class A> DI void undef(A& a) { asm volatile("" : "=v"(a)); }
DI void ald_if0(u32 m, u32x4& d, srd_t s, u32 voff, u32 soff) {
  asm volatile("s_cmp_lg_u32 %4, 0\n\ts_cbranch_scc1 1f\n\tbuffer_load_dwordx4 %0, %1, %2, %3 offen sc1\n1:" : "+v"(d) : "v"(voff), "s"(s), "s"(soff), "s"(m) : "scc");
}
DI void sbar() { asm volatile("s_barrier" ::: "memory"); }
DI void fence() { __builtin_amdgcn_sched_barrier(0); }
template <int V> using IC = std::integral_constant<int, V>;
DI u32 clamp1(u32 v) { return min(v, SENT - 1); }
DI u32 umax3(u32 a, u32 b, u32 c) { return max(max(a, b), c); }
DI u32 smax(u32 m, u32x4 v) { return umax3(umax3(m, v.x, v.y), v.z, v.w); }
template <int I, int N> struct Unr {
  template <class F> static DI void go(F&& f) { f(std::integral_constant<int, I>{}); Unr<I + 1, N>::go(f); }
};
template <int N> struct Unr<N, N> { template <class F> static DI void go(F&&) {} };
template <int NF> DI int refetch(u32x4* a, srd_t s, const u32* voff, const u32* soff) {
  int spin = 0;
#pragma unroll 1
  for (; spin < SPIN_CAP; spin++) {
    __builtin_amdgcn_s_sleep(2);
    u64 bad = 0;
    Unr<0, NF>::go([&](auto k) {
      const u64 m = __ballot(smax(0, a[k]) == SENT);
      ald_if(m, a[k], s, voff[k], soff[k]);
      bad |= m;
    });
    if (!bad) break;
    Unr<0, NF>::go([&](auto k) { rf_wait(a[k]); });
  }
  return spin;
}
typedef f32x4 GeluK;
DI f32x2 sp2(float v) { return f32x2{v, v}; }
DI GeluK gelu_k(float r, float ir) { const float k0 = r * -2.3022082f, k1 = k0 * r * r * 0.044715f; return GeluK{k0, k1, ir, 0}; }
struct GeluT { f32x2 a0, a1, t0, t1; u32x2 o; };
template <int S> DI void gelu_step(GeluT& q, GeluK k) {
  if constexpr (S == 0) { q.t0 = q.a0 * q.a0; q.t1 = q.a1 * q.a1; }
  if constexpr (S == 1) { q.t0 = __builtin_elementwise_fma(q.t0, sp2(k[1]), sp2(k[0])); q.t1 = __builtin_elementwise_fma(q.t1, sp2(k[1]), sp2(k[0])); }
  if constexpr (S == 2) { q.t0 *= q.a0; q.t1 *= q.a1; }
  if constexpr (S == 3) { q.t0 = f32x2{__builtin_amdgcn_exp2f(q.t0[0]), __builtin_amdgcn_exp2f(q.t0[1])}; }
  if constexpr (S == 4) { q.t1 = f32x2{__builtin_amdgcn_exp2f(q.t1[0]), __builtin_amdgcn_exp2f(q.t1[1])}; q.t0 = __builtin_elementwise_fma(q.t0, sp2(k[2]), sp2(k[2])); }
  if constexpr (S == 5) { q.t0 = f32x2{__builtin_amdgcn_rcpf(q.t0[0]), __builtin_amdgcn_rcpf(q.t0[1])}; q.t1 = __builtin_elementwise_fma(q.t1, sp2(k[2]), sp2(k[2])); }
  if constexpr (S == 6) { q.t1 = f32x2{__builtin_amdgcn_rcpf(q.t1[0]), __builtin_amdgcn_rcpf(q.t1[1])}; q.t0 *= q.a0; }
  if constexpr (S == 7) { q.t1 *= q.a1; q.o = u32x2{clamp1(pk_bf16(q.t0[0], q.t0[1])), clamp1(pk_bf16(q.t1[0], q.t1[1]))}; pin(q.o); }
  else pin(q.t0, q.t1);
}
DI void gelu_in(GeluT& q, f32x4 a) { q.a0 = f32x2{a[0], a[1]}; q.a1 = f32x2{a[2], a[3]}; }
DI float bf_lo(u32 v) { return __uint_as_float(v << 16); }
DI float bf_hi(u32 v) { return __uint_as_float(v & 0xFFFF0000u); }
#define G_PREWAIT "s_waitcnt vmcnt(0)\n\t"
#define GLA 6
#define D1 2
#define D2 2
#define D3 2
#define D4 2
#define D5 2

#define G_TL(i) ""
#define G_R4(r) "v[" #r ":" #r "+3]"
#define G_LD(k) "buffer_load_dwordx4 " G_R4(16 + 4 * (k)) ", v181, s[84:87], s94 offen offset:1024*((" #k ")&3) sc1\n\t"
#define G_SO(q) "s_add_u32 s94, s88, 4096*" #q "\n\t"
#define G_LD4(q) G_SO(q) G_LD(4 * q) G_LD(4 * q + 1) G_LD(4 * q + 2) G_LD(4 * q + 3)
#define G_LDS "buffer_load_dwordx4 v[80:83], v182, s[84:87], s89 offen sc1\n\t"
#define G_MX(r) "v_max_u32 v180, v[" #r "], v[" #r "+2]\n\t"
#define G_CMP "v_cmp_eq_u32 vcc, -1, v180\n\t"
#define G_BR(k) "s_cbranch_vccnz .Lgs%=_" #k "\n.Lgb%=_" #k ":\n\t"
#define G_STUB(k) ".Lgs%=_" #k ":\n\ts_call_b64 s[90:91], .Lfb_refetch\n\ts_branch .Lgb%=_" #k "\n"
#define G_STUBS "s_branch .Lgb%=_x\n" G_STUB(s) G_STUB(0) G_STUB(1) G_STUB(2) G_STUB(3) G_STUB(4) G_STUB(5) G_STUB(6) G_STUB(7) G_STUB(8) G_STUB(9) \
  G_STUB(10) G_STUB(11) G_STUB(12) G_STUB(13) G_STUB(14) ".Lgb%=_x:\n\t"
#define G_CHK(r) G_MX(r) G_CMP "s_cbranch_vccz 1f\n\ts_call_b64 s[90:91], .Lfb_refetch\n1:\n\t"
#define G_RF(r, LD) G_MX(r) G_CMP "s_cbranch_vccz 1f\n\t" LD "s_mov_b32 s93, 1\n1:\n\t"
#define G_RF4(q) G_SO(q) G_RF(16 + 16 * q, G_LD(4 * q)) G_RF(20 + 16 * q, G_LD(4 * q + 1)) G_RF(24 + 16 * q, G_LD(4 * q + 2)) G_RF(28 + 16 * q, G_LD(4 * q + 3))
#define G_REFETCH                                                                                              \
  "s_branch 9f\n.Lfb_refetch:\n\ts_mov_b32 s92, 200000\n\ts_mov_b32 s96, s94\n8:\n\ts_waitcnt vmcnt(0) ; RF\n\ts_mov_b32 s93, 0\n\t"  \
  G_RF4(0) G_RF4(1) G_RF4(2) G_RF4(3) G_RF(80, G_LDS)                                                          \
  "s_cmp_eq_u32 s93, 0\n\ts_cbranch_scc1 7f\n\t" RFCNT "s_sub_u32 s92, s92, 1\n\ts_cbranch_scc0 8b\n\ts_waitcnt vmcnt(0) ; RF\n" \
  "7:\n\ts_mov_b32 s94, s96\n\ts_setpc_b64 s[90:91]\n9:"
#define RFCNT ""
#define G_TS(i) ""
#define G_STV
#define G_MM(f, k, sl) "v_mfma_f32_16x16x32_bf16 " G_R4(4 * (f)) ", " G_R4(84 + 4 * (sl)) ", " G_R4(16 + 4 * (k)) ", " G_R4(4 * (f)) "\n\t"
#define G_RD(sl, a, off) "ds_read_b128 " G_R4(84 + 4 * (sl)) ", %[" a "] offset:" #off "\n\t"
#define G_W(n, m) "s_waitcnt vmcnt(" #n ") lgkmcnt(" #m ")\n\t"
#define G_FIRST "s_waitcnt vmcnt(15)\n\t" G_MX(16) G_CMP G_BR(s)
#define M24(x) ((x) - 24 * ((x) / 24))
#define G_A(j) G_RD(M24(j), "lb", 1024 * ((j) + 24))
#define G_N(j) ""
#define G_S24(k, R, L, m) G_MM(0, k, M24(4 * k)) L R(4 * k) G_MM(1, k, M24(4 * k + 1)) R(4 * k + 1) G_W(14 - k, m) G_MX(20 + 4 * k) \
  G_MM(2, k, M24(4 * k + 2)) R(4 * k + 2) G_CMP G_MM(3, k, M24(4 * k + 3)) R(4 * k + 3) G_BR(k)
#define G_LOOP24(X)                                                                                            \
  G_STUBS G_FIRST G_S24(0, G_A, "", 15) G_S24(1, G_A, "", 15) G_S24(2, G_A, "", 15) G_S24(3, G_A, "", 15) G_S24(4, G_A, "", 15) G_S24(5, G_A, "", 15)    \
  G_S24(6, G_A, "", 15) G_S24(7, G_A, "", 15) G_S24(8, G_A, "", 15) G_S24(9, G_A, "", 15) G_S24(10, G_N, "", 15) G_S24(11, G_N, "", 12)           \
  G_S24(12, G_N, "", 8) G_S24(13, G_N, "", 4) G_S24(14, G_N, "", 0) G_MM(0, 15, 12) X G_MM(1, 15, 13) G_MM(2, 15, 14) G_MM(3, 15, 15) "s_nop 7"
#define G_P4(q) G_RD(4 * q, "lb", 4096 * q) G_RD(4 * q + 1, "lb", 4096 * q + 1024) G_RD(4 * q + 2, "lb", 4096 * q + 2048) G_RD(4 * q + 3, "lb", 4096 * q + 3072)
#define M12(x) ((x) - 12 * ((x) / 12))
#define G_B(k, f) G_RD(M12(4 * (k + 3) + f), "lb", 16384 * f + 1024 * (k + 3))
#define G_C(k, f) G_RD(M12(4 * (k + 3) + f), "lc", 16384 * (f & 1) + 1024 * (2 * (k - 13) + (f >> 1)))
#define G_S12(k, R) G_MM(0, k, M12(4 * k)) R(k, 0) G_MM(1, k, M12(4 * k + 1)) R(k, 1) G_W(14 - k, 6) G_MX(20 + 4 * k) \
  G_MM(2, k, M12(4 * k + 2)) R(k, 2) G_CMP G_MM(3, k, M12(4 * k + 3)) R(k, 3) G_BR(k)
#define G_LOOP12                                                                                               \
  G_STUBS "s_waitcnt lgkmcnt(6)\n\t" G_FIRST G_S12(0, G_B) G_S12(1, G_B) G_S12(2, G_B) G_S12(3, G_B) G_S12(4, G_B) G_S12(5, G_B) G_S12(6, G_B) \
  G_S12(7, G_B) G_S12(8, G_B) G_S12(9, G_B) G_S12(10, G_B) G_S12(11, G_B) G_S12(12, G_B) G_S12(13, G_C) G_S12(14, G_C)                         \
  G_MM(0, 15, 0) G_C(15, 0) G_MM(1, 15, 1) G_C(15, 1) "s_waitcnt lgkmcnt(8)\n\t" G_MM(2, 15, 2) G_C(15, 2) G_MM(3, 15, 3) G_C(15, 3) "s_waitcnt lgkmcnt(0)"
#define G_FIX [srd] "{s[84:87]}"(sws), [so] "{s88}"(so), [sso] "{s89}"(sso), [vo] "{v181}"(svoff), [vs] "{v182}"(sv)
#define G_CLOB "memory", "scc", "vcc", "v180", "s90", "s91", "s92", "s93", "s94", "s95", "s96"
template <int I> DI u32x4 frag(f32x32 t) { return __builtin_bit_cast(u32x4, __builtin_shufflevector(t, t, 4 * I, 4 * I + 1, 4 * I + 2, 4 * I + 3)); }
template <int I> DI f32x4 quad(f32x16 t) { return __builtin_shufflevector(t, t, 4 * I, 4 * I + 1, 4 * I + 2, 4 * I + 3); }
DI f32x16 cat4(f32x4 a, f32x4 b, f32x4 c, f32x4 d) {
  return __builtin_shufflevector(__builtin_shufflevector(a, b, 0, 1, 2, 3, 4, 5, 6, 7), __builtin_shufflevector(c, d, 0, 1, 2, 3, 4, 5, 6, 7), 0, 1, 2, 3, 4, 5, 6, 7, 8, 9, 10, 11, 12, 13, 14, 15);
}

__global__ void __launch_bounds__(512, 2)
fused_block(const char* __restrict__ px, const char* __restrict__ pwqkv, const char* __restrict__ pwo, const char* __restrict__ pw1,
            const char* __restrict__ pw2, const char* __restrict__ pw3, const char* __restrict__ pw4, char* __restrict__ pout, char* __restrict__ pws) {
  __shared__ __attribute__((aligned(16))) char lds_[163840];
  LAS char* lds = (LAS char*)lds_;
  const u32 lane = __builtin_amdgcn_mbcnt_hi(~0u, __builtin_amdgcn_mbcnt_lo(~0u, 0u));
  const u32 w = rfl(threadIdx.x >> 6);
  const u32 b = blockIdx.x, xcd = b & 7, jj = b >> 3;
  const u32 rb = 8 * (xcd >> 1) + (jj >> 2), hq = 4 * (xcd & 1) + (jj & 3);
  STAMP(0);
  STAMPR(62);
  asm volatile("" ::"s"(px), "s"(pwqkv), "s"(pwo), "s"(pw1), "s"(pw2), "s"(pw3), "s"(pw4), "s"(pout), "s"(pws));
  STAMP(20);
  const srd_t sws = __builtin_amdgcn_make_buffer_rsrc(pws, 0, WS_END, 0x00020000);
  const u64 ep = __hip_atomic_load((GAS u64*)(pws + WS_EPOCH), RLX_AGENT);
  const u32 h = lane >> 5, r31 = lane & 31;
  const u32 rot = (rb + 2 * hq) & 7;
  u64 tag;
  f32x32 acc, sp, rg0, rg1, rg2;
  const u32 kh = w & 1, hl = w >> 1, hd = 4 * hq + hl;
  const u32 kvoff = WS_K + lane * 16, vvoff = WS_V + h * 256 + (lane & 15) * 16;
  const u64 vmask = 0x0000FFFF0000FFFFull;
  const u32 hbase = hd * 65536;
  const u32 T0 = (2 * rb + kh) * 1024 - 2048;
  u32 tmp, tmp2;
#define ATT_INIT()                                                                                                          \
  asm volatile(A_INIT1(68) A_INIT1(80) A_INIT1(92) A_INIT1(104) A_INIT1(116) A_INIT1(128) A_INIT1(140) A_INIT1(152)         \
               A_INIT0(0) A_INIT0(8) A_INIT0(16) A_INIT0(24) A_INIT0(56)                                                    \
               : "={v[0:31]}"(acc), "={v[32:63]}"(sp), "={v[64:95]}"(rg0), "={v[96:127]}"(rg1), "={v[128:159]}"(rg2)        \
               : [one] "s"(0x3F803F803F803F80ull))
#define ATT_FILL(CMP)                                                                                                       \
  asm volatile(CMP " %[w], 4\n\ts_cbranch_scc0 1f\n\ts_mov_b32 %[T], %[T0]\n\t"                                              \
               A_RING(0) A_RING(1) A_RING(2) A_RING(3) A_RING(4) A_RING(5) A_RING(6) "1:"                                   \
               : "+{v[64:95]}"(rg0), "+{v[96:127]}"(rg1), "+{v[128:159]}"(rg2), [T] "=&s"(tmp2), [t] "=&s"(tmp)             \
               : [kv] "v"(kvoff), [vv] "v"(vvoff), [srd] "s"(sws), [vm] "s"(vmask), [hb] "s"(hbase), [w] "s"(w), [T0] "s"(T0) \
               : "memory", "scc")

  if (w < 4) {
    const u32 rt = w & 1, ch = w >> 1;
    u32 touch = 0;
    if (w == 0) {
      u64 pc;
      u32 len;
      asm volatile("s_getpc_b64 %0\n.Lfb_here:\n\ts_mov_b32 %1, .Lfb_last-.Lfb_here" : "=s"(pc), "=s"(len));
      touch = *(const GAS u32*)(pc + min(((jj & 7) * 64 + lane) * 128, len));
    } else if (w == 1) {
      touch = __builtin_amdgcn_raw_buffer_load_b32(sws, min(lane, 25u) << 20, 0, 0);
    }
    u32x4 xf[32];
    f32x16 accK = {}, accV = {}, accQ = {};
    float s0 = 0, s1 = 0, q0 = 0, q1 = 0, ck = 0, cv = 0, cq = 0;
    const u32 xl = (32 * rt + r31) * 128 + ((h ^ ((r31 >> 1) & 7)) * 16);
    const u32 xl4[4] = {opaque(xl), opaque(xl ^ 32), opaque(xl ^ 64), opaque(xl ^ 96)};
    const u32 wl = (8 * h + ((lane >> 2) & 3)) * 128 + ((ch ^ ((lane >> 3) & 1)) * 64) + ((lane >> 4) & 1) * 32 + (lane & 3) * 8;
    const u32 wlk = opaque(wl + L_RK), wlq = opaque(wl + L_WQ);
    auto csum = [&](float c, u32x4 f) { return dot2(f.w, 0x3F803F80u, dot2(f.z, 0x3F803F80u, dot2(f.y, 0x3F803F80u, dot2(f.x, 0x3F803F80u, c)))); };
#pragma unroll
    for (int blk = 0; blk < 8; blk++) {
      bar();
      if (blk == 0) STAMP(2);
      if (blk == 4) STAMP(3);
      if (blk == 7) STAMP(4);
      const u32 so = (blk & 3) * 8192;
#pragma unroll
      for (int s4 = 0; s4 < 4; s4++) xf[4 * blk + s4] = lds_r128(lds, xl4[s4] + so);
#pragma unroll
      for (int s4 = 0; s4 < 4; s4++) {
        const int s = 4 * blk + s4;
        const u32x4 wk = lds_tr2(lds, wlk + so + s4 * 2048, wlk + so + s4 * 2048 + 512);
        const u32x4 wv = lds_tr2(lds, wlk + so + 32768 + s4 * 2048, wlk + so + 32768 + s4 * 2048 + 512);
        accK = mfma32(wk, xf[s], accK);
        accV = mfma32(xf[s], wv, accV);
        s0 = dot2(xf[s].x, 0x3F803F80u, s0); q0 = dot2(xf[s].x, xf[s].x, q0);
        s1 = dot2(xf[s].y, 0x3F803F80u, s1); q1 = dot2(xf[s].y, xf[s].y, q1);
        s0 = dot2(xf[s].z, 0x3F803F80u, s0); q0 = dot2(xf[s].z, xf[s].z, q0);
        s1 = dot2(xf[s].w, 0x3F803F80u, s1); q1 = dot2(xf[s].w, xf[s].w, q1);
        ck = csum(ck, wk);
        cv = csum(cv, wv);
      }
      asm volatile("" : "+v"(s0), "+v"(s1), "+v"(q0), "+v"(q1), "+v"(ck), "+v"(cv));
    }
    const float sum = swap32_add(s0 + s1), sq = swap32_add(q0 + q1);
    const float mean = sum * (1.f / 512.f);
    const float rstd = __builtin_amdgcn_rsqf(fmaxf(sq * (1.f / 512.f) - mean * mean, 0.f) + 1e-5f);
    u32x4 mf;
    {
      const float nm = -mean, hi = __uint_as_float(__float_as_uint(nm) & 0xFFFF0000u), sel = h ? nm - hi : hi;
      mf = u32x4{pk_bf16(sel, sel), 0, 0, 0};
    }
    auto csfrag = [&](float c) {
      const float t = swap32_add(c), hi = __uint_as_float(__float_as_uint(t) & 0xFFFF0000u);
      return u32x4{pk_bf16(hi, t - hi), 0, 0, 0};
    };
    *(LAS float*)(lds + L_RST + w * 128 + r31 * 4) = rstd;
    asm volatile("" : "+v"(touch));
    tag = ep + 1;
    const u32 tile = 2 * rb + rt, hp = 2 * hq + ch;
    {
      accK = mfma32(csfrag(ck), mf, accK);
      accV = mfma32(mf, csfrag(cv), accV);
      const u32 pi = (r31 & 19) | ((r31 & 4) << 1) | ((r31 & 8) >> 1);
      const u32 ko = WS_K + (2 * hp * 64 + tile) * 1024 + h * 512 + pi * 16;
      u32x4 a, c;
      for (int i = 0; i < 4; i++) { a[i] = pk_bf16(accK[2 * i] * rstd, accK[2 * i + 1] * rstd); c[i] = pk_bf16(accK[8 + 2 * i] * rstd, accK[9 + 2 * i] * rstd); }
      st_c(sws, ko, a);
      st_c(sws, ko + 65536, c);
    }
    {
      f32x4 rs[4];
      for (int i = 0; i < 4; i++) rs[i] = *(LAS f32x4*)(lds + L_RST + w * 128 + i * 32 + h * 16);
      const u32 vo = WS_V + ((2 * hp + (r31 >> 4)) * 64 + tile) * 1024 + h * 256 + (r31 & 15) * 16;
      u32x4 a, c;
      for (int i = 0; i < 4; i++) {
        a[i] = pk_bf16(accV[2 * i] * rs[i >> 1][(2 * i) & 3], accV[2 * i + 1] * rs[i >> 1][(2 * i + 1) & 3]);
        c[i] = pk_bf16(accV[8 + 2 * i] * rs[2 + (i >> 1)][(2 * i) & 3], accV[9 + 2 * i] * rs[2 + (i >> 1)][(2 * i + 1) & 3]);
      }
      st_c(sws, vo, a);
      st_c(sws, vo + 512, c);
    }
    STAMP(5);
#pragma unroll
    for (int qc = 0; qc < 4; qc++) {
      bar();
      if (qc == 0) STAMP(6);
      if (qc == 3) {
        STAMP(7);
        wait_vm<0>();
        if (lane == 0) __hip_atomic_store((GAS u64*)(pws + WS_KVFLAG + (hp * 64 + tile) * 8), tag, RLX_AGENT);
      }
#pragma unroll
      for (int s = 8 * qc; s < 8 * qc + 8; s++) {
        const u32x4 wq = lds_tr2(lds, wlq + s * 2048, wlq + s * 2048 + 512);
        accQ = mfma32(wq, xf[s], accQ);
        cq = csum(cq, wq);
      }
      asm volatile("" : "+v"(cq));
    }
    accQ = mfma32(csfrag(cq), mf, accQ);
    {
      const float qs = rstd * (0.25f * 1.4426950408889634f);
      u32x4 a, c;
      for (int i = 0; i < 4; i++) { a[i] = pk_bf16(accQ[2 * i] * qs, accQ[2 * i + 1] * qs); c[i] = pk_bf16(accQ[8 + 2 * i] * qs, accQ[9 + 2 * i] * qs); }
      lds_w128(lds, L_QX + w * 2048 + lane * 16, a);
      lds_w128(lds, L_QX + w * 2048 + 1024 + lane * 16, c);
    }
    STAMP(8);
  } else {
    const u32 hw = w - 4;
    const srd_t sx = __builtin_amdgcn_make_buffer_rsrc((void*)px, 0, 2048u * 1024u, 0x00020000);
    const srd_t sw = __builtin_amdgcn_make_buffer_rsrc((void*)pwqkv, 0, 512u * 3072u, 0x00020000);
    const u32 xv0 = (lane >> 3) * 1024 + (((lane & 7) ^ (lane >> 4)) * 16), xv1 = (xv0 ^ 64) + 8192;
    const u32 wv0 = (lane >> 3) * 3072 + (((lane & 7) ^ ((lane >> 2) & 4)) * 16), wv1 = wv0 + 8 * 3072;
    const u32 xs = (64 * rb + 16 * hw) * 1024, ws_ = 16 * hw * 3072 + hq * 128;
    auto blk_issue = [&](u32 blk) {
      const u32 kb = (blk + rot) & 7, l = (blk & 3) * 8192 + hw * 2048, wo = ws_ + kb * (64 * 3072);
      dma16(sx, xv0, xs + kb * 128, l);
      dma16(sx, xv1, xs + kb * 128, l + 1024);
      dma16(sw, wv0, wo + 1024, l + L_RK);
      dma16(sw, wv1, wo + 1024, l + L_RK + 1024);
      dma16(sw, wv0, wo + 2048, l + L_RV);
      dma16(sw, wv1, wo + 2048, l + L_RV + 1024);
    };
    auto q_issue = [&](u32 blk) {
      const u32 kb = (blk + rot) & 7, l = L_WQ + blk * 8192 + hw * 2048, wo = ws_ + kb * (64 * 3072);
      dma16(sw, wv0, wo, l);
      dma16(sw, wv1, wo, l + 1024);
    };
    blk_issue(0);
    blk_issue(1);
    STAMP(1);
#pragma unroll 1
    for (u32 blk = 0; blk < 6; blk++) {
      blk_issue(blk + 2);
      wait_vm<12>();
      bar();
    }
    STAMP(3);
    q_issue(0); q_issue(1); q_issue(2);
    wait_vm<12>();
    bar();
    q_issue(3); q_issue(4); q_issue(5);
    wait_vm<12>();
    bar();
    STAMP(4);
    q_issue(6); q_issue(7);
    __builtin_amdgcn_sched_barrier(0);
    u32x4 tu[5];
    u32 tdst[5];
    const u32 wid = (b * 4 + hw) & 1023;
#pragma unroll
    for (int n = 0; n < 5; n++) {
      const u32 U = n * 1024 + wid, g = U & 3;
      const char* src;
      u32 rlo, rhi, pitch = 1024, fstr = 1024, col, dst;
      if (U < 1536) {
        const u32 m = U >> 9, kb = (U >> 2) & 15, cs = (U >> 6) & 7;
        rlo = m == 0 ? 32 * kb + 16 * (g >> 1) + 4 * (g & 1) : 64 * (kb >> 1) + 16 * g + 8 * (kb & 1);
        rhi = rlo + (m == 0 ? 8 : 4);
        src = m == 0 ? pwo : m == 1 ? pw1 : pw2;
        col = cs * 128;
        dst = WS_WOT + m * (512u << 10) + (cs * 16 + kb) * 4096;
      } else if (U < 1536 + 1792) {
        const u32 t = (U - 1536) >> 2, kb = t & 15, sl = t >> 4;
        rlo = 64 * (kb >> 1) + 16 * g + 8 * (kb & 1);
        rhi = rlo + 4;
        src = pw3; pitch = 3584; fstr = 16384;
        col = sl * 128;
        dst = WS_W3T + (64 * sl + kb) * 1024;
      } else {
        const u32 t = (U - 1536 - 1792) >> 2, cs = t & 7, kb = t >> 3;
        const bool sg = kb >= 48;
        const u32 Tt = sg ? 7 * (2 * (kb - 48) + (g >> 1)) + 6 : 7 * (kb & 15) + 2 * (kb >> 4);
        const u32 T1 = sg ? Tt : Tt + 1, g0 = sg ? 2 * (g & 1) : g, g1 = sg ? g0 + 1 : g;
        rlo = 64 * (Tt >> 2) + 16 * g0 + 4 * (Tt & 3);
        rhi = 64 * (T1 >> 2) + 16 * g1 + 4 * (T1 & 3);
        src = pw4;
        col = cs * 128;
        dst = WS_W4T + (cs * 56 + kb) * 4096;
      }
      tdst[n] = dst + g * 256 + (lane >> 4) * fstr + (lane & 15) * 16;
      tu[n] = *(const u32x4*)(src + (((h ? rhi : rlo) + ((lane >> 3) & 3)) * pitch + col + (lane & 7) * 16));
    }
    __builtin_amdgcn_sched_barrier(0);
    STAMP(5);
    wait_vm<17>(); bar();
    STAMP(6);
    wait_vm<13>(); bar();
    wait_vm<9>(); bar();
    wait_vm<5>(); bar();
    STAMP(7);
    tag = ep + 1;
    const u32 sc = L_TSC + hw * 5120;
#pragma unroll
    for (int n = 0; n < 5; n++) lds_w128(lds, sc + n * 1024 + lane * 16, tu[n]);
    const u32 tl = sc + ((lane >> 2) & 3) * 128 + (lane & 3) * 32 + (lane >> 4) * 8;
#pragma unroll
    for (int n = 0; n < 5; n++) st_c(sws, tdst[n], lds_tr2(lds, tl + n * 1024, tl + n * 1024 + 512));
    STAMP(8);
  }
  ATT_INIT();
  if (w >= 4) {
    const u32 tl_ = (u32)tag, th_ = (u32)(tag >> 32);
#pragma unroll 1
    for (int spin = 0; spin < SPIN_CAP; spin++) {
      asm volatile("" ::: "memory");
      const u32x4 f = ld_c(sws, WS_KVFLAG + hq * 1024 + lane * 16);
      if (__all(f.x == tl_ && f.y == th_ && f.z == tl_ && f.w == th_)) break;
      __builtin_amdgcn_s_sleep(4);
    }
  }
  STAMP(10);
  ATT_FILL("s_cmp_ge_u32");
  bar();
  STAMP(9);
  ATT_FILL("s_cmp_lt_u32");
  u32 T = T0 + 7 * 2048;

  const u32x4 qa = lds_r128(lds, L_QX + (2 * (hl >> 1)) * 2048 + (hl & 1) * 1024 + lane * 16);
  const u32x4 qb = lds_r128(lds, L_QX + (2 * (hl >> 1) + 1) * 2048 + (hl & 1) * 1024 + lane * 16);
  if (threadIdx.x == 0) *(LAS u32*)(lds + L_CNT) = 0;
  const f32x16 zero16 = {};
  const u32x4 sent = {SENT, SENT, SENT, SENT};
  const u32 svoff = lane * 16;
  const u32 flds = L_XCH + w * 2304;
  asm volatile("s_waitcnt vmcnt(18)\n\t" A_MFMA(32, 64, "qa") "s_nop 11" : "+{v[32:63]}"(sp) : "{v[64:95]}"(rg0), [qa] "v"(qa) : "memory");
  STAMP(11);
#pragma unroll 1
  for (u32 outer = 0; outer < 4; outer++) {
    asm volatile(A_STEP(0, 7, 1, 18, A_MEM0, "") A_STEP(1, 0, 2, 17, A_MEM(1, 0), "") A_STEP(2, 1, 3, 16, A_MEM(2, 1), "")
                 A_STEP(3, 2, 4, 15, A_MEM(3, 2), A_FLAGS) A_STEP(4, 3, 5, 14, A_MEM(4, 3), "") A_STEP(5, 4, 6, 13, A_MEM(5, 4), "")
                 A_STEP(6, 5, 7, 12, A_MEM(6, 5), "") A_STEP(7, 6, 0, 18, A_MEM(7, 6), "") ROUND_BAR
                 : "+{v[0:31]}"(acc), "+{v[32:63]}"(sp), "+{v[64:95]}"(rg0), "+{v[96:127]}"(rg1), "+{v[128:159]}"(rg2), [T] "+s"(T), [t] "=&s"(tmp)
                 : [qa] "v"(qa), [qb] "v"(qb), [kv] "v"(kvoff), [vv] "v"(vvoff), [sv] "v"(svoff), [sent] "v"(sent), [idx] "v"(0xDDDDu), [srd] "s"(sws),
                   [vm] "s"(vmask), [hb] "s"(hbase), [outer] "s"(outer), [sso] "s"(WS_O + (b * 61 + w * 8) * 1024), [ssm] "s"(WS_O + (b * 61 + 60) * 1024),
                   [dso] "s"(WS_WOT + ((jj & 7) * 64 + w * 8) * 1024), [dl] "s"(w * 8192), [fl] "s"(flds)
                 : "memory", "scc");
    if (outer == 1) {
      if (lane == 0) {
        const u32 c = __hip_atomic_fetch_add((LAS u32*)(lds + L_CNT), 1u, __ATOMIC_RELAXED, __HIP_MEMORY_SCOPE_WORKGROUP);
        if (c == 7) __hip_atomic_store((GAS u64*)(pws + WS_WGFLAG + b * 8), tag, RLX_AGENT);
      }
    }
    if (outer == 2) {
      wait_vm<12>();
      u32x4 fl0 = lds_r128(lds, flds + svoff), fl1 = lds_r128(lds, flds + 1024 + svoff);
      const u32 tl = (u32)tag, th = (u32)(tag >> 32);
#pragma unroll 1
      for (int spin = 0; spin < SPIN_CAP; spin++) {
        const bool ok = fl0.x == tl && fl0.y == th && fl0.z == tl && fl0.w == th && fl1.x == tl && fl1.y == th && fl1.z == tl && fl1.w == th;
        if (__all(ok)) break;
        __builtin_amdgcn_s_sleep(8);
        asm volatile("" ::: "memory");
        fl0 = ld_c(sws, WS_WGFLAG + svoff);
        fl1 = ld_c(sws, WS_WGFLAG + 1024 + svoff);
      }
      if (b == 0 && w == 0 && lane == 0) __hip_atomic_store((GAS u64*)(pws + WS_EPOCH), tag, RLX_AGENT);
      STAMP(33);
    }
  }
  asm volatile("s_nop 1\n\t" A_SMF(16, 72 + 12 * 7, 56) "s_nop 15\n\ts_nop 3"
               : "+{v[0:31]}"(acc) : "{v[32:63]}"(sp), "{v[128:159]}"(rg2), [idx] "v"(0xDDDDu));
  const f32x16 oa = __builtin_shufflevector(acc, acc, 0, 1, 2, 3, 4, 5, 6, 7, 8, 9, 10, 11, 12, 13, 14, 15);
  const f32x16 ob = __builtin_shufflevector(acc, acc, 16, 17, 18, 19, 20, 21, 22, 23, 24, 25, 26, 27, 28, 29, 30, 31);
  STAMP(12);
  const u32 rb2 = 2 * xcd + (jj >> 4), u = jj & 15, prb = 2 * rb2 + (u >> 3), cs = u & 7;
  const u32 st = w & 3, e = w >> 2, g = lane >> 4, i15 = lane & 15;
  const u32 s16 = 4 * prb + st;
  u32x4 xr0, xr1;
  {
    const srd_t sx = __builtin_amdgcn_make_buffer_rsrc((void*)px, 0, 2048u * 1024u, 0x00020000);
    asm volatile("s_cmp_lg_u32 %5, 0\n\ts_cbranch_scc1 1f\n\tbuffer_load_dwordx4 %0, %2, %3, %4 offen\n\tbuffer_load_dwordx4 %1, %2, %3, %4 offen offset:16\n1:"
                 : "=&v"(xr0), "=&v"(xr1) : "v"(i15 * 1024 + g * 32), "s"(sx), "s"(s16 * 16384 + cs * 128), "s"(e) : "scc");
  }
  {
    const f32x16 snd = kh ? oa : ob;
    f32x16 o = kh ? ob : oa;
    const u32 xo = L_XCH + w * 2304;
    *(LAS f32x4*)(lds + xo + lane * 16) = f32x4{snd[0], snd[1], snd[2], snd[3]};
    *(LAS f32x4*)(lds + xo + 1024 + lane * 16) = f32x4{snd[4], snd[5], snd[6], snd[7]};
    *(LAS float*)(lds + xo + 2048 + lane * 4) = snd[8];
    bar();
    const u32 xi = L_XCH + (w ^ 1) * 2304;
    const f32x4 r0 = *(LAS f32x4*)(lds + xi + lane * 16), r1 = *(LAS f32x4*)(lds + xi + 1024 + lane * 16);
    float l = o[8] + *(LAS float*)(lds + xi + 2048 + lane * 4);
    for (int i = 0; i < 4; i++) { o[i] += r0[i]; o[4 + i] += r1[i]; }
    float z = 0.f;
    for (int i = 0; i < 8; i++) z = fmaf(o[i], 0.f, z);
    const bool safe = l > 0x1p-40f && l < 0x1p40f && z == 0.f;
    if (__builtin_expect(!__all(safe), 0)) {
      const u32x4 q = kh ? qb : qa;
      u32x4 k1 = {0, 0, 0, 0}, v1 = {0x3F803F80u, 0x3F803F80u, 0x3F803F80u, 0x3F803F80u}, v2 = v1;
      float m = -3.0e38f;
      o = zero16;
#pragma unroll 1
      for (u32 t = 0; t < 64; t++) {
        ring_issue(k1, v1, v2, sws, kvoff, vvoff, hbase + t * 1024, vmask);
        ring_wait(0, k1, v1, v2);
        f32x16 s = mfma32(k1, q, zero16);
        float mx = m;
        for (int i = 0; i < 16; i++) mx = fmaxf(mx, s[i]);
        mx = swap32_max(mx);
        const float al = __builtin_amdgcn_exp2f(m - mx);
        m = mx;
        for (int i = 0; i < 16; i++) { s[i] -= mx; o[i] *= al; }
        o = smf(v1, exp_lo(s), o);
        o = smf(v2, exp_hi(s), o);
      }
      l = o[8];
    }
    const float il = __builtin_amdgcn_rcpf(l);
    u32x4 ov;
    for (int i = 0; i < 4; i++) ov[i] = pk_bf16(o[2 * i] * il, o[2 * i + 1] * il);
    const u32 rb16 = 4 * rb + 2 * kh + (r31 >> 4);
    st_c(sws, WS_O + (rb16 * 16 + (hd >> 1)) * 1024 + (2 * (hl & 1) + h) * 256 + (lane & 15) * 16, clampS(ov));
  }

  STAMP(13);
  constexpr u32 L_A = 0, L_B = 65536, L_X = 131072, L_C = 147456, L_S = L_A + 49152;
  const u32x4 ones = {0x3F803F80u, 0x3F803F80u, 0x3F803F80u, 0x3F803F80u};
  const f32x4 zero4 = {};
  const u32 w4src = WS_W4T + cs * 56 * 4096, w3src = WS_W3T + u * 7 * 16384;
  const u32 sv = g * 256 + i15 * 16;
  int nrf = 0;
  float mean, r1, var;
  auto row_stats = [&](u32x4 a) {
    const f32x4 p = __builtin_bit_cast(f32x4, a);
    const float s = swap32_add(swap16_add(p[0] + p[2])), q = swap32_add(swap16_add(p[1] + p[3]));
    mean = s * (1.f / 512.f);
    var = fmaxf(q * (1.f / 512.f) - mean * mean, 0.f);
    r1 = __builtin_amdgcn_rsqf(var + 1e-5f);
  };
  u32x4 w4p[18];
  auto w4_off = [&](int k) { return k < 6 ? (k >> 1) * 16384 + (w * 2 + (k & 1)) * 1024 : (3 + (k - 6) / 3) * 16384 + (e ? 4 + 3 * st + (k - 6) % 3 : st) * 1024; };
  f32x4 res0, res1;
  asm volatile(G_REFETCH ::: "memory");
  if (e) wait_vm<1>(); else wait_vm<3>();
  bar();
  STAMP(40); CNTS(20);
  if (e == 0) {
    f32x4 ac[4], hv[4], yv[4];
    const u32 fo = opaque((s16 * 16 + 2 * cs) * 1024 + svoff), sto = opaque(s16 * 1024 + (cs >> 1) * 256 + i15 * 16 + (cs & 1) * 8);
    u32 hho;
    {
      const int sh = (cs & 2 ? -1 : 1) * (cs & 4 ? 2 : 1);
      const u32 rd = s16 * 16 + i15 + sh, rw = rd & 2047;
      hho = opaque(WS_HH + (rw >> 4) * 16384 + 2 * cs * 1024 + (g * 16 + (rw & 15)) * 16 + (rd != rw));
    }
    auto gemm = [&](auto ns_, auto dl_, u32 lbase, u32 abase) {
      constexpr int NS = ns_, DL = dl_;
      const u32 lb = svoff + lbase, so = abase + s16 * 16384, sso = 0;
      f32x32 a0, a1, q0, q1, q2;
      u32x4 sx = {};
      f32x16 c = cat4(ac[0], ac[1], ac[2], ac[3]);
#define G_GEMM(X, T0, T1, T2, T3)                                                                                                    \
      asm volatile(G_P4(0) G_P4(1) G_P4(2) G_P4(3) G_P4(4) G_P4(5) T0 G_PREWAIT T1 "s_sleep %[dl]\n\t" G_LD4(0) G_LD4(1) G_LD4(2) G_LD4(3) T2         \
                   "s_waitcnt lgkmcnt(0)\n\ts_barrier\n\t" T3 G_LOOP24(X)                                                            \
                   : "+{v[0:15]}"(c), "={v[16:47]}"(a0), "={v[48:79]}"(a1), "+{v[80:83]}"(sx), "={v[84:115]}"(q0), "={v[116:147]}"(q1), "={v[148:179]}"(q2) G_STV \
                   : [lb] "v"(lb), [dl] "n"(DL), G_FIX : G_CLOB)
      if constexpr (NS == 1) G_GEMM("s_barrier\n\t", G_TS(34), G_TS(35), G_TS(36), G_TS(37));
      else if constexpr (NS == 2) G_GEMM("", G_TS(58), G_TS(59), G_TS(60), G_TS(61));
      else G_GEMM("", "", "", "", "");
      ac[0] = quad<0>(c); ac[1] = quad<1>(c); ac[2] = quad<2>(c); ac[3] = quad<3>(c);
    };
    auto pack8 = [&](f32x4 a, f32x4 c) { return clampS(u32x4{pk_bf16(a[0], a[1]), pk_bf16(a[2], a[3]), pk_bf16(c[0], c[1]), pk_bf16(c[2], c[3])}); };
    auto put = [&](u32 dbase, u32 sbase) {
      st_c(sws, fo + dbase, pack8(ac[0], ac[1]));
      st_c(sws, fo + dbase + 1024, pack8(ac[2], ac[3]));
      const f32x4 s4 = (ac[0] + ac[1]) + (ac[2] + ac[3]);
      const f32x4 q4 = __builtin_elementwise_fma(ac[0], ac[0], ac[1] * ac[1]) + __builtin_elementwise_fma(ac[2], ac[2], ac[3] * ac[3]);
      const float s = swap32_add(swap16_add((s4[0] + s4[1]) + (s4[2] + s4[3]))), q = swap32_add(swap16_add((q4[0] + q4[1]) + (q4[2] + q4[3])));
      if (g == 0) __builtin_amdgcn_raw_buffer_store_b64(u32x2{clamp1(__float_as_uint(s)), clamp1(__float_as_uint(q))}, sws, sto + sbase, 0, 16);
    };
    vm_wait<1>(xr0, xr1);
    ac[0] = f32x4{bf_lo(xr0[0]), bf_hi(xr0[0]), bf_lo(xr0[1]), bf_hi(xr0[1])}; ac[1] = f32x4{bf_lo(xr0[2]), bf_hi(xr0[2]), bf_lo(xr0[3]), bf_hi(xr0[3])};
    ac[2] = f32x4{bf_lo(xr1[0]), bf_hi(xr1[0]), bf_lo(xr1[1]), bf_hi(xr1[1])}; ac[3] = f32x4{bf_lo(xr1[2]), bf_hi(xr1[2]), bf_lo(xr1[3]), bf_hi(xr1[3])};
    gemm(IC<0>{}, IC<D1>{}, L_A, WS_O);
    STAMP(42); CNTS(21);
    put(WS_H, WS_HST);
    sbar();
    for (int f = 0; f < 4; f++) hv[f] = ac[f];
    STAMP(43);
    for (int f = 0; f < 4; f++) { yv[f] = *(LAS f32x4*)(lds + L_C + f * 64 + g * 16); ac[f] = zero4; }
    gemm(IC<1>{}, IC<D2>{}, L_B, WS_H);
    STAMP(45); CNTS(22);
    {
      const f32x4 rs = *(LAS f32x4*)(lds + L_X + st * 4096 + i15 * 16);
      mean = rs[3];
      const GeluK gk = rs;
      r1 = *(LAS float*)(lds + L_X + st * 4096 + 256 + i15 * 4);
      for (int f = 0; f < 4; f++) ac[f] -= yv[f] * mean;
      GeluT q[4];
      for (int f = 0; f < 4; f++) gelu_in(q[f], ac[f]);
      Unr<0, 8>::go([&](auto s) { gelu_step<s>(q[0], gk); gelu_step<s>(q[1], gk); });
      u32x4 o = {q[0].o.x, q[0].o.y, q[1].o.x, q[1].o.y};
      if (hho & 1) o = u32x4{0, 0, 0, 0};
      st_c(sws, hho & ~1u, o);
      Unr<0, 8>::go([&](auto s) { gelu_step<s>(q[2], gk); gelu_step<s>(q[3], gk); });
      o = u32x4{q[2].o.x, q[2].o.y, q[3].o.x, q[3].o.y};
      if (hho & 1) o = u32x4{0, 0, 0, 0};
      st_c(sws, (hho & ~1u) + 1024, o);
      sbar();
      for (int f = 0; f < 4; f++) yv[f] = (hv[f] - mean) * r1;
    }
    STAMP(46);
    for (int f = 0; f < 4; f++) ac[f] = yv[f];
    gemm(IC<2>{}, IC<D3>{}, L_A, WS_HH);
    STAMP(48); CNTS(23);
    put(WS_X1, WS_XST);
    sbar();
    res0 = hv[0] + ac[0];
    res1 = hv[1] + ac[1];
    *(LAS f32x4*)(lds + L_S + 1024 + st * 2048 + svoff) = hv[2] + ac[2];
    *(LAS f32x4*)(lds + L_S + 2048 + st * 2048 + svoff) = hv[3] + ac[3];
    for (int k = 0; k < 18; k++) undef(w4p[k]);
    STAMP(49);
    wait_vm<0>();
  } else {
    auto dma_n = [&](int n, u32 src, u32 dst) { for (int q = 0; q < n; q++) dma16c(sws, svoff, src + q * 1024, dst + q * 1024); };
    auto colsum = [&](f32x4 c, int n, u32 base, u32 str) {
#pragma unroll
      for (int k = 0; k < n; k++) c = mfma16(ones, lds_r128(lds, base + k * str + svoff), c);
      return c;
    };
    auto w4_ld = [&](auto k) { ald_c<0>(w4p[k], sws, svoff, w4src + 7 * 16384 + w4_off(k)); };
    const float c1 = colsum(zero4, 16, L_B + st * 1024, 4096)[0];
    if (g == 0) *(LAS float*)(lds + L_C + st * 64 + i15 * 4) = c1;
    bar();
    Unr<0, 10>::go(w4_ld);
    sbar();
    dma_n(8, WS_W2T + cs * 65536 + st * 16384, L_A + st * 16384);
    sbar();
    {
      u32x4 sx;
      const u32 vo = sv, sf = rfl(WS_HST + s16 * 1024);
      ald_c<0>(sx, sws, vo, sf);
      dma_n(8, WS_W2T + cs * 65536 + st * 16384 + 8192, L_A + st * 16384 + 8192);
      vm_wait<8>(sx);
      if (__any(smax(0, sx) == SENT)) refetch<1>(&sx, sws, &vo, &sf);
      row_stats(sx);
      const float v2 = fmaf(var * r1, r1, 1e-5f), r2 = __builtin_amdgcn_rsqf(v2);
      const GeluK gk = gelu_k(r1 * r2, (var + 1e-5f) * r1 * v2 * r2);
      if (g == 0) {
        *(LAS f32x4*)(lds + L_X + st * 4096 + i15 * 16) = f32x4{gk[0], gk[1], gk[2], mean};
        *(LAS float*)(lds + L_X + st * 4096 + 256 + i15 * 4) = r1;
      }
    }
    if (st == 1) dma_n(16, w3src + 5 * 16384, L_C);
    Unr<10, 18>::go(w4_ld);
    bar();
    if (st == 1) wait_vm<24>(); else wait_vm<8>();
    sbar();
    dma_n(8, w3src + st * 16384, L_B + st * 16384);
    if (st == 0) dma_n(16, w3src + 4 * 16384, L_X);
    sbar();
    dma_n(8, w3src + st * 16384 + 8192, L_B + st * 16384 + 8192);
    if (st == 3) { dma_n(16, w3src + 6 * 16384, L_A); wait_vm<24>(); } else wait_vm<8>();
    f32x4 c2 = zero4, c3 = colsum(zero4, 8, L_B + st * 16384, 1024);
    if (st < 2) c2 = colsum(zero4, 16, st == 0 ? L_X : L_C, 1024);
    pin(c2, c3);
    bar();
    dma_n(8, w4src + st * 8192, L_A + 16384 + st * 8192);
    Unr<0, 18>::go([&](auto k) { vm_wait<8>(w4p[k]); });
    c3 = colsum(c3, 8, L_B + st * 16384 + 8192, 1024);
    if (st == 3) c2 = colsum(zero4, 16, L_A, 1024);
    if (g == 0) {
      *(LAS float*)(lds + L_S + st * 64 + i15 * 4) = c3[0];
      if (st != 2) *(LAS float*)(lds + L_S + (st == 3 ? 6 : 4 + st) * 64 + i15 * 4) = c2[0];
    }
    undef(res0); undef(res1);
  }
  STAMP(50);
  {
    const u32 t16 = 8 * rb2 + w, so = WS_X1 + t16 * 16384, sso = WS_XST + t16 * 1024;
    u32x4 af[16], wq[112];
    f32x4 ga[7];
    const u32 lb = L_B + svoff, lc = opaque(L_X + svoff), la = opaque(L_A + svoff);
    auto rd = [&](int j) {
      if (j < 96) wq[j] = lds_r128(lds, lc + (j & 1) * 16384 + ((j - 64) >> 1) * 1024);
      else if (j < 112) wq[j] = lds_r128(lds, la + (j - 96) * 1024);
    };
    constexpr int LA = 8;
    GeluK gk;
    {
      f32x32 a0, a1, q0;
      u32x4 sx;
      f32x16 q1;
      asm volatile("s_cmp_lg_u32 %[e], 0\n\ts_cbranch_scc1 1f\n\ts_sleep %[dl]\n1:\n\t" G_LDS G_LD4(0) G_LD4(1) G_LD4(2) G_LD4(3) "s_waitcnt lgkmcnt(0)\n\ts_barrier"
                   : "={v[16:47]}"(a0), "={v[48:79]}"(a1), "={v[80:83]}"(sx) : [e] "s"(e), [dl] "n"(D4), G_FIX : G_CLOB);
      for (int t = 0; t < 7; t++) ga[t] = *(LAS f32x4*)(lds + L_S + t * 64 + g * 16);
      if (e) {
        res0 = *(LAS f32x4*)(lds + L_S + 1024 + st * 2048 + svoff);
        res1 = *(LAS f32x4*)(lds + L_S + 2048 + st * 2048 + svoff);
      }
      asm volatile(G_B(-3, 0) G_B(-3, 1) G_B(-3, 2) G_B(-3, 3) G_B(-2, 0) G_B(-2, 1) G_B(-2, 2) G_B(-2, 3) G_B(-1, 0) G_B(-1, 1) G_B(-1, 2) G_B(-1, 3)
                   "s_waitcnt vmcnt(16)\n\t" G_CHK(80)
                   : "+{v[16:47]}"(a0), "+{v[48:79]}"(a1), "+{v[80:83]}"(sx), "={v[84:115]}"(q0), "={v[116:131]}"(q1)
                   : [lb] "v"(lb), G_FIX : G_CLOB);
      row_stats(sx);
      gk = gelu_k(r1, (var + 1e-5f) * r1);
      pin(gk);
      for (int t = 0; t < 7; t++) ga[t] *= -mean;
      pin(ga[4], ga[5], ga[6]);
      f32x16 c = cat4(ga[0], ga[1], ga[2], ga[3]);
      asm volatile(G_LOOP12
                   : "+{v[0:15]}"(c), "+{v[16:47]}"(a0), "+{v[48:79]}"(a1), "+{v[80:83]}"(sx), "+{v[84:115]}"(q0), "+{v[116:131]}"(q1)
                   : [lb] "v"(lb), [lc] "v"(lc), G_FIX : G_CLOB);
      ga[0] = quad<0>(c); ga[1] = quad<1>(c); ga[2] = quad<2>(c); ga[3] = quad<3>(c);
      Unr<0, 8>::go([&](auto k) { af[k] = frag<k>(a0); af[8 + k] = frag<k>(a1); });
      Unr<0, 4>::go([&](auto k) { wq[64 + k] = frag<4 + k>(q0); wq[68 + k] = __builtin_bit_cast(u32x4, quad<k>(q1)); wq[72 + k] = frag<k>(q0); });
    }
    STAMP(51); CNTS(24);
    auto bfill = [&] {
      sbar();
      for (int q = 0; q < 8; q++) dma16c(sws, svoff, w4src + 3 * 16384 + (w * 8 + q) * 1024, L_B + (w * 8 + q) * 1024);
      for (int q = 0; q < 2; q++) dma16c(sws, svoff, w4src + 2 * 16384 + (w * 2 + q) * 1024, L_S + (w * 2 + q) * 1024);
    };
    if (e) bfill();
    const u32 go = opaque(WS_G + t16 * 57344 + u * 1024 + svoff);
    const u32 go6 = opaque(WS_G + t16 * 57344 + (48 + (u >> 1)) * 1024 + ((2 * (u & 1) + (g >> 1)) * 16 + i15) * 16 + (g & 1) * 8);
    GeluT q[7];
    Unr<0, 32>::go([&](auto j_) {
      constexpr int j = j_, t = j >> 3;
      if ((j & 7) == 0) gelu_in(q[t], ga[t]);
      ga[4 + (j & 1)] = mfma16(wq[64 + j], af[j >> 1], ga[4 + (j & 1)]);
      if (j >= 4) rd(64 + j + LA);
      gelu_step<(j & 7)>(q[t], gk);
      if constexpr (j % 3 == 2) {
        constexpr int m = j / 3, k = m < 6 ? m : 6 + 3 * (m - 6);
        ald_if0(e, w4p[k], sws, svoff, w4src + 7 * 16384 + w4_off(k));
      }
      if ((j & 15) == 15) st_c(sws, go + (t >> 1) * 16384, u32x4{q[t - 1].o.x, q[t - 1].o.y, q[t].o.x, q[t].o.y});
      fence();
    });
    STAMP(57);
    if (!e) bfill();
    Unr<0, 16>::go([&](auto j_) {
      constexpr int j = j_, t = 4 + (j >> 3);
      if ((j & 7) == 0) gelu_in(q[t], ga[t]);
      ga[6] = mfma16(wq[96 + j], af[j], ga[6]);
      rd(96 + j + LA);
      gelu_step<(j & 7)>(q[t], gk);
      fence();
    });
    st_c(sws, go + 32768, u32x4{q[4].o.x, q[4].o.y, q[5].o.x, q[5].o.y});
    gelu_in(q[6], ga[6]);
    Unr<0, 8>::go([&](auto s) { gelu_step<s>(q[6], gk); });
    __builtin_amdgcn_raw_buffer_store_b64(q[6].o, sws, go6, 0, 16);
  }
  STAMP(52);
  {
    const u32 kq = 2 * e + (st & 1), rh = st >> 1;
    u32x4 gf[28];
    const u32 so0 = WS_G + (4 * prb + 2 * rh + (kq & 1)) * 57344 + kq * 1024, so1 = WS_G + (4 * prb + 2 * rh + 1 - (kq & 1)) * 57344 + kq * 1024;
    auto g_ld = [&](auto n) { if constexpr (n < 14) { ald_c<0>(gf[2 * n], sws, svoff, so0 + n * 4096); ald_c<0>(gf[2 * n + 1], sws, svoff, so1 + n * 4096); } };
    constexpr int GL = GLA;
    Unr<0, 18>::go([&](auto k) { vm_wait<2>(w4p[k]); });
    bar();
    __builtin_amdgcn_s_sleep(D5);
    Unr<0, GL>::go(g_ld);
    auto slot = [&](int n) { return n < 3 ? L_A + 16384 + n * 16384 : n < 7 ? L_B + (n - 3) * 16384 : n == 7 ? L_X : n == 8 ? L_C : n == 9 ? L_A : n < 13 ? L_A + (n - 9) * 16384 : L_B; };
    for (int k = 0; k < 6; k++) lds_w128(lds, slot(7 + (k >> 1)) + (w * 2 + (k & 1)) * 1024 + svoff, w4p[k]);
    f32x4 oc[2][4];
    u32x4 wf[14][4];
    const u32 wl0 = opaque(kq * 4096 + e * 2048 + svoff), wl1 = opaque(kq * 4096 + 2048 - e * 2048 + svoff);
    auto rdw = [&](int n) {
      wf[n][0] = lds_r128(lds, wl0 + slot(n)); wf[n][1] = lds_r128(lds, wl0 + slot(n) + 1024);
      wf[n][2] = lds_r128(lds, wl1 + slot(n)); wf[n][3] = lds_r128(lds, wl1 + slot(n) + 1024);
    };
    oc[0][0] = res0; oc[0][1] = res1;
    oc[0][2] = oc[0][3] = oc[1][0] = oc[1][1] = oc[1][2] = oc[1][3] = zero4;
    rdw(0);
    Unr<0, 14>::go([&](auto n_) {
      constexpr int n = n_;
      if (n == 4) {
        STAMP(53);
        bar();
        const u32 wo = (e ? 4 + 3 * st : st) * 1024 + svoff;
        for (int m = 0; m < 4; m++) lds_w128(lds, slot(10 + m) + wo, w4p[6 + 3 * m]);
        if (e) for (int m = 0; m < 4; m++) { lds_w128(lds, slot(10 + m) + wo + 1024, w4p[7 + 3 * m]); lds_w128(lds, slot(10 + m) + wo + 2048, w4p[8 + 3 * m]); }
      }
      if (n == 6) { bar(); STAMP(54); }
      if (n < 13) rdw(n + 1);
      vm_wait<(n + GL > 14 ? 2 * (13 - n) : 2 * GL - 2)>(gf[2 * n], gf[2 * n + 1]);
      g_ld(IC<n + GL>{});
      if (__builtin_expect(__any(umax3(umax3(gf[2 * n].x, gf[2 * n].z, gf[2 * n + 1].x), gf[2 * n + 1].z, 0) == SENT), 0)) {
        const u32 vo[2] = {svoff, svoff}, sf[2] = {so0 + n * 4096, so1 + n * 4096};
        nrf += refetch<2>(gf + 2 * n, sws, vo, sf);
      }
      for (int f = 0; f < 4; f++)
        for (int s = 0; s < 2; s++) oc[1 - s][3 - f] = mfma16(wf[n][3 - f], gf[2 * n + 1 - s], oc[1 - s][3 - f]);
      fence();
    });
    STAMP(55); CNT(25, nrf);
    const u32 ro = L_B + 16384 + w * 6144 + svoff;
    *(LAS f32x4*)(lds + ro + 5120) = oc[1][3]; *(LAS f32x4*)(lds + ro + 3072) = oc[0][3];
    *(LAS f32x4*)(lds + ro + 4096) = oc[1][2]; *(LAS f32x4*)(lds + ro + 2048) = oc[0][2];
    *(LAS f32x4*)(lds + ro + 1024) = oc[1][1]; *(LAS f32x4*)(lds + ro) = oc[1][0];
    bar();
    for (int d = 1; d < 4; d++) {
      const u32 ri = L_B + 16384 + (w ^ ((d & 1) | ((d & 2) << 1))) * 6144 + (d - 1) * 2048 + svoff;
      oc[0][0] += *(LAS f32x4*)(lds + ri);
      oc[0][1] += *(LAS f32x4*)(lds + ri + 1024);
    }
    const u32x4 o = {pk_bf16(oc[0][0][0], oc[0][0][1]), pk_bf16(oc[0][0][2], oc[0][0][3]), pk_bf16(oc[0][1][0], oc[0][1][1]), pk_bf16(oc[0][1][2], oc[0][1][3])};
    asm volatile(".Lfb_last:");
    *(u32x4*)(pout + (s16 * 16 + i15) * 1024 + cs * 128 + g * 32 + e * 16) = o;
  }
  STAMP(56);
  wait_vm<0>();
  STAMP(14);
  STAMPR(63);
}

extern "C" void kernel_launch(void* const* d_in, const int* in_sizes, int n_in, void* d_out, int out_size, void* d_ws, size_t ws_size,
                              hipStream_t stream) {
  fused_block<<<256, 512, 0, stream>>>((const char*)d_in[0], (const char*)d_in[1], (const char*)d_in[2], (const char*)d_in[4], (const char*)d_in[5],
                                       (const char*)d_in[6], (const char*)d_in[7], (char*)d_out, (char*)d_ws);
}
```

```cpp
#define ROT 2
#define TCH 6
#define KXR 1
#define VXR 2
#define P2G 1
#define UDMA 1
#define G0M 1
#define FLG 2
#define UJ 8
#define ULATE 2
#define UA 2
#define EAR 1
#define NOFW 1
#define WQ 2
#include <hip/hip_runtime.h>
#include <type_traits>

typedef unsigned u32;
typedef unsigned long long u64;
typedef __attribute__((ext_vector_type(4))) u32 u32x4;
typedef __attribute__((ext_vector_type(2))) u32 u32x2;
typedef __attribute__((ext_vector_type(4))) short s16x4;
typedef __attribute__((ext_vector_type(2))) __bf16 bf16x2;
typedef __attribute__((ext_vector_type(8))) __bf16 bf16x8;
typedef __attribute__((ext_vector_type(16))) __bf16 bf16x16;
typedef __attribute__((ext_vector_type(2))) float f32x2;
typedef __attribute__((ext_vector_type(4))) float f32x4;
typedef __attribute__((ext_vector_type(8))) float f32x8;
typedef __attribute__((ext_vector_type(16))) float f32x16;
typedef __attribute__((ext_vector_type(32))) float f32x32;
typedef __amdgpu_buffer_rsrc_t srd_t;
#define LAS __attribute__((address_space(3)))
#define GAS __attribute__((address_space(1)))
#define RLX_AGENT __ATOMIC_RELAXED, __HIP_MEMORY_SCOPE_AGENT
#define DI __device__ __forceinline__

#define EPO 0
constexpr u32 WS_EPOCH = EPO;
constexpr u32 WS_KVFLAG = 4096;
constexpr u32 WS_WGFLAG = 16384;
constexpr u32 WS_WFLAG = 20480;
constexpr u32 WS_STAMP = 32768;
constexpr u32 WS_K = 1u << 20;
constexpr u32 WS_V = 3u << 20;
constexpr u32 WS_WOT = 5u << 20;
constexpr u32 WS_W1T = WS_WOT + (512u << 10);
constexpr u32 WS_W2T = WS_WOT + (1024u << 10);
constexpr u32 WS_W3T = WS_WOT + (1536u << 10);
constexpr u32 WS_W4T = WS_W3T + (1792u << 10);
constexpr u32 WS_O = 10u << 20;
constexpr u32 WS_H = 12u << 20;
constexpr u32 WS_HH = 14u << 20;
constexpr u32 WS_X1 = 16u << 20;
constexpr u32 WS_G = 18u << 20;
constexpr u32 WS_HST = 25u << 20;
constexpr u32 WS_XST = WS_HST + (128u << 10);
constexpr u32 WS_HINT = 26u << 20;
constexpr u32 WS_END = WS_HINT + (512u << 10);
constexpr u32 SENT = 0xFFFFFFFFu;
constexpr int SPIN_CAP = 200000;

constexpr u32 l_k(int j) { return 65536 + 16384 * (j < 6 ? j : j - 6); }
constexpr u32 l_q(int j) { return j < 3 ? 8192 * j : j == 5 ? 24576 : j < 5 ? l_k(2) + 8192 * (j - 3) : l_k(3) + 8192 * (j - 6); }
constexpr u32 L_RST = 131072;
constexpr u32 L_TSC = 32768;
constexpr u32 L_UN = 147456;
constexpr u32 L_CNT = L_RST + 768;
constexpr u32 L_QP = 65536;
constexpr u32 L_CQ = L_RST + 1024;
constexpr u32 L_EX = L_RST + 4096;
constexpr u32 L_QX = 139264;
constexpr u32 L_XCH = 131072 + 1024;

DI u32 opaque(u32 v) { asm volatile("" : "+v"(v)); return v; }
DI u32 rfl(u32 v) { return __builtin_amdgcn_readfirstlane(v); }
DI bf16x8 as_bf8(u32x4 v) { return __builtin_bit_cast(bf16x8, v); }
DI u32 pk_bf16(float a, float b) { f32x2 p = {a, b}; return __builtin_bit_cast(u32, __builtin_convertvector(p, bf16x2)); }
DI float swap32_add(float v) { auto r = __builtin_amdgcn_permlane32_swap(__float_as_uint(v), __float_as_uint(v), false, false); return __uint_as_float(r[0]) + __uint_as_float(r[1]); }
DI float swap16_add(float v) { auto r = __builtin_amdgcn_permlane16_swap(__float_as_uint(v), __float_as_uint(v), false, false); return __uint_as_float(r[0]) + __uint_as_float(r[1]); }
DI float swap32_max(float v) { auto r = __builtin_amdgcn_permlane32_swap(__float_as_uint(v), __float_as_uint(v), false, false); return fmaxf(__uint_as_float(r[0]), __uint_as_float(r[1])); }
DI f32x16 mfma32(u32x4 a, u32x4 b, f32x16 c) { return __builtin_amdgcn_mfma_f32_32x32x16_bf16(as_bf8(a), as_bf8(b), c, 0, 0, 0); }
DI f32x4 mfma16(u32x4 a, u32x4 b, f32x4 c) { return __builtin_amdgcn_mfma_f32_16x16x32_bf16(as_bf8(a), as_bf8(b), c, 0, 0, 0); }
DI f32x16 smf(u32x4 a, f32x8 p, f32x16 c) { return __builtin_amdgcn_smfmac_f32_32x32x32_bf16(as_bf8(a), __builtin_bit_cast(bf16x16, p), c, 0xDDDD, 0, 0); }
DI u32x4 lds_r128(LAS char* l, u32 off) { return *(LAS u32x4*)(l + off); }
DI void lds_w128(LAS char* l, u32 off, u32x4 v) { *(LAS u32x4*)(l + off) = v; }
DI u32x2 lds_tr(LAS char* l, u32 off) { return __builtin_bit_cast(u32x2, __builtin_amdgcn_ds_read_tr16_b64_v4i16((LAS s16x4*)(l + off))); }
DI u32x4 lds_tr2(LAS char* l, u32 off0, u32 off1) { u32x2 a = lds_tr(l, off0), b = lds_tr(l, off1); return u32x4{a.x, a.y, b.x, b.y}; }
#define P1NT ""
DI void dma16(srd_t srd, u32 voff, u32 soff, u32 ldsaddr) {
  asm volatile("s_mov_b32 m0, %0\n\ts_nop 0\n\tbuffer_load_dwordx4 %1, %2, %3 offen" P1NT " lds" ::"s"(ldsaddr), "v"(voff), "s"(srd), "s"(soff) : "memory");
}
template <int N> DI void wait_vm() { asm volatile("s_waitcnt vmcnt(%0)" ::"n"(N) : "memory"); }
DI void wait_lgkm0() { asm volatile("s_waitcnt lgkmcnt(0)" ::: "memory"); }
DI void bar() { asm volatile("s_waitcnt lgkmcnt(0)\n\ts_barrier" ::: "memory"); }
DI u32x4 ld_c(srd_t s, u32 voff) { return __builtin_amdgcn_raw_buffer_load_b128(s, voff, 0, 16); }
DI void st_c(srd_t s, u32 voff, u32x4 v) { __builtin_amdgcn_raw_buffer_store_b128(v, s, voff, 0, 16); }
DI u32x4 clampS(u32x4 v) { return u32x4{min(v.x, SENT - 1), min(v.y, SENT - 1), min(v.z, SENT - 1), min(v.w, SENT - 1)}; }

#define STAMP(i) do {} while (0)
#define CNT(i, v) do {} while (0)
#define CNTS(i) do {} while (0)
#define STAMPR(i) do {} while (0)
#define POST_STV

DI void ring_issue(u32x4& kf, u32x4& va, u32x4& vb, srd_t srd, u32 kvoff, u32 vvoff, u32 soff, u64 vmask) {
  asm volatile(
      "buffer_load_dwordx4 %0, %3, %5, %6 offen sc1\n\t"
      "s_mov_b64 exec, %7\n\t"
      "buffer_load_dwordx4 %1, %4, %5, %6 offen sc1\n\t"
      "buffer_load_dwordx4 %2, %4, %5, %6 offen offset:512 sc1\n\t"
      "s_mov_b64 exec, -1"
      : "+v"(kf), "+v"(va), "+v"(vb)
      : "v"(kvoff), "v"(vvoff), "s"(srd), "s"(soff), "s"(vmask)
      : "memory");
}
DI void ring_wait(int n, u32x4& kf, u32x4& va, u32x4& vb) {
  asm volatile("s_waitcnt vmcnt(%3)" : "+v"(kf), "+v"(va), "+v"(vb) : "n"(n) : "memory");
}
DI f32x8 exp_lo(f32x16 s) { f32x8 p; for (int i = 0; i < 8; i++) p[i] = __builtin_amdgcn_exp2f(s[i]); return p; }
DI f32x8 exp_hi(f32x16 s) { f32x8 p; for (int i = 0; i < 8; i++) p[i] = __builtin_amdgcn_exp2f(s[8 + i]); return p; }

#define TOP 0
#define OTL 0
#define NTL 0
#define TST 128
#define WPF 0
#define ACKT 0
#define EPS 1
#define WXR 0
#define UNT " nt"
#define TSRC
#define TMASK 3
#define DEP 3
#define WSOFF 0
#define FLAGQ 2
#define HQR 0
#define ROT3 0
#define T0RB 0
static_assert(WS_WGFLAG == 0x4000 && WS_WFLAG == 0x5000 && WS_W1T - WS_WOT == 0x80000 && WS_HINT == 0x1A00000 && L_XCH == 132096, "asm constants");
#define P1_ASM \
  "s_waitcnt vmcnt(1)\n" \
  "s_barrier\n" \
  "ds_read_b128 v[64:67], v216 offset:0\n" \
  "ds_read_b128 v[68:71], v217 offset:0\n" \
  "ds_read_b128 v[72:75], v218 offset:0\n" \
  "ds_read_b128 v[76:79], v219 offset:0\n" \
  "ds_read_b64_tr_b16 v[32:33], v232 offset:0\n" \
  "ds_read_b64_tr_b16 v[34:35], v232 offset:512\n" \
  "ds_read_b64_tr_b16 v[36:37], v232 offset:8192\n" \
  "ds_read_b64_tr_b16 v[38:39], v232 offset:8704\n" \
  "ds_read_b64_tr_b16 v[40:41], v233 offset:0\n" \
  "ds_read_b64_tr_b16 v[42:43], v233 offset:512\n" \
  "ds_read_b64_tr_b16 v[44:45], v233 offset:8192\n" \
  "ds_read_b64_tr_b16 v[46:47], v233 offset:8704\n" \
  "ds_read_b64_tr_b16 v[48:49], v234 offset:0\n" \
  "ds_read_b64_tr_b16 v[50:51], v234 offset:512\n" \
  "ds_read_b64_tr_b16 v[52:53], v234 offset:8192\n" \
  "ds_read_b64_tr_b16 v[54:55], v234 offset:8704\n" \
  "ds_read_b64_tr_b16 v[56:57], v235 offset:0\n" \
  "ds_read_b64_tr_b16 v[58:59], v235 offset:512\n" \
  "ds_read_b64_tr_b16 v[60:61], v235 offset:8192\n" \
  "ds_read_b64_tr_b16 v[62:63], v235 offset:8704\n" \
  "s_waitcnt lgkmcnt(14)\n" \
  "v_mfma_f32_32x32x16_bf16 v[0:15], v[32:35], v[64:67], 0\n" \
  "s_waitcnt lgkmcnt(12)\n" \
  "v_mfma_f32_32x32x16_bf16 v[16:31], v[64:67], v[36:39], 0\n" \
  "v_mfma_f32_16x16x32_bf16 v[204:207], v[212:215], v[32:35], 0\n" \
  "v_mfma_f32_16x16x32_bf16 v[208:211], v[212:215], v[36:39], 0\n" \
  "s_waitcnt lgkmcnt(10)\n" \
  "v_mfma_f32_32x32x16_bf16 v[0:15], v[40:43], v[68:71], v[0:15]\n" \
  "s_waitcnt lgkmcnt(8)\n" \
  "v_mfma_f32_32x32x16_bf16 v[16:31], v[68:71], v[44:47], v[16:31]\n" \
  "v_mfma_f32_16x16x32_bf16 v[204:207], v[212:215], v[40:43], v[204:207]\n" \
  "v_mfma_f32_16x16x32_bf16 v[208:211], v[212:215], v[44:47], v[208:211]\n" \
  "s_waitcnt lgkmcnt(6)\n" \
  "v_mfma_f32_32x32x16_bf16 v[0:15], v[48:51], v[72:75], v[0:15]\n" \
  "s_waitcnt lgkmcnt(4)\n" \
  "v_mfma_f32_32x32x16_bf16 v[16:31], v[72:75], v[52:55], v[16:31]\n" \
  "s_waitcnt lgkmcnt(2)\n" \
  "v_mfma_f32_32x32x16_bf16 v[0:15], v[56:59], v[76:79], v[0:15]\n" \
  "s_waitcnt lgkmcnt(0)\n" \
  "v_mfma_f32_32x32x16_bf16 v[16:31], v[76:79], v[60:63], v[16:31]\n" \
  "s_barrier\n" \
  "ds_read_b128 v[80:83], v216 offset:8192\n" \
  "ds_read_b128 v[84:87], v217 offset:8192\n" \
  "v_mfma_f32_4x4x4_16b_bf16 v[196:199], v[64:65], v[64:65], 0\n" \
  "ds_read_b128 v[88:91], v218 offset:8192\n" \
  "v_mfma_f32_4x4x4_16b_bf16 v[200:203], v[66:67], v[66:67], 0\n" \
  "ds_read_b128 v[92:95], v219 offset:8192\n" \
  "v_mfma_f32_16x16x32_bf16 v[192:195], v[212:215], v[64:67], 0\n" \
  "ds_read_b64_tr_b16 v[32:33], v232 offset:16384\n" \
  "ds_read_b64_tr_b16 v[34:35], v232 offset:16896\n" \
  "v_mfma_f32_4x4x4_16b_bf16 v[196:199], v[72:73], v[72:73], v[196:199]\n" \
  "ds_read_b64_tr_b16 v[36:37], v232 offset:24576\n" \
  "ds_read_b64_tr_b16 v[38:39], v232 offset:25088\n" \
  "v_mfma_f32_4x4x4_16b_bf16 v[200:203], v[74:75], v[74:75], v[200:203]\n" \
  "ds_read_b64_tr_b16 v[40:41], v233 offset:16384\n" \
  "ds_read_b64_tr_b16 v[42:43], v233 offset:16896\n" \
  "v_mfma_f32_16x16x32_bf16 v[192:195], v[212:215], v[72:75], v[192:195]\n" \
  "ds_read_b64_tr_b16 v[44:45], v233 offset:24576\n" \
  "ds_read_b64_tr_b16 v[46:47], v233 offset:25088\n" \
  "ds_read_b64_tr_b16 v[48:49], v234 offset:16384\n" \
  "ds_read_b64_tr_b16 v[50:51], v234 offset:16896\n" \
  "ds_read_b64_tr_b16 v[52:53], v234 offset:24576\n" \
  "ds_read_b64_tr_b16 v[54:55], v234 offset:25088\n" \
  "ds_read_b64_tr_b16 v[56:57], v235 offset:16384\n" \
  "ds_read_b64_tr_b16 v[58:59], v235 offset:16896\n" \
  "ds_read_b64_tr_b16 v[60:61], v235 offset:24576\n" \
  "ds_read_b64_tr_b16 v[62:63], v235 offset:25088\n" \
  "s_waitcnt lgkmcnt(14)\n" \
  "v_mfma_f32_32x32x16_bf16 v[0:15], v[32:35], v[80:83], v[0:15]\n" \
  "s_waitcnt lgkmcnt(12)\n" \
  "v_mfma_f32_32x32x16_bf16 v[16:31], v[80:83], v[36:39], v[16:31]\n" \
  "v_mfma_f32_16x16x32_bf16 v[204:207], v[212:215], v[32:35], v[204:207]\n" \
  "v_mfma_f32_16x16x32_bf16 v[208:211], v[212:215], v[36:39], v[208:211]\n" \
  "s_waitcnt lgkmcnt(10)\n" \
  "v_mfma_f32_32x32x16_bf16 v[0:15], v[40:43], v[84:87], v[0:15]\n" \
  "s_waitcnt lgkmcnt(8)\n" \
  "v_mfma_f32_32x32x16_bf16 v[16:31], v[84:87], v[44:47], v[16:31]\n" \
  "v_mfma_f32_16x16x32_bf16 v[204:207], v[212:215], v[40:43], v[204:207]\n" \
  "v_mfma_f32_16x16x32_bf16 v[208:211], v[212:215], v[44:47], v[208:211]\n" \
  "s_waitcnt lgkmcnt(6)\n" \
  "v_mfma_f32_32x32x16_bf16 v[0:15], v[48:51], v[88:91], v[0:15]\n" \
  "s_waitcnt lgkmcnt(4)\n" \
  "v_mfma_f32_32x32x16_bf16 v[16:31], v[88:91], v[52:55], v[16:31]\n" \
  "s_waitcnt lgkmcnt(2)\n" \
  "v_mfma_f32_32x32x16_bf16 v[0:15], v[56:59], v[92:95], v[0:15]\n" \
  "s_waitcnt lgkmcnt(0)\n" \
  "v_mfma_f32_32x32x16_bf16 v[16:31], v[92:95], v[60:63], v[16:31]\n" \
  "s_barrier\n" \
  "global_load_dwordx4 v[224:227], v224, %[p0] nt\n" \
  "ds_read_b128 v[96:99], v216 offset:16384\n" \
  "ds_read_b128 v[100:103], v217 offset:16384\n" \
  "v_mfma_f32_4x4x4_16b_bf16 v[196:199], v[80:81], v[80:81], v[196:199]\n" \
  "ds_read_b128 v[104:107], v218 offset:16384\n" \
  "v_mfma_f32_4x4x4_16b_bf16 v[200:203], v[82:83], v[82:83], v[200:203]\n" \
  "ds_read_b128 v[108:111], v219 offset:16384\n" \
  "v_mfma_f32_16x16x32_bf16 v[192:195], v[212:215], v[80:83], v[192:195]\n" \
  "ds_read_b64_tr_b16 v[32:33], v232 offset:32768\n" \
  "ds_read_b64_tr_b16 v[34:35], v232 offset:33280\n" \
  "v_mfma_f32_4x4x4_16b_bf16 v[196:199], v[88:89], v[88:89], v[196:199]\n" \
  "ds_read_b64_tr_b16 v[36:37], v232 offset:40960\n" \
  "ds_read_b64_tr_b16 v[38:39], v232 offset:41472\n" \
  "v_mfma_f32_4x4x4_16b_bf16 v[200:203], v[90:91], v[90:91], v[200:203]\n" \
  "ds_read_b64_tr_b16 v[40:41], v233 offset:32768\n" \
  "ds_read_b64_tr_b16 v[42:43], v233 offset:33280\n" \
  "v_mfma_f32_16x16x32_bf16 v[192:195], v[212:215], v[88:91], v[192:195]\n" \
  "ds_read_b64_tr_b16 v[44:45], v233 offset:40960\n" \
  "ds_read_b64_tr_b16 v[46:47], v233 offset:41472\n" \
  "ds_read_b64_tr_b16 v[48:49], v234 offset:32768\n" \
  "ds_read_b64_tr_b16 v[50:51], v234 offset:33280\n" \
  "ds_read_b64_tr_b16 v[52:53], v234 offset:40960\n" \
  "ds_read_b64_tr_b16 v[54:55], v234 offset:41472\n" \
  "ds_read_b64_tr_b16 v[56:57], v235 offset:32768\n" \
  "ds_read_b64_tr_b16 v[58:59], v235 offset:33280\n" \
  "ds_read_b64_tr_b16 v[60:61], v235 offset:40960\n" \
  "ds_read_b64_tr_b16 v[62:63], v235 offset:41472\n" \
  "s_waitcnt lgkmcnt(14)\n" \
  "v_mfma_f32_32x32x16_bf16 v[0:15], v[32:35], v[96:99], v[0:15]\n" \
  "s_waitcnt lgkmcnt(12)\n" \
  "v_mfma_f32_32x32x16_bf16 v[16:31], v[96:99], v[36:39], v[16:31]\n" \
  "v_mfma_f32_16x16x32_bf16 v[204:207], v[212:215], v[32:35], v[204:207]\n" \
  "v_mfma_f32_16x16x32_bf16 v[208:211], v[212:215], v[36:39], v[208:211]\n" \
  "s_waitcnt lgkmcnt(10)\n" \
  "v_mfma_f32_32x32x16_bf16 v[0:15], v[40:43], v[100:103], v[0:15]\n" \
  "s_waitcnt lgkmcnt(8)\n" \
  "v_mfma_f32_32x32x16_bf16 v[16:31], v[100:103], v[44:47], v[16:31]\n" \
  "v_mfma_f32_16x16x32_bf16 v[204:207], v[212:215], v[40:43], v[204:207]\n" \
  "v_mfma_f32_16x16x32_bf16 v[208:211], v[212:215], v[44:47], v[208:211]\n" \
  "s_waitcnt lgkmcnt(6)\n" \
  "v_mfma_f32_32x32x16_bf16 v[0:15], v[48:51], v[104:107], v[0:15]\n" \
  "s_waitcnt lgkmcnt(4)\n" \
  "v_mfma_f32_32x32x16_bf16 v[16:31], v[104:107], v[52:55], v[16:31]\n" \
  "s_waitcnt lgkmcnt(2)\n" \
  "v_mfma_f32_32x32x16_bf16 v[0:15], v[56:59], v[108:111], v[0:15]\n" \
  "s_waitcnt lgkmcnt(0)\n" \
  "v_mfma_f32_32x32x16_bf16 v[16:31], v[108:111], v[60:63], v[16:31]\n" \
  "s_barrier\n" \
  "ds_read_b128 v[112:115], v216 offset:24576\n" \
  "ds_read_b128 v[116:119], v217 offset:24576\n" \
  "v_mfma_f32_4x4x4_16b_bf16 v[196:199], v[96:97], v[96:97], v[196:199]\n" \
  "ds_read_b128 v[120:123], v218 offset:24576\n" \
  "v_mfma_f32_4x4x4_16b_bf16 v[200:203], v[98:99], v[98:99], v[200:203]\n" \
  "ds_read_b128 v[124:127], v219 offset:24576\n" \
  "v_mfma_f32_16x16x32_bf16 v[192:195], v[212:215], v[96:99], v[192:195]\n" \
  "ds_read_b64_tr_b16 v[32:33], v232 offset:49152\n" \
  "ds_read_b64_tr_b16 v[34:35], v232 offset:49664\n" \
  "v_mfma_f32_4x4x4_16b_bf16 v[196:199], v[104:105], v[104:105], v[196:199]\n" \
  "ds_read_b64_tr_b16 v[36:37], v232 offset:57344\n" \
  "ds_read_b64_tr_b16 v[38:39], v232 offset:57856\n" \
  "v_mfma_f32_4x4x4_16b_bf16 v[200:203], v[106:107], v[106:107], v[200:203]\n" \
  "ds_read_b64_tr_b16 v[40:41], v233 offset:49152\n" \
  "ds_read_b64_tr_b16 v[42:43], v233 offset:49664\n" \
  "v_mfma_f32_16x16x32_bf16 v[192:195], v[212:215], v[104:107], v[192:195]\n" \
  "ds_read_b64_tr_b16 v[44:45], v233 offset:57344\n" \
  "ds_read_b64_tr_b16 v[46:47], v233 offset:57856\n" \
  "ds_read_b64_tr_b16 v[48:49], v234 offset:49152\n" \
  "ds_read_b64_tr_b16 v[50:51], v234 offset:49664\n" \
  "ds_read_b64_tr_b16 v[52:53], v234 offset:57344\n" \
  "ds_read_b64_tr_b16 v[54:55], v234 offset:57856\n" \
  "ds_read_b64_tr_b16 v[56:57], v235 offset:49152\n" \
  "ds_read_b64_tr_b16 v[58:59], v235 offset:49664\n" \
  "ds_read_b64_tr_b16 v[60:61], v235 offset:57344\n" \
  "ds_read_b64_tr_b16 v[62:63], v235 offset:57856\n" \
  "s_waitcnt lgkmcnt(14)\n" \
  "v_mfma_f32_32x32x16_bf16 v[0:15], v[32:35], v[112:115], v[0:15]\n" \
  "s_waitcnt lgkmcnt(12)\n" \
  "v_mfma_f32_32x32x16_bf16 v[16:31], v[112:115], v[36:39], v[16:31]\n" \
  "v_mfma_f32_16x16x32_bf16 v[204:207], v[212:215], v[32:35], v[204:207]\n" \
  "v_mfma_f32_16x16x32_bf16 v[208:211], v[212:215], v[36:39], v[208:211]\n" \
  "s_waitcnt lgkmcnt(10)\n" \
  "v_mfma_f32_32x32x16_bf16 v[0:15], v[40:43], v[116:119], v[0:15]\n" \
  "s_waitcnt lgkmcnt(8)\n" \
  "v_mfma_f32_32x32x16_bf16 v[16:31], v[116:119], v[44:47], v[16:31]\n" \
  "v_mfma_f32_16x16x32_bf16 v[204:207], v[212:215], v[40:43], v[204:207]\n" \
  "v_mfma_f32_16x16x32_bf16 v[208:211], v[212:215], v[44:47], v[208:211]\n" \
  "v_add_u32 v232, 0x10000, v232\n" \
  "v_add_u32 v233, 0x10000, v233\n" \
  "v_add_u32 v234, 0x10000, v234\n" \
  "v_add_u32 v235, 0x10000, v235\n" \
  "s_waitcnt lgkmcnt(6)\n" \
  "v_mfma_f32_32x32x16_bf16 v[0:15], v[48:51], v[120:123], v[0:15]\n" \
  "s_waitcnt lgkmcnt(4)\n" \
  "v_mfma_f32_32x32x16_bf16 v[16:31], v[120:123], v[52:55], v[16:31]\n" \
  "s_waitcnt lgkmcnt(2)\n" \
  "v_mfma_f32_32x32x16_bf16 v[0:15], v[56:59], v[124:127], v[0:15]\n" \
  "s_waitcnt lgkmcnt(0)\n" \
  "v_mfma_f32_32x32x16_bf16 v[16:31], v[124:127], v[60:63], v[16:31]\n" \
  "s_barrier\n" \
  "ds_read_b128 v[128:131], v216 offset:32768\n" \
  "ds_read_b128 v[132:135], v217 offset:32768\n" \
  "v_mfma_f32_4x4x4_16b_bf16 v[196:199], v[112:113], v[112:113], v[196:199]\n" \
  "ds_read_b128 v[136:139], v218 offset:32768\n" \
  "v_mfma_f32_4x4x4_16b_bf16 v[200:203], v[114:115], v[114:115], v[200:203]\n" \
  "ds_read_b128 v[140:143], v219 offset:32768\n" \
  "v_mfma_f32_16x16x32_bf16 v[192:195], v[212:215], v[112:115], v[192:195]\n" \
  "ds_read_b64_tr_b16 v[32:33], v232 offset:0\n" \
  "ds_read_b64_tr_b16 v[34:35], v232 offset:512\n" \
  "v_mfma_f32_4x4x4_16b_bf16 v[196:199], v[120:121], v[120:121], v[196:199]\n" \
  "ds_read_b64_tr_b16 v[36:37], v232 offset:8192\n" \
  "ds_read_b64_tr_b16 v[38:39], v232 offset:8704\n" \
  "v_mfma_f32_4x4x4_16b_bf16 v[200:203], v[122:123], v[122:123], v[200:203]\n" \
  "ds_read_b64_tr_b16 v[40:41], v233 offset:0\n" \
  "ds_read_b64_tr_b16 v[42:43], v233 offset:512\n" \
  "v_mfma_f32_16x16x32_bf16 v[192:195], v[212:215], v[120:123], v[192:195]\n" \
  "ds_read_b64_tr_b16 v[44:45], v233 offset:8192\n" \
  "ds_read_b64_tr_b16 v[46:47], v233 offset:8704\n" \
  "ds_read_b64_tr_b16 v[48:49], v234 offset:0\n" \
  "ds_read_b64_tr_b16 v[50:51], v234 offset:512\n" \
  "ds_read_b64_tr_b16 v[52:53], v234 offset:8192\n" \
  "ds_read_b64_tr_b16 v[54:55], v234 offset:8704\n" \
  "ds_read_b64_tr_b16 v[56:57], v235 offset:0\n" \
  "ds_read_b64_tr_b16 v[58:59], v235 offset:512\n" \
  "ds_read_b64_tr_b16 v[60:61], v235 offset:8192\n" \
  "ds_read_b64_tr_b16 v[62:63], v235 offset:8704\n" \
  "s_waitcnt lgkmcnt(14)\n" \
  "v_mfma_f32_32x32x16_bf16 v[0:15], v[32:35], v[128:131], v[0:15]\n" \
  "s_waitcnt lgkmcnt(12)\n" \
  "v_mfma_f32_32x32x16_bf16 v[16:31], v[128:131], v[36:39], v[16:31]\n" \
  "v_mfma_f32_16x16x32_bf16 v[204:207], v[212:215], v[32:35], v[204:207]\n" \
  "v_mfma_f32_16x16x32_bf16 v[208:211], v[212:215], v[36:39], v[208:211]\n" \
  "s_waitcnt lgkmcnt(10)\n" \
  "v_mfma_f32_32x32x16_bf16 v[0:15], v[40:43], v[132:135], v[0:15]\n" \
  "s_waitcnt lgkmcnt(8)\n" \
  "v_mfma_f32_32x32x16_bf16 v[16:31], v[132:135], v[44:47], v[16:31]\n" \
  "v_mfma_f32_16x16x32_bf16 v[204:207], v[212:215], v[40:43], v[204:207]\n" \
  "v_mfma_f32_16x16x32_bf16 v[208:211], v[212:215], v[44:47], v[208:211]\n" \
  "s_waitcnt lgkmcnt(6)\n" \
  "v_mfma_f32_32x32x16_bf16 v[0:15], v[48:51], v[136:139], v[0:15]\n" \
  "s_waitcnt lgkmcnt(4)\n" \
  "v_mfma_f32_32x32x16_bf16 v[16:31], v[136:139], v[52:55], v[16:31]\n" \
  "s_waitcnt lgkmcnt(2)\n" \
  "v_mfma_f32_32x32x16_bf16 v[0:15], v[56:59], v[140:143], v[0:15]\n" \
  "s_waitcnt lgkmcnt(0)\n" \
  "v_mfma_f32_32x32x16_bf16 v[16:31], v[140:143], v[60:63], v[16:31]\n" \
  "s_barrier\n" \
  "ds_read_b128 v[144:147], v216 offset:40960\n" \
  "ds_read_b128 v[148:151], v217 offset:40960\n" \
  "v_mfma_f32_4x4x4_16b_bf16 v[196:199], v[128:129], v[128:129], v[196:199]\n" \
  "ds_read_b128 v[152:155], v218 offset:40960\n" \
  "v_mfma_f32_4x4x4_16b_bf16 v[200:203], v[130:131], v[130:131], v[200:203]\n" \
  "ds_read_b128 v[156:159], v219 offset:40960\n" \
  "v_mfma_f32_16x16x32_bf16 v[192:195], v[212:215], v[128:131], v[192:195]\n" \
  "ds_read_b64_tr_b16 v[32:33], v232 offset:16384\n" \
  "ds_read_b64_tr_b16 v[34:35], v232 offset:16896\n" \
  "v_mfma_f32_4x4x4_16b_bf16 v[196:199], v[136:137], v[136:137], v[196:199]\n" \
  "ds_read_b64_tr_b16 v[36:37], v232 offset:24576\n" \
  "ds_read_b64_tr_b16 v[38:39], v232 offset:25088\n" \
  "v_mfma_f32_4x4x4_16b_bf16 v[200:203], v[138:139], v[138:139], v[200:203]\n" \
  "ds_read_b64_tr_b16 v[40:41], v233 offset:16384\n" \
  "ds_read_b64_tr_b16 v[42:43], v233 offset:16896\n" \
  "v_mfma_f32_16x16x32_bf16 v[192:195], v[212:215], v[136:139], v[192:195]\n" \
  "ds_read_b64_tr_b16 v[44:45], v233 offset:24576\n" \
  "ds_read_b64_tr_b16 v[46:47], v233 offset:25088\n" \
  "ds_read_b64_tr_b16 v[48:49], v234 offset:16384\n" \
  "ds_read_b64_tr_b16 v[50:51], v234 offset:16896\n" \
  "ds_read_b64_tr_b16 v[52:53], v234 offset:24576\n" \
  "ds_read_b64_tr_b16 v[54:55], v234 offset:25088\n" \
  "ds_read_b64_tr_b16 v[56:57], v235 offset:16384\n" \
  "ds_read_b64_tr_b16 v[58:59], v235 offset:16896\n" \
  "ds_read_b64_tr_b16 v[60:61], v235 offset:24576\n" \
  "ds_read_b64_tr_b16 v[62:63], v235 offset:25088\n" \
  "s_waitcnt lgkmcnt(14)\n" \
  "v_mfma_f32_32x32x16_bf16 v[0:15], v[32:35], v[144:147], v[0:15]\n" \
  "s_waitcnt lgkmcnt(12)\n" \
  "v_mfma_f32_32x32x16_bf16 v[16:31], v[144:147], v[36:39], v[16:31]\n" \
  "v_mfma_f32_16x16x32_bf16 v[204:207], v[212:215], v[32:35], v[204:207]\n" \
  "v_mfma_f32_16x16x32_bf16 v[208:211], v[212:215], v[36:39], v[208:211]\n" \
  "s_waitcnt lgkmcnt(10)\n" \
  "v_mfma_f32_32x32x16_bf16 v[0:15], v[40:43], v[148:151], v[0:15]\n" \
  "s_waitcnt lgkmcnt(8)\n" \
  "v_mfma_f32_32x32x16_bf16 v[16:31], v[148:151], v[44:47], v[16:31]\n" \
  "v_mfma_f32_16x16x32_bf16 v[204:207], v[212:215], v[40:43], v[204:207]\n" \
  "v_mfma_f32_16x16x32_bf16 v[208:211], v[212:215], v[44:47], v[208:211]\n" \
  "v_add_u32 v232, 0xffff0000, v232\n" \
  "v_add_u32 v233, 0xffff0000, v233\n" \
  "v_add_u32 v234, 0xffff0000, v234\n" \
  "v_add_u32 v235, 0xffff0000, v235\n" \
  "s_waitcnt lgkmcnt(6)\n" \
  "v_mfma_f32_32x32x16_bf16 v[0:15], v[48:51], v[152:155], v[0:15]\n" \
  "s_waitcnt lgkmcnt(4)\n" \
  "v_mfma_f32_32x32x16_bf16 v[16:31], v[152:155], v[52:55], v[16:31]\n" \
  "s_waitcnt lgkmcnt(2)\n" \
  "v_mfma_f32_32x32x16_bf16 v[0:15], v[56:59], v[156:159], v[0:15]\n" \
  "s_waitcnt lgkmcnt(0)\n" \
  "v_mfma_f32_32x32x16_bf16 v[16:31], v[156:159], v[60:63], v[16:31]\n" \
  "s_barrier\n" \
  "ds_read_b128 v[160:163], v216 offset:49152\n" \
  "ds_read_b128 v[164:167], v217 offset:49152\n" \
  "v_mfma_f32_4x4x4_16b_bf16 v[196:199], v[144:145], v[144:145], v[196:199]\n" \
  "ds_read_b128 v[168:171], v218 offset:49152\n" \
  "v_mfma_f32_4x4x4_16b_bf16 v[200:203], v[146:147], v[146:147], v[200:203]\n" \
  "ds_read_b128 v[172:175], v219 offset:49152\n" \
  "v_mfma_f32_16x16x32_bf16 v[192:195], v[212:215], v[144:147], v[192:195]\n" \
  "ds_read_b64_tr_b16 v[32:33], v232 offset:0\n" \
  "ds_read_b64_tr_b16 v[34:35], v232 offset:512\n" \
  "v_mfma_f32_4x4x4_16b_bf16 v[196:199], v[152:153], v[152:153], v[196:199]\n" \
  "ds_read_b64_tr_b16 v[36:37], v232 offset:8192\n" \
  "ds_read_b64_tr_b16 v[38:39], v232 offset:8704\n" \
  "v_mfma_f32_4x4x4_16b_bf16 v[200:203], v[154:155], v[154:155], v[200:203]\n" \
  "ds_read_b64_tr_b16 v[40:41], v233 offset:0\n" \
  "ds_read_b64_tr_b16 v[42:43], v233 offset:512\n" \
  "v_mfma_f32_16x16x32_bf16 v[192:195], v[212:215], v[152:155], v[192:195]\n" \
  "ds_read_b64_tr_b16 v[44:45], v233 offset:8192\n" \
  "ds_read_b64_tr_b16 v[46:47], v233 offset:8704\n" \
  "ds_read_b64_tr_b16 v[48:49], v234 offset:0\n" \
  "ds_read_b64_tr_b16 v[50:51], v234 offset:512\n" \
  "ds_read_b64_tr_b16 v[52:53], v234 offset:8192\n" \
  "ds_read_b64_tr_b16 v[54:55], v234 offset:8704\n" \
  "ds_read_b64_tr_b16 v[56:57], v235 offset:0\n" \
  "ds_read_b64_tr_b16 v[58:59], v235 offset:512\n" \
  "ds_read_b64_tr_b16 v[60:61], v235 offset:8192\n" \
  "ds_read_b64_tr_b16 v[62:63], v235 offset:8704\n" \
  "ds_write2st64_b32 v236, v204, v208 offset1:1\n" \
  "s_waitcnt lgkmcnt(15)\n" \
  "v_mfma_f32_32x32x16_bf16 v[0:15], v[32:35], v[160:163], v[0:15]\n" \
  "s_waitcnt lgkmcnt(13)\n" \
  "v_mfma_f32_32x32x16_bf16 v[16:31], v[160:163], v[36:39], v[16:31]\n" \
  "v_mfma_f32_16x16x32_bf16 v[204:207], v[212:215], v[32:35], v[204:207]\n" \
  "v_mfma_f32_16x16x32_bf16 v[208:211], v[212:215], v[36:39], v[208:211]\n" \
  "s_waitcnt lgkmcnt(11)\n" \
  "v_mfma_f32_32x32x16_bf16 v[0:15], v[40:43], v[164:167], v[0:15]\n" \
  "s_waitcnt lgkmcnt(9)\n" \
  "v_mfma_f32_32x32x16_bf16 v[16:31], v[164:167], v[44:47], v[16:31]\n" \
  "v_mfma_f32_16x16x32_bf16 v[204:207], v[212:215], v[40:43], v[204:207]\n" \
  "v_mfma_f32_16x16x32_bf16 v[208:211], v[212:215], v[44:47], v[208:211]\n" \
  "s_mov_b32 s94, 0xaaaaaaaa\n" \
  "s_mov_b32 s95, s94\n" \
  "s_mov_b32 s96, 0xcccccccc\n" \
  "s_mov_b32 s97, s96\n" \
  "v_add_f32 v220, v196, v200\n" \
  "v_add_f32 v221, v197, v201\n" \
  "v_add_f32 v223, v198, v202\n" \
  "v_cndmask_b32_e64 v220, v220, v221, s[94:95]\n" \
  "v_add_f32 v221, v199, v203\n" \
  "v_cndmask_b32_e64 v223, v223, v221, s[94:95]\n" \
  "v_cndmask_b32_e64 v220, v220, v223, s[96:97]\n" \
  "ds_write2st64_b32 v236, v192, v220 offset0:2 offset1:3\n" \
  "v_xor_b32 v223, 0x800, v236\n" \
  "v_xor_b32 v236, 0x400, v236\n" \
  "s_waitcnt lgkmcnt(8)\n" \
  "v_mfma_f32_32x32x16_bf16 v[0:15], v[48:51], v[168:171], v[0:15]\n" \
  "s_waitcnt lgkmcnt(6)\n" \
  "v_mfma_f32_32x32x16_bf16 v[16:31], v[168:171], v[52:55], v[16:31]\n" \
  "v_mfma_f32_16x16x32_bf16 v[204:207], v[212:215], v[48:51], v[204:207]\n" \
  "v_mfma_f32_16x16x32_bf16 v[208:211], v[212:215], v[52:55], v[208:211]\n" \
  "s_waitcnt lgkmcnt(4)\n" \
  "v_mfma_f32_32x32x16_bf16 v[0:15], v[56:59], v[172:175], v[0:15]\n" \
  "s_waitcnt lgkmcnt(2)\n" \
  "v_mfma_f32_32x32x16_bf16 v[16:31], v[172:175], v[60:63], v[16:31]\n" \
  "v_mfma_f32_16x16x32_bf16 v[204:207], v[212:215], v[56:59], v[204:207]\n" \
  "v_mfma_f32_16x16x32_bf16 v[208:211], v[212:215], v[60:63], v[208:211]\n" \
  "s_waitcnt lgkmcnt(0)\n" \
  "s_barrier\n" \
  "ds_read_b128 v[176:179], v216 offset:57344\n" \
  "ds_read_b128 v[180:183], v217 offset:57344\n" \
  "v_mfma_f32_4x4x4_16b_bf16 v[196:199], v[160:161], v[160:161], v[196:199]\n" \
  "ds_read_b128 v[184:187], v218 offset:57344\n" \
  "v_mfma_f32_4x4x4_16b_bf16 v[200:203], v[162:163], v[162:163], v[200:203]\n" \
  "ds_read_b128 v[188:191], v219 offset:57344\n" \
  "v_mfma_f32_16x16x32_bf16 v[192:195], v[212:215], v[160:163], v[192:195]\n" \
  "ds_read2st64_b32 v[220:221], v223 offset0:2 offset1:3\n" \
  "v_mfma_f32_4x4x4_16b_bf16 v[196:199], v[164:165], v[164:165], v[196:199]\n" \
  "ds_read2st64_b32 v[236:237], v236 offset1:1\n" \
  "v_mfma_f32_4x4x4_16b_bf16 v[200:203], v[166:167], v[166:167], v[200:203]\n" \
  "ds_read_b64_tr_b16 v[32:33], v232 offset:16384\n" \
  "ds_read_b64_tr_b16 v[34:35], v232 offset:16896\n" \
  "v_mfma_f32_16x16x32_bf16 v[192:195], v[212:215], v[164:167], v[192:195]\n" \
  "ds_read_b64_tr_b16 v[36:37], v232 offset:24576\n" \
  "ds_read_b64_tr_b16 v[38:39], v232 offset:25088\n" \
  "v_mfma_f32_4x4x4_16b_bf16 v[196:199], v[168:169], v[168:169], v[196:199]\n" \
  "ds_read_b64_tr_b16 v[40:41], v233 offset:16384\n" \
  "ds_read_b64_tr_b16 v[42:43], v233 offset:16896\n" \
  "v_mfma_f32_4x4x4_16b_bf16 v[200:203], v[170:171], v[170:171], v[200:203]\n" \
  "ds_read_b64_tr_b16 v[44:45], v233 offset:24576\n" \
  "ds_read_b64_tr_b16 v[46:47], v233 offset:25088\n" \
  "v_mfma_f32_16x16x32_bf16 v[192:195], v[212:215], v[168:171], v[192:195]\n" \
  "ds_read_b64_tr_b16 v[48:49], v234 offset:16384\n" \
  "ds_read_b64_tr_b16 v[50:51], v234 offset:16896\n" \
  "v_mfma_f32_4x4x4_16b_bf16 v[196:199], v[172:173], v[172:173], v[196:199]\n" \
  "ds_read_b64_tr_b16 v[52:53], v234 offset:24576\n" \
  "ds_read_b64_tr_b16 v[54:55], v234 offset:25088\n" \
  "v_mfma_f32_4x4x4_16b_bf16 v[200:203], v[174:175], v[174:175], v[200:203]\n" \
  "ds_read_b64_tr_b16 v[56:57], v235 offset:16384\n" \
  "ds_read_b64_tr_b16 v[58:59], v235 offset:16896\n" \
  "v_mfma_f32_16x16x32_bf16 v[192:195], v[212:215], v[172:175], v[192:195]\n" \
  "ds_read_b64_tr_b16 v[60:61], v235 offset:24576\n" \
  "ds_read_b64_tr_b16 v[62:63], v235 offset:25088\n" \
  "s_waitcnt lgkmcnt(15)\n" \
  "v_mfma_f32_4x4x4_16b_bf16 v[196:199], v[176:177], v[176:177], v[196:199]\n" \
  "v_mfma_f32_4x4x4_16b_bf16 v[200:203], v[178:179], v[178:179], v[200:203]\n" \
  "v_mfma_f32_16x16x32_bf16 v[192:195], v[212:215], v[176:179], v[192:195]\n" \
  "v_mfma_f32_4x4x4_16b_bf16 v[196:199], v[180:181], v[180:181], v[196:199]\n" \
  "v_mfma_f32_4x4x4_16b_bf16 v[200:203], v[182:183], v[182:183], v[200:203]\n" \
  "v_mfma_f32_16x16x32_bf16 v[192:195], v[212:215], v[180:183], v[192:195]\n" \
  "v_mfma_f32_4x4x4_16b_bf16 v[196:199], v[184:185], v[184:185], v[196:199]\n" \
  "v_mfma_f32_4x4x4_16b_bf16 v[200:203], v[186:187], v[186:187], v[200:203]\n" \
  "v_mfma_f32_16x16x32_bf16 v[192:195], v[212:215], v[184:187], v[192:195]\n" \
  "v_mfma_f32_4x4x4_16b_bf16 v[196:199], v[188:189], v[188:189], v[196:199]\n" \
  "v_mfma_f32_4x4x4_16b_bf16 v[200:203], v[190:191], v[190:191], v[200:203]\n" \
  "v_mfma_f32_16x16x32_bf16 v[192:195], v[212:215], v[188:191], v[192:195]\n" \
  "s_mov_b32 s98, 0\n" \
  "s_mov_b32 s99, -1\n" \
  "s_mov_b32 s93, 0x10000\n" \
  "s_waitcnt lgkmcnt(15)\n" \
  "s_waitcnt lgkmcnt(14)\n" \
  "v_mfma_f32_16x16x32_bf16 v[204:207], v[212:215], v[32:35], v[204:207]\n" \
  "s_waitcnt lgkmcnt(12)\n" \
  "v_mfma_f32_16x16x32_bf16 v[208:211], v[212:215], v[36:39], v[208:211]\n" \
  "s_waitcnt lgkmcnt(10)\n" \
  "v_mfma_f32_16x16x32_bf16 v[204:207], v[212:215], v[40:43], v[204:207]\n" \
  "v_add_f32 v196, v196, v200\n" \
  "v_add_f32 v197, v197, v201\n" \
  "v_add_f32 v198, v198, v202\n" \
  "s_waitcnt lgkmcnt(8)\n" \
  "v_mfma_f32_16x16x32_bf16 v[208:211], v[212:215], v[44:47], v[208:211]\n" \
  "v_add_f32 v199, v199, v203\n" \
  "v_cndmask_b32_e64 v193, v196, v197, s[94:95]\n" \
  "v_cndmask_b32_e64 v194, v198, v199, s[94:95]\n" \
  "s_waitcnt lgkmcnt(6)\n" \
  "v_mfma_f32_16x16x32_bf16 v[204:207], v[212:215], v[48:51], v[204:207]\n" \
  "v_add_f32 v192, v192, v220\n" \
  "v_mul_f32 v192, 0xbb000000, v192\n" \
  "v_cndmask_b32_e64 v193, v193, v194, s[96:97]\n" \
  "s_waitcnt lgkmcnt(4)\n" \
  "v_mfma_f32_16x16x32_bf16 v[208:211], v[212:215], v[52:55], v[208:211]\n" \
  "v_add_f32 v193, v193, v221\n" \
  "v_and_b32 v195, 0xffff0000, v192\n" \
  "v_mov_b32 v194, v193\n" \
  "s_waitcnt lgkmcnt(2)\n" \
  "v_mfma_f32_16x16x32_bf16 v[204:207], v[212:215], v[56:59], v[204:207]\n" \
  "v_sub_f32 v216, v192, v195\n" \
  "v_mov_b32 v217, 0\n" \
  "v_permlane32_swap_b32 v193, v194\n" \
  "s_waitcnt lgkmcnt(0)\n" \
  "v_mfma_f32_16x16x32_bf16 v[208:211], v[212:215], v[60:63], v[208:211]\n" \
  "v_cndmask_b32_e64 v216, v195, v216, s[98:99]\n" \
  "v_mov_b32 v218, 0\n" \
  "v_add_f32 v193, v193, v194\n" \
  "v_mfma_f32_32x32x16_bf16 v[0:15], v[32:35], v[176:179], v[0:15]\n" \
  "v_cvt_pk_bf16_f32 v216, v216, v216\n" \
  "v_mul_f32 v193, 0x3b000000, v193\n" \
  "v_mov_b32 v219, 0\n" \
  "v_fma_f32 v193, -v192, v192, v193\n" \
  "v_mov_b32 v197, 0\n" \
  "v_max_f32 v193, 0, v193\n" \
  "v_mov_b32 v198, 0\n" \
  "v_mfma_f32_32x32x16_bf16 v[16:31], v[176:179], v[36:39], v[16:31]\n" \
  "v_add_f32 v193, 0x3727c5ac, v193\n" \
  "v_mov_b32 v199, 0\n" \
  "v_rsq_f32 v222, v193\n" \
  "v_mov_b32 v201, 0\n" \
  "v_mov_b32 v202, 0\n" \
  "v_mov_b32 v203, 0\n" \
  "ds_write_b32 v230, v222\n" \
  "v_mfma_f32_32x32x16_bf16 v[0:15], v[40:43], v[180:183], v[0:15]\n" \
  "v_add_f32 v204, v204, v236\n" \
  "v_and_b32 v195, 0xffff0000, v204\n" \
  "v_sub_f32 v196, v204, v195\n" \
  "v_cvt_pk_bf16_f32 v196, v195, v196\n" \
  "v_add_f32 v208, v208, v237\n" \
  "v_and_b32 v195, 0xffff0000, v208\n" \
  "v_sub_f32 v200, v208, v195\n" \
  "v_cvt_pk_bf16_f32 v200, v195, v200\n" \
  "v_mfma_f32_32x32x16_bf16 v[16:31], v[180:183], v[44:47], v[16:31]\n" \
  "ds_read_b128 v[32:35], v231 offset:0\n" \
  "ds_read_b128 v[36:39], v231 offset:32\n" \
  "ds_read_b128 v[40:43], v231 offset:64\n" \
  "ds_read_b128 v[44:47], v231 offset:96\n" \
  "v_mfma_f32_32x32x16_bf16 v[0:15], v[196:199], v[216:219], v[0:15]\n" \
  "v_mfma_f32_32x32x16_bf16 v[0:15], v[48:51], v[184:187], v[0:15]\n" \
  "v_mfma_f32_32x32x16_bf16 v[0:15], v[56:59], v[188:191], v[0:15]\n" \
  "v_mfma_f32_32x32x16_bf16 v[16:31], v[216:219], v[200:203], v[16:31]\n" \
  "v_mfma_f32_32x32x16_bf16 v[16:31], v[184:187], v[52:55], v[16:31]\n" \
  "v_mul_f32 v0, v0, v222\n" \
  "v_mul_f32 v1, v1, v222\n" \
  "v_mul_f32 v2, v2, v222\n" \
  "v_mul_f32 v3, v3, v222\n" \
  "v_mul_f32 v4, v4, v222\n" \
  "v_mul_f32 v5, v5, v222\n" \
  "v_mul_f32 v6, v6, v222\n" \
  "v_mfma_f32_32x32x16_bf16 v[16:31], v[188:191], v[60:63], v[16:31]\n" \
  "v_mul_f32 v7, v7, v222\n" \
  "v_cvt_pk_bf16_f32 v196, v0, v1\n" \
  "v_cvt_pk_bf16_f32 v197, v2, v3\n" \
  "v_cvt_pk_bf16_f32 v198, v4, v5\n" \
  "v_cvt_pk_bf16_f32 v199, v6, v7\n" \
  "buffer_store_dwordx4 v[196:199], v228, s[84:87], 0 offen sc1\n" \
  "v_mul_f32 v8, v8, v222\n" \
  "v_mul_f32 v9, v9, v222\n" \
  "v_mul_f32 v10, v10, v222\n" \
  "v_mul_f32 v11, v11, v222\n" \
  "v_mul_f32 v12, v12, v222\n" \
  "v_mul_f32 v13, v13, v222\n" \
  "v_mul_f32 v14, v14, v222\n" \
  "v_mul_f32 v15, v15, v222\n" \
  "v_cvt_pk_bf16_f32 v200, v8, v9\n" \
  "v_cvt_pk_bf16_f32 v201, v10, v11\n" \
  "v_cvt_pk_bf16_f32 v202, v12, v13\n" \
  "v_cvt_pk_bf16_f32 v203, v14, v15\n" \
  "buffer_store_dwordx4 v[200:203], v228, s[84:87], s93 offen sc1\n" \
  "s_waitcnt lgkmcnt(0)\n" \
  "v_mul_f32 v16, v16, v32\n" \
  "v_mul_f32 v17, v17, v33\n" \
  "v_mul_f32 v18, v18, v34\n" \
  "v_mul_f32 v19, v19, v35\n" \
  "v_mul_f32 v20, v20, v36\n" \
  "v_mul_f32 v21, v21, v37\n" \
  "v_mul_f32 v22, v22, v38\n" \
  "v_mul_f32 v23, v23, v39\n" \
  "v_cvt_pk_bf16_f32 v48, v16, v17\n" \
  "v_cvt_pk_bf16_f32 v49, v18, v19\n" \
  "v_cvt_pk_bf16_f32 v50, v20, v21\n" \
  "v_cvt_pk_bf16_f32 v51, v22, v23\n" \
  "buffer_store_dwordx4 v[48:51], v229, s[84:87], 0 offen sc1\n" \
  "v_mul_f32 v24, v24, v40\n" \
  "v_mul_f32 v25, v25, v41\n" \
  "v_mul_f32 v26, v26, v42\n" \
  "v_mul_f32 v27, v27, v43\n" \
  "v_mul_f32 v28, v28, v44\n" \
  "v_mul_f32 v29, v29, v45\n" \
  "v_mul_f32 v30, v30, v46\n" \
  "v_mul_f32 v31, v31, v47\n" \
  "v_cvt_pk_bf16_f32 v52, v24, v25\n" \
  "v_cvt_pk_bf16_f32 v53, v26, v27\n" \
  "v_cvt_pk_bf16_f32 v54, v28, v29\n" \
  "v_cvt_pk_bf16_f32 v55, v30, v31\n" \
  "buffer_store_dwordx4 v[52:55], v229, s[84:87], 0 offen offset:512 sc1\n" \
  "v_add_u32 v232, 0xffff0000, v232\n" \
  "v_add_u32 v233, 0xffff0000, v233\n" \
  "v_add_u32 v234, 0xffff0000, v234\n" \
  "v_add_u32 v235, 0xffff0000, v235\n" \
  "s_nop 1\n"
#define Q0_ASM \
  "ds_read_b64_tr_b16 v[32:33], v232 offset:0\n" \
  "ds_read_b64_tr_b16 v[34:35], v232 offset:512\n" \
  "ds_read_b64_tr_b16 v[36:37], v233 offset:0\n" \
  "ds_read_b64_tr_b16 v[38:39], v233 offset:512\n" \
  "ds_read_b64_tr_b16 v[40:41], v234 offset:0\n" \
  "ds_read_b64_tr_b16 v[42:43], v234 offset:512\n" \
  "ds_read_b64_tr_b16 v[44:45], v235 offset:0\n" \
  "ds_read_b64_tr_b16 v[46:47], v235 offset:512\n" \
  "ds_read_b64_tr_b16 v[48:49], v232 offset:8192\n" \
  "ds_read_b64_tr_b16 v[50:51], v232 offset:8704\n" \
  "ds_read_b64_tr_b16 v[52:53], v233 offset:8192\n" \
  "ds_read_b64_tr_b16 v[54:55], v233 offset:8704\n" \
  "ds_read_b64_tr_b16 v[56:57], v234 offset:8192\n" \
  "ds_read_b64_tr_b16 v[58:59], v234 offset:8704\n" \
  "ds_read_b64_tr_b16 v[60:61], v235 offset:8192\n" \
  "ds_read_b64_tr_b16 v[62:63], v235 offset:8704\n" \
  "s_waitcnt lgkmcnt(14)\n" \
  "v_mfma_f32_32x32x16_bf16 v[0:15], v[32:35], v[64:67], 0\n" \
  "v_mfma_f32_16x16x32_bf16 v[204:207], v[212:215], v[32:35], 0\n" \
  "ds_read_b64_tr_b16 v[32:33], v232 offset:16384\n" \
  "ds_read_b64_tr_b16 v[34:35], v232 offset:16896\n" \
  "s_waitcnt lgkmcnt(14)\n" \
  "v_mfma_f32_32x32x16_bf16 v[0:15], v[36:39], v[68:71], v[0:15]\n" \
  "v_mfma_f32_16x16x32_bf16 v[204:207], v[212:215], v[36:39], v[204:207]\n" \
  "ds_read_b64_tr_b16 v[36:37], v233 offset:16384\n" \
  "ds_read_b64_tr_b16 v[38:39], v233 offset:16896\n" \
  "s_waitcnt lgkmcnt(14)\n" \
  "v_mfma_f32_32x32x16_bf16 v[0:15], v[40:43], v[72:75], v[0:15]\n" \
  "v_mfma_f32_16x16x32_bf16 v[204:207], v[212:215], v[40:43], v[204:207]\n" \
  "ds_read_b64_tr_b16 v[40:41], v234 offset:16384\n" \
  "ds_read_b64_tr_b16 v[42:43], v234 offset:16896\n" \
  "s_waitcnt lgkmcnt(14)\n" \
  "v_mfma_f32_32x32x16_bf16 v[0:15], v[44:47], v[76:79], v[0:15]\n" \
  "v_mfma_f32_16x16x32_bf16 v[204:207], v[212:215], v[44:47], v[204:207]\n" \
  "ds_read_b64_tr_b16 v[44:45], v235 offset:16384\n" \
  "ds_read_b64_tr_b16 v[46:47], v235 offset:16896\n" \
  "s_waitcnt lgkmcnt(14)\n" \
  "v_mfma_f32_32x32x16_bf16 v[0:15], v[48:51], v[80:83], v[0:15]\n" \
  "v_mfma_f32_16x16x32_bf16 v[204:207], v[212:215], v[48:51], v[204:207]\n" \
  "v_add_u32 v232, 0x10000, v232\n" \
  "ds_read_b64_tr_b16 v[48:49], v232 offset:32768\n" \
  "ds_read_b64_tr_b16 v[50:51], v232 offset:33280\n" \
  "s_waitcnt lgkmcnt(14)\n" \
  "v_mfma_f32_32x32x16_bf16 v[0:15], v[52:55], v[84:87], v[0:15]\n" \
  "v_mfma_f32_16x16x32_bf16 v[204:207], v[212:215], v[52:55], v[204:207]\n" \
  "v_add_u32 v233, 0x10000, v233\n" \
  "ds_read_b64_tr_b16 v[52:53], v233 offset:32768\n" \
  "ds_read_b64_tr_b16 v[54:55], v233 offset:33280\n" \
  "s_waitcnt lgkmcnt(14)\n" \
  "v_mfma_f32_32x32x16_bf16 v[0:15], v[56:59], v[88:91], v[0:15]\n" \
  "v_mfma_f32_16x16x32_bf16 v[204:207], v[212:215], v[56:59], v[204:207]\n" \
  "v_add_u32 v234, 0x10000, v234\n" \
  "ds_read_b64_tr_b16 v[56:57], v234 offset:32768\n" \
  "ds_read_b64_tr_b16 v[58:59], v234 offset:33280\n" \
  "s_waitcnt lgkmcnt(14)\n" \
  "v_mfma_f32_32x32x16_bf16 v[0:15], v[60:63], v[92:95], v[0:15]\n" \
  "v_mfma_f32_16x16x32_bf16 v[204:207], v[212:215], v[60:63], v[204:207]\n" \
  "v_add_u32 v235, 0x10000, v235\n" \
  "ds_read_b64_tr_b16 v[60:61], v235 offset:32768\n" \
  "ds_read_b64_tr_b16 v[62:63], v235 offset:33280\n" \
  "s_waitcnt lgkmcnt(14)\n" \
  "v_mfma_f32_32x32x16_bf16 v[0:15], v[32:35], v[96:99], v[0:15]\n" \
  "v_mfma_f32_16x16x32_bf16 v[204:207], v[212:215], v[32:35], v[204:207]\n" \
  "s_waitcnt lgkmcnt(12)\n" \
  "v_mfma_f32_32x32x16_bf16 v[0:15], v[36:39], v[100:103], v[0:15]\n" \
  "v_mfma_f32_16x16x32_bf16 v[204:207], v[212:215], v[36:39], v[204:207]\n" \
  "s_waitcnt lgkmcnt(10)\n" \
  "v_mfma_f32_32x32x16_bf16 v[0:15], v[40:43], v[104:107], v[0:15]\n" \
  "v_mfma_f32_16x16x32_bf16 v[204:207], v[212:215], v[40:43], v[204:207]\n" \
  "s_waitcnt lgkmcnt(8)\n" \
  "v_mfma_f32_32x32x16_bf16 v[0:15], v[44:47], v[108:111], v[0:15]\n" \
  "v_mfma_f32_16x16x32_bf16 v[204:207], v[212:215], v[44:47], v[204:207]\n" \
  "s_waitcnt lgkmcnt(6)\n" \
  "v_mfma_f32_32x32x16_bf16 v[0:15], v[48:51], v[112:115], v[0:15]\n" \
  "v_mfma_f32_16x16x32_bf16 v[204:207], v[212:215], v[48:51], v[204:207]\n" \
  "s_waitcnt lgkmcnt(4)\n" \
  "v_mfma_f32_32x32x16_bf16 v[0:15], v[52:55], v[116:119], v[0:15]\n" \
  "v_mfma_f32_16x16x32_bf16 v[204:207], v[212:215], v[52:55], v[204:207]\n" \
  "s_waitcnt lgkmcnt(2)\n" \
  "v_mfma_f32_32x32x16_bf16 v[0:15], v[56:59], v[120:123], v[0:15]\n" \
  "v_mfma_f32_16x16x32_bf16 v[204:207], v[212:215], v[56:59], v[204:207]\n" \
  "s_waitcnt lgkmcnt(0)\n" \
  "v_mfma_f32_32x32x16_bf16 v[0:15], v[60:63], v[124:127], v[0:15]\n" \
  "v_mfma_f32_16x16x32_bf16 v[204:207], v[212:215], v[60:63], v[204:207]\n"
#define Q1_ASM \
  "ds_read_b64_tr_b16 v[32:33], v232 offset:40960\n" \
  "ds_read_b64_tr_b16 v[34:35], v232 offset:41472\n" \
  "ds_read_b64_tr_b16 v[36:37], v233 offset:40960\n" \
  "ds_read_b64_tr_b16 v[38:39], v233 offset:41472\n" \
  "ds_read_b64_tr_b16 v[40:41], v234 offset:40960\n" \
  "ds_read_b64_tr_b16 v[42:43], v234 offset:41472\n" \
  "ds_read_b64_tr_b16 v[44:45], v235 offset:40960\n" \
  "ds_read_b64_tr_b16 v[46:47], v235 offset:41472\n" \
  "v_add_u32 v232, 0xffff0000, v232\n" \
  "ds_read_b64_tr_b16 v[48:49], v232 offset:24576\n" \
  "ds_read_b64_tr_b16 v[50:51], v232 offset:25088\n" \
  "v_add_u32 v233, 0xffff0000, v233\n" \
  "ds_read_b64_tr_b16 v[52:53], v233 offset:24576\n" \
  "ds_read_b64_tr_b16 v[54:55], v233 offset:25088\n" \
  "v_add_u32 v234, 0xffff0000, v234\n" \
  "ds_read_b64_tr_b16 v[56:57], v234 offset:24576\n" \
  "ds_read_b64_tr_b16 v[58:59], v234 offset:25088\n" \
  "v_add_u32 v235, 0xffff0000, v235\n" \
  "ds_read_b64_tr_b16 v[60:61], v235 offset:24576\n" \
  "ds_read_b64_tr_b16 v[62:63], v235 offset:25088\n" \
  "s_waitcnt lgkmcnt(14)\n" \
  "v_mfma_f32_32x32x16_bf16 v[0:15], v[32:35], v[128:131], v[0:15]\n" \
  "v_mfma_f32_16x16x32_bf16 v[204:207], v[212:215], v[32:35], v[204:207]\n" \
  "v_add_u32 v232, 0x10000, v232\n" \
  "ds_read_b64_tr_b16 v[32:33], v232 offset:49152\n" \
  "ds_read_b64_tr_b16 v[34:35], v232 offset:49664\n" \
  "s_waitcnt lgkmcnt(14)\n" \
  "v_mfma_f32_32x32x16_bf16 v[0:15], v[36:39], v[132:135], v[0:15]\n" \
  "v_mfma_f32_16x16x32_bf16 v[204:207], v[212:215], v[36:39], v[204:207]\n" \
  "v_add_u32 v233, 0x10000, v233\n" \
  "ds_read_b64_tr_b16 v[36:37], v233 offset:49152\n" \
  "ds_read_b64_tr_b16 v[38:39], v233 offset:49664\n" \
  "s_waitcnt lgkmcnt(14)\n" \
  "v_mfma_f32_32x32x16_bf16 v[0:15], v[40:43], v[136:139], v[0:15]\n" \
  "v_mfma_f32_16x16x32_bf16 v[204:207], v[212:215], v[40:43], v[204:207]\n" \
  "v_add_u32 v234, 0x10000, v234\n" \
  "ds_read_b64_tr_b16 v[40:41], v234 offset:49152\n" \
  "ds_read_b64_tr_b16 v[42:43], v234 offset:49664\n" \
  "s_waitcnt lgkmcnt(14)\n" \
  "v_mfma_f32_32x32x16_bf16 v[0:15], v[44:47], v[140:143], v[0:15]\n" \
  "v_mfma_f32_16x16x32_bf16 v[204:207], v[212:215], v[44:47], v[204:207]\n" \
  "v_add_u32 v235, 0x10000, v235\n" \
  "ds_read_b64_tr_b16 v[44:45], v235 offset:49152\n" \
  "ds_read_b64_tr_b16 v[46:47], v235 offset:49664\n" \
  "s_waitcnt lgkmcnt(14)\n" \
  "v_mfma_f32_32x32x16_bf16 v[0:15], v[48:51], v[144:147], v[0:15]\n" \
  "v_mfma_f32_16x16x32_bf16 v[204:207], v[212:215], v[48:51], v[204:207]\n" \
  "ds_read_b64_tr_b16 v[48:49], v232 offset:57344\n" \
  "ds_read_b64_tr_b16 v[50:51], v232 offset:57856\n" \
  "s_waitcnt lgkmcnt(14)\n" \
  "v_mfma_f32_32x32x16_bf16 v[0:15], v[52:55], v[148:151], v[0:15]\n" \
  "v_mfma_f32_16x16x32_bf16 v[204:207], v[212:215], v[52:55], v[204:207]\n" \
  "ds_read_b64_tr_b16 v[52:53], v233 offset:57344\n" \
  "ds_read_b64_tr_b16 v[54:55], v233 offset:57856\n" \
  "s_waitcnt lgkmcnt(14)\n" \
  "v_mfma_f32_32x32x16_bf16 v[0:15], v[56:59], v[152:155], v[0:15]\n" \
  "v_mfma_f32_16x16x32_bf16 v[204:207], v[212:215], v[56:59], v[204:207]\n" \
  "ds_read_b64_tr_b16 v[56:57], v234 offset:57344\n" \
  "ds_read_b64_tr_b16 v[58:59], v234 offset:57856\n" \
  "s_waitcnt lgkmcnt(14)\n" \
  "v_mfma_f32_32x32x16_bf16 v[0:15], v[60:63], v[156:159], v[0:15]\n" \
  "v_mfma_f32_16x16x32_bf16 v[204:207], v[212:215], v[60:63], v[204:207]\n" \
  "ds_read_b64_tr_b16 v[60:61], v235 offset:57344\n" \
  "ds_read_b64_tr_b16 v[62:63], v235 offset:57856\n" \
  "s_waitcnt lgkmcnt(14)\n" \
  "v_mfma_f32_32x32x16_bf16 v[0:15], v[32:35], v[160:163], v[0:15]\n" \
  "v_mfma_f32_16x16x32_bf16 v[204:207], v[212:215], v[32:35], v[204:207]\n" \
  "s_waitcnt lgkmcnt(12)\n" \
  "v_mfma_f32_32x32x16_bf16 v[0:15], v[36:39], v[164:167], v[0:15]\n" \
  "v_mfma_f32_16x16x32_bf16 v[204:207], v[212:215], v[36:39], v[204:207]\n" \
  "s_waitcnt lgkmcnt(10)\n" \
  "v_mfma_f32_32x32x16_bf16 v[0:15], v[40:43], v[168:171], v[0:15]\n" \
  "v_mfma_f32_16x16x32_bf16 v[204:207], v[212:215], v[40:43], v[204:207]\n" \
  "s_waitcnt lgkmcnt(8)\n" \
  "v_mfma_f32_32x32x16_bf16 v[0:15], v[44:47], v[172:175], v[0:15]\n" \
  "v_mfma_f32_16x16x32_bf16 v[204:207], v[212:215], v[44:47], v[204:207]\n" \
  "s_waitcnt lgkmcnt(6)\n" \
  "v_mfma_f32_32x32x16_bf16 v[0:15], v[48:51], v[176:179], v[0:15]\n" \
  "v_mfma_f32_16x16x32_bf16 v[204:207], v[212:215], v[48:51], v[204:207]\n" \
  "s_waitcnt lgkmcnt(4)\n" \
  "v_mfma_f32_32x32x16_bf16 v[0:15], v[52:55], v[180:183], v[0:15]\n" \
  "v_mfma_f32_16x16x32_bf16 v[204:207], v[212:215], v[52:55], v[204:207]\n" \
  "s_waitcnt lgkmcnt(2)\n" \
  "v_mfma_f32_32x32x16_bf16 v[0:15], v[56:59], v[184:187], v[0:15]\n" \
  "v_mfma_f32_16x16x32_bf16 v[204:207], v[212:215], v[56:59], v[204:207]\n" \
  "s_waitcnt lgkmcnt(0)\n" \
  "v_mfma_f32_32x32x16_bf16 v[0:15], v[60:63], v[188:191], v[0:15]\n" \
  "v_mfma_f32_16x16x32_bf16 v[204:207], v[212:215], v[60:63], v[204:207]\n" \
  "s_nop 7\n"
#define QS 0
#define KVE 2
#define HX 3
#define QF 0
#define OTA 1
#define NC 1
#define ATT_ASM \
  "s_mov_b32 s88, 0x3f803f80\n" \
  "s_mov_b32 s89, s88\n" \
  "s_mov_b32 s92, 200000\n" \
  "v_and_b32 v32, 432, v248\n" \
  "v_cmp_eq_u32 vcc, 256, v32\n" \
  "v_mov_b32 v33, s88\n" \
  "s_nop 0\n" \
  "v_cndmask_b32 v32, 0, v33, vcc\n" \
  "v_mov_b32 v33, v32\n" \
  "v_mov_b64 v[68:69], v[32:33]\n" \
  "v_mov_b64 v[70:71], v[32:33]\n" \
  "v_mov_b64 v[72:73], v[32:33]\n" \
  "v_mov_b64 v[74:75], v[32:33]\n" \
  "v_mov_b64 v[80:81], v[32:33]\n" \
  "v_mov_b64 v[82:83], v[32:33]\n" \
  "v_mov_b64 v[84:85], v[32:33]\n" \
  "v_mov_b64 v[86:87], v[32:33]\n" \
  "s_mov_b32 s82, 0xffff\n" \
  "s_mov_b32 s83, s82\n" \
  "s_mov_b32 s80, s71\n" \
  "v_add_u32 v244, s79, v248\n" \
  "s_cmp_lt_u32 s56, 4\n" \
  "s_cbranch_scc1 .La_c\n" \
  "v_mov_b64 v[92:93], v[32:33]\n" \
  "v_mov_b64 v[94:95], v[32:33]\n" \
  "v_mov_b64 v[96:97], v[32:33]\n" \
  "v_mov_b64 v[98:99], v[32:33]\n" \
  "v_mov_b64 v[104:105], v[32:33]\n" \
  "v_mov_b64 v[106:107], v[32:33]\n" \
  "v_mov_b64 v[108:109], v[32:33]\n" \
  "v_mov_b64 v[110:111], v[32:33]\n" \
  "v_mov_b64 v[116:117], v[32:33]\n" \
  "v_mov_b64 v[118:119], v[32:33]\n" \
  "v_mov_b64 v[120:121], v[32:33]\n" \
  "v_mov_b64 v[122:123], v[32:33]\n" \
  "v_mov_b64 v[128:129], v[32:33]\n" \
  "v_mov_b64 v[130:131], v[32:33]\n" \
  "v_mov_b64 v[132:133], v[32:33]\n" \
  "v_mov_b64 v[134:135], v[32:33]\n" \
  "v_mov_b64 v[140:141], v[32:33]\n" \
  "v_mov_b64 v[142:143], v[32:33]\n" \
  "v_mov_b64 v[144:145], v[32:33]\n" \
  "v_mov_b64 v[146:147], v[32:33]\n" \
  "v_mov_b64 v[152:153], v[32:33]\n" \
  "v_mov_b64 v[154:155], v[32:33]\n" \
  "v_mov_b64 v[156:157], v[32:33]\n" \
  "v_mov_b64 v[158:159], v[32:33]\n" \
  "v_mov_b64 v[0:1], 0\n" \
  "v_mov_b64 v[2:3], 0\n" \
  "v_mov_b64 v[4:5], 0\n" \
  "v_mov_b64 v[6:7], 0\n" \
  "v_mov_b64 v[8:9], 0\n" \
  "v_mov_b64 v[10:11], 0\n" \
  "v_mov_b64 v[12:13], 0\n" \
  "v_mov_b64 v[14:15], 0\n" \
  "v_mov_b64 v[16:17], 0\n" \
  "v_mov_b64 v[18:19], 0\n" \
  "v_mov_b64 v[20:21], 0\n" \
  "v_mov_b64 v[22:23], 0\n" \
  "v_mov_b64 v[24:25], 0\n" \
  "v_mov_b64 v[26:27], 0\n" \
  "v_mov_b64 v[28:29], 0\n" \
  "v_mov_b64 v[30:31], 0\n" \
  "v_mov_b64 v[56:57], 0\n" \
  "v_mov_b64 v[58:59], 0\n" \
  "v_mov_b64 v[60:61], 0\n" \
  "v_mov_b64 v[62:63], 0\n" \
  "v_mov_b64 v[236:237], -1\n" \
  "v_mov_b64 v[238:239], -1\n" \
  "v_mov_b32 v242, 0xdddd\n" \
  "v_mov_b64 v[246:247], s[68:69]\n" \
  "v_and_b32 v243, 16, v248\n" \
  "v_cmp_eq_u32 vcc, 0, v243\n" \
  "v_lshrrev_b32 v245, 2, v248\n" \
  "v_lshrrev_b32 v206, 1, v248\n" \
  "v_and_b32 v207, 0x180, v248\n" \
  "v_and_b32 v206, 56, v206\n" \
  "v_add3_u32 v206, v206, v207, s72\n" \
  "s_sub_u32 s88, s100, s72\n" \
  "s_mov_b32 exec_lo, 0\n" \
  "v_add_u32 v206, s88, v206\n" \
  "s_mov_b32 exec_lo, -1\n" \
  "s_lshl_b32 s91, s56, 13\n" \
  "v_cndmask_b32 v243, v247, v246, vcc\n" \
  "s_add_u32 s96, s71, 16384\n" \
  "s_lshr_b32 s96, s96, 14\n" \
  "s_lshl_b32 s96, s96, 3\n" \
  "s_lshl_b32 s96, 0xff, s96\n" \
  "s_mov_b32 s97, s96\n" \
  "s_bitcmp1_b32 s78, 5\n" \
  "s_cbranch_scc1 .La_ns\n" \
  "s_sleep 12\n" \
  ".La_ns:\n" \
  "s_mov_b64 exec, s[96:97]\n" \
  ".La_p:\n" \
  "buffer_load_dwordx2 v[232:233], v206, s[84:87], 0 offen sc1 ; RF\n" \
  "s_waitcnt vmcnt(0) ; RF\n" \
  "v_cmp_ne_u64 vcc, s[68:69], v[232:233] ; RF\n" \
  "s_and_b64 s[88:89], vcc, exec\n" \
  "s_cbranch_scc0 .La_go\n" \
  "s_sleep 2\n" \
  "s_sub_u32 s92, s92, 1\n" \
  "s_cbranch_scc0 .La_p\n" \
  ".La_go:\n" \
  "s_mov_b64 exec, -1\n" \
  "s_add_u32 s80, s80, 16384\n" \
  "s_and_b32 s77, s80, 0xfc00\n" \
  "s_or_b32 s77, s77, s70\n" \
  "buffer_load_dwordx4 v[64:67], v240, s[84:87], s77 offen offset:0 sc1\n" \
  "buffer_load_dwordx4 v[76:79], v240, s[84:87], s77 offen offset:2048 sc1\n" \
  "s_mov_b64 exec, s[82:83]\n" \
  "buffer_load_dwordx4 v[68:71], v241, s[84:87], s77 offen offset:0 sc1\n" \
  "buffer_load_dwordx4 v[72:75], v241, s[84:87], s77 offen offset:512 sc1\n" \
  "buffer_load_dwordx4 v[80:83], v241, s[84:87], s77 offen offset:2048 sc1\n" \
  "buffer_load_dwordx4 v[84:87], v241, s[84:87], s77 offen offset:2560 sc1\n" \
  "s_mov_b64 exec, -1\n" \
  "s_barrier\n" \
  "ds_read_b128 v[228:231], v244 offset:0\n" \
  "ds_read_b128 v[232:235], v244 offset:2048\n" \
  "s_branch .La_j\n" \
  ".La_c: ; RESET\n" \
  "s_waitcnt lgkmcnt(0)\n" \
  "s_barrier\n" \
  "s_add_u32 s80, s80, 16384\n" \
  "s_and_b32 s77, s80, 0xfc00\n" \
  "s_or_b32 s77, s77, s70\n" \
  "buffer_load_dwordx4 v[64:67], v240, s[84:87], s77 offen offset:0 sc1\n" \
  "buffer_load_dwordx4 v[76:79], v240, s[84:87], s77 offen offset:2048 sc1\n" \
  "s_mov_b64 exec, s[82:83]\n" \
  "buffer_load_dwordx4 v[68:71], v241, s[84:87], s77 offen offset:0 sc1\n" \
  "buffer_load_dwordx4 v[72:75], v241, s[84:87], s77 offen offset:512 sc1\n" \
  "buffer_load_dwordx4 v[80:83], v241, s[84:87], s77 offen offset:2048 sc1\n" \
  "buffer_load_dwordx4 v[84:87], v241, s[84:87], s77 offen offset:2560 sc1\n" \
  "s_mov_b64 exec, -1\n" \
  "ds_read_b64_tr_b16 v[168:169], v165 offset:0\n" \
  "ds_read_b64_tr_b16 v[170:171], v165 offset:512\n" \
  "ds_read_b128 v[228:231], v244 offset:0\n" \
  "ds_read_b128 v[232:235], v244 offset:2048\n" \
  "v_mov_b64 v[92:93], v[32:33]\n" \
  "v_mov_b64 v[94:95], v[32:33]\n" \
  "v_mov_b64 v[96:97], v[32:33]\n" \
  "v_mov_b64 v[98:99], v[32:33]\n" \
  "v_mov_b64 v[104:105], v[32:33]\n" \
  "v_mov_b64 v[106:107], v[32:33]\n" \
  "v_mov_b64 v[108:109], v[32:33]\n" \
  "v_mov_b64 v[110:111], v[32:33]\n" \
  "v_mov_b64 v[116:117], v[32:33]\n" \
  "v_mov_b64 v[118:119], v[32:33]\n" \
  "v_mov_b64 v[120:121], v[32:33]\n" \
  "v_mov_b64 v[122:123], v[32:33]\n" \
  "v_mov_b64 v[128:129], v[32:33]\n" \
  "v_mov_b64 v[130:131], v[32:33]\n" \
  "v_mov_b64 v[132:133], v[32:33]\n" \
  "v_mov_b64 v[134:135], v[32:33]\n" \
  "v_mov_b64 v[140:141], v[32:33]\n" \
  "v_mov_b64 v[142:143], v[32:33]\n" \
  "v_mov_b64 v[144:145], v[32:33]\n" \
  "v_mov_b64 v[146:147], v[32:33]\n" \
  "v_mov_b64 v[152:153], v[32:33]\n" \
  "v_mov_b64 v[154:155], v[32:33]\n" \
  "v_mov_b64 v[156:157], v[32:33]\n" \
  "v_mov_b64 v[158:159], v[32:33]\n" \
  "v_mov_b64 v[0:1], 0\n" \
  "v_mov_b64 v[2:3], 0\n" \
  "v_mov_b64 v[4:5], 0\n" \
  "v_mov_b64 v[6:7], 0\n" \
  "v_mov_b64 v[8:9], 0\n" \
  "v_mov_b64 v[10:11], 0\n" \
  "v_mov_b64 v[12:13], 0\n" \
  "v_mov_b64 v[14:15], 0\n" \
  "v_mov_b64 v[16:17], 0\n" \
  "v_mov_b64 v[18:19], 0\n" \
  "v_mov_b64 v[20:21], 0\n" \
  "v_mov_b64 v[22:23], 0\n" \
  "v_mov_b64 v[24:25], 0\n" \
  "v_mov_b64 v[26:27], 0\n" \
  "v_mov_b64 v[28:29], 0\n" \
  "v_mov_b64 v[30:31], 0\n" \
  "v_mov_b64 v[56:57], 0\n" \
  "v_mov_b64 v[58:59], 0\n" \
  "v_mov_b64 v[60:61], 0\n" \
  "v_mov_b64 v[62:63], 0\n" \
  "v_mov_b64 v[236:237], -1\n" \
  "v_mov_b64 v[238:239], -1\n" \
  "v_mov_b32 v242, 0xdddd\n" \
  "v_mov_b64 v[246:247], s[68:69]\n" \
  "v_and_b32 v243, 16, v248\n" \
  "v_cmp_eq_u32 vcc, 0, v243\n" \
  "v_lshrrev_b32 v245, 2, v248\n" \
  "v_lshrrev_b32 v206, 1, v248\n" \
  "v_and_b32 v207, 0x180, v248\n" \
  "v_and_b32 v206, 56, v206\n" \
  "v_add3_u32 v206, v206, v207, s72\n" \
  "s_sub_u32 s88, s100, s72\n" \
  "s_mov_b32 exec_lo, 0\n" \
  "v_add_u32 v206, s88, v206\n" \
  "s_mov_b32 exec_lo, -1\n" \
  "s_lshl_b32 s91, s56, 13\n" \
  "v_cndmask_b32 v243, v247, v246, vcc\n" \
  ".La_j:\n" \
  "s_waitcnt vmcnt(5) lgkmcnt(0)\n" \
  "v_mfma_f32_32x32x16_bf16 v[32:47], v[64:67], v[228:231], 0\n" \
  "s_add_u32 s89, s73, 4096\n" \
  "s_sub_u32 s88, s74, 3072\n" \
  "s_min_u32 s74, s88, s89\n" \
  "s_nop 8\n" \
  "s_setprio 3\n" \
  "v_exp_f32 v32, v32\n" \
  "v_exp_f32 v33, v33\n" \
  "v_smfmac_f32_32x32x32_bf16 v[16:31], v[156:159], v[56:63], v242\n" \
  "v_exp_f32 v34, v34\n" \
  "v_exp_f32 v35, v35\n" \
  "v_exp_f32 v36, v36\n" \
  "v_mfma_f32_32x32x16_bf16 v[48:63], v[64:67], v[232:235], 0\n" \
  "s_add_u32 s77, s77, 4096\n" \
  "buffer_load_dwordx4 v[88:91], v240, s[84:87], s77 offen offset:0 sc1\n" \
  "buffer_load_dwordx4 v[100:103], v240, s[84:87], s77 offen offset:2048 sc1\n" \
  "s_mov_b64 exec, s[82:83]\n" \
  "buffer_load_dwordx4 v[92:95], v241, s[84:87], s77 offen offset:0 sc1\n" \
  "buffer_load_dwordx4 v[96:99], v241, s[84:87], s77 offen offset:512 sc1\n" \
  "buffer_load_dwordx4 v[104:107], v241, s[84:87], s77 offen offset:2048 sc1\n" \
  "buffer_load_dwordx4 v[108:111], v241, s[84:87], s77 offen offset:2560 sc1\n" \
  "s_mov_b64 exec, -1\n" \
  "v_exp_f32 v37, v37\n" \
  "v_exp_f32 v38, v38\n" \
  "v_exp_f32 v39, v39\n" \
  "v_exp_f32 v40, v40\n" \
  "v_exp_f32 v41, v41\n" \
  "s_waitcnt vmcnt(8)\n" \
  "v_smfmac_f32_32x32x32_bf16 v[0:15], v[68:71], v[32:39], v242\n" \
  "v_exp_f32 v42, v42\n" \
  "v_exp_f32 v43, v43\n" \
  "v_exp_f32 v44, v44\n" \
  "v_exp_f32 v45, v45\n" \
  "v_exp_f32 v46, v46\n" \
  "v_exp_f32 v47, v47\n" \
  "v_exp_f32 v48, v48\n" \
  "v_exp_f32 v49, v49\n" \
  "v_smfmac_f32_32x32x32_bf16 v[0:15], v[72:75], v[40:47], v242\n" \
  "v_exp_f32 v50, v50\n" \
  "v_exp_f32 v51, v51\n" \
  "v_exp_f32 v52, v52\n" \
  "s_waitcnt vmcnt(6)\n" \
  "v_mfma_f32_32x32x16_bf16 v[32:47], v[76:79], v[228:231], 0\n" \
  "v_exp_f32 v53, v53\n" \
  "v_exp_f32 v54, v54\n" \
  "v_exp_f32 v55, v55\n" \
  "v_exp_f32 v56, v56\n" \
  "v_exp_f32 v57, v57\n" \
  "v_smfmac_f32_32x32x32_bf16 v[16:31], v[68:71], v[48:55], v242\n" \
  "v_exp_f32 v58, v58\n" \
  "v_exp_f32 v59, v59\n" \
  "v_exp_f32 v60, v60\n" \
  "v_exp_f32 v61, v61\n" \
  "v_exp_f32 v62, v62\n" \
  "v_exp_f32 v63, v63\n" \
  "v_exp_f32 v32, v32\n" \
  "v_exp_f32 v33, v33\n" \
  "v_smfmac_f32_32x32x32_bf16 v[16:31], v[72:75], v[56:63], v242\n" \
  "v_exp_f32 v34, v34\n" \
  "v_exp_f32 v35, v35\n" \
  "v_exp_f32 v36, v36\n" \
  "v_mfma_f32_32x32x16_bf16 v[48:63], v[76:79], v[232:235], 0\n" \
  "s_add_u32 s77, s77, 4096\n" \
  "buffer_load_dwordx4 v[112:115], v240, s[84:87], s77 offen offset:0 sc1\n" \
  "buffer_load_dwordx4 v[124:127], v240, s[84:87], s77 offen offset:2048 sc1\n" \
  "s_mov_b64 exec, s[82:83]\n" \
  "buffer_load_dwordx4 v[116:119], v241, s[84:87], s77 offen offset:0 sc1\n" \
  "buffer_load_dwordx4 v[120:123], v241, s[84:87], s77 offen offset:512 sc1\n" \
  "buffer_load_dwordx4 v[128:131], v241, s[84:87], s77 offen offset:2048 sc1\n" \
  "buffer_load_dwordx4 v[132:135], v241, s[84:87], s77 offen offset:2560 sc1\n" \
  "s_mov_b64 exec, -1\n" \
  "buffer_load_dwordx2 v[196:197], v206, s[84:87], 0 offen sc1\n" \
  "v_exp_f32 v37, v37\n" \
  "v_exp_f32 v38, v38\n" \
  "v_exp_f32 v39, v39\n" \
  "v_exp_f32 v40, v40\n" \
  "v_exp_f32 v41, v41\n" \
  "v_smfmac_f32_32x32x32_bf16 v[0:15], v[80:83], v[32:39], v242\n" \
  "v_exp_f32 v42, v42\n" \
  "v_exp_f32 v43, v43\n" \
  "v_exp_f32 v44, v44\n" \
  "v_exp_f32 v45, v45\n" \
  "v_exp_f32 v46, v46\n" \
  "v_exp_f32 v47, v47\n" \
  "v_exp_f32 v48, v48\n" \
  "v_exp_f32 v49, v49\n" \
  "v_smfmac_f32_32x32x32_bf16 v[0:15], v[84:87], v[40:47], v242\n" \
  "v_exp_f32 v50, v50\n" \
  "v_exp_f32 v51, v51\n" \
  "v_exp_f32 v52, v52\n" \
  "s_waitcnt vmcnt(9)\n" \
  "v_mfma_f32_32x32x16_bf16 v[32:47], v[88:91], v[228:231], 0\n" \
  "v_exp_f32 v53, v53\n" \
  "v_exp_f32 v54, v54\n" \
  "v_exp_f32 v55, v55\n" \
  "v_exp_f32 v56, v56\n" \
  "v_exp_f32 v57, v57\n" \
  "v_smfmac_f32_32x32x32_bf16 v[16:31], v[80:83], v[48:55], v242\n" \
  "v_exp_f32 v58, v58\n" \
  "v_exp_f32 v59, v59\n" \
  "v_exp_f32 v60, v60\n" \
  "v_exp_f32 v61, v61\n" \
  "v_exp_f32 v62, v62\n" \
  "v_exp_f32 v63, v63\n" \
  "s_setprio 2\n" \
  "v_exp_f32 v32, v32\n" \
  "v_exp_f32 v33, v33\n" \
  "v_smfmac_f32_32x32x32_bf16 v[16:31], v[84:87], v[56:63], v242\n" \
  "v_exp_f32 v34, v34\n" \
  "v_exp_f32 v35, v35\n" \
  "v_exp_f32 v36, v36\n" \
  "v_mfma_f32_32x32x16_bf16 v[48:63], v[88:91], v[232:235], 0\n" \
  "s_add_u32 s77, s77, 4096\n" \
  "buffer_load_dwordx4 v[136:139], v240, s[84:87], s77 offen offset:0 sc1\n" \
  "buffer_load_dwordx4 v[148:151], v240, s[84:87], s77 offen offset:2048 sc1\n" \
  "s_mov_b64 exec, s[82:83]\n" \
  "buffer_load_dwordx4 v[140:143], v241, s[84:87], s77 offen offset:0 sc1\n" \
  "buffer_load_dwordx4 v[144:147], v241, s[84:87], s77 offen offset:512 sc1\n" \
  "buffer_load_dwordx4 v[152:155], v241, s[84:87], s77 offen offset:2048 sc1\n" \
  "buffer_load_dwordx4 v[156:159], v241, s[84:87], s77 offen offset:2560 sc1\n" \
  "s_mov_b64 exec, -1\n" \
  "v_exp_f32 v37, v37\n" \
  "v_exp_f32 v38, v38\n" \
  "v_exp_f32 v39, v39\n" \
  "v_exp_f32 v40, v40\n" \
  "v_exp_f32 v41, v41\n" \
  "v_smfmac_f32_32x32x32_bf16 v[0:15], v[92:95], v[32:39], v242\n" \
  "v_exp_f32 v42, v42\n" \
  "v_exp_f32 v43, v43\n" \
  "v_exp_f32 v44, v44\n" \
  "v_exp_f32 v45, v45\n" \
  "v_exp_f32 v46, v46\n" \
  "v_exp_f32 v47, v47\n" \
  "v_exp_f32 v48, v48\n" \
  "v_exp_f32 v49, v49\n" \
  "v_smfmac_f32_32x32x32_bf16 v[0:15], v[96:99], v[40:47], v242\n" \
  "v_exp_f32 v50, v50\n" \
  "v_exp_f32 v51, v51\n" \
  "v_exp_f32 v52, v52\n" \
  "s_waitcnt vmcnt(13)\n" \
  "v_mfma_f32_32x32x16_bf16 v[32:47], v[100:103], v[228:231], 0\n" \
  "v_exp_f32 v53, v53\n" \
  "v_exp_f32 v54, v54\n" \
  "v_exp_f32 v55, v55\n" \
  "v_exp_f32 v56, v56\n" \
  "v_exp_f32 v57, v57\n" \
  "v_smfmac_f32_32x32x32_bf16 v[16:31], v[92:95], v[48:55], v242\n" \
  "v_exp_f32 v58, v58\n" \
  "v_exp_f32 v59, v59\n" \
  "v_exp_f32 v60, v60\n" \
  "v_exp_f32 v61, v61\n" \
  "v_exp_f32 v62, v62\n" \
  "v_exp_f32 v63, v63\n" \
  "v_exp_f32 v32, v32\n" \
  "v_exp_f32 v33, v33\n" \
  "v_smfmac_f32_32x32x32_bf16 v[16:31], v[96:99], v[56:63], v242\n" \
  "v_exp_f32 v34, v34\n" \
  "v_exp_f32 v35, v35\n" \
  "v_exp_f32 v36, v36\n" \
  "v_mfma_f32_32x32x16_bf16 v[48:63], v[100:103], v[232:235], 0\n" \
  "s_waitcnt vmcnt(6)\n" \
  "v_cmp_ne_u64 vcc, s[68:69], v[196:197]\n" \
  "s_and_b64 s[88:89], vcc, exec\n" \
  "s_cbranch_scc1 .La_s9\n" \
  ".La_b9:\n" \
  "s_add_u32 s80, s80, 16384\n" \
  "s_and_b32 s77, s80, 0xfc00\n" \
  "s_or_b32 s77, s77, s70\n" \
  "buffer_load_dwordx4 v[64:67], v240, s[84:87], s77 offen offset:0 sc1\n" \
  "buffer_load_dwordx4 v[76:79], v240, s[84:87], s77 offen offset:2048 sc1\n" \
  "s_mov_b64 exec, s[82:83]\n" \
  "buffer_load_dwordx4 v[68:71], v241, s[84:87], s77 offen offset:0 sc1\n" \
  "buffer_load_dwordx4 v[72:75], v241, s[84:87], s77 offen offset:512 sc1\n" \
  "buffer_load_dwordx4 v[80:83], v241, s[84:87], s77 offen offset:2048 sc1\n" \
  "buffer_load_dwordx4 v[84:87], v241, s[84:87], s77 offen offset:2560 sc1\n" \
  "s_mov_b64 exec, -1\n" \
  "v_exp_f32 v37, v37\n" \
  "v_exp_f32 v38, v38\n" \
  "v_exp_f32 v39, v39\n" \
  "v_exp_f32 v40, v40\n" \
  "v_exp_f32 v41, v41\n" \
  "v_smfmac_f32_32x32x32_bf16 v[0:15], v[104:107], v[32:39], v242\n" \
  "v_exp_f32 v42, v42\n" \
  "v_exp_f32 v43, v43\n" \
  "v_exp_f32 v44, v44\n" \
  "v_exp_f32 v45, v45\n" \
  "v_exp_f32 v46, v46\n" \
  "v_exp_f32 v47, v47\n" \
  "v_exp_f32 v48, v48\n" \
  "v_exp_f32 v49, v49\n" \
  "v_smfmac_f32_32x32x32_bf16 v[0:15], v[108:111], v[40:47], v242\n" \
  "v_exp_f32 v50, v50\n" \
  "v_exp_f32 v51, v51\n" \
  "v_exp_f32 v52, v52\n" \
  "v_mfma_f32_32x32x16_bf16 v[32:47], v[112:115], v[228:231], 0\n" \
  "s_cmp_ge_u32 s56, 4\n" \
  "s_cbranch_scc1 .La_t\n" \
  "buffer_store_dwordx4 v[168:171], v160, s[84:87], 0 offen sc1\n" \
  ".La_t:\n" \
  "v_exp_f32 v53, v53\n" \
  "v_exp_f32 v54, v54\n" \
  "v_exp_f32 v55, v55\n" \
  "v_exp_f32 v56, v56\n" \
  "v_exp_f32 v57, v57\n" \
  "v_smfmac_f32_32x32x32_bf16 v[16:31], v[104:107], v[48:55], v242\n" \
  "v_exp_f32 v58, v58\n" \
  "v_exp_f32 v59, v59\n" \
  "v_exp_f32 v60, v60\n" \
  "v_exp_f32 v61, v61\n" \
  "v_exp_f32 v62, v62\n" \
  "v_exp_f32 v63, v63\n" \
  "s_setprio 1\n" \
  "v_exp_f32 v32, v32\n" \
  "v_exp_f32 v33, v33\n" \
  "v_smfmac_f32_32x32x32_bf16 v[16:31], v[108:111], v[56:63], v242\n" \
  "v_exp_f32 v34, v34\n" \
  "v_exp_f32 v35, v35\n" \
  "v_exp_f32 v36, v36\n" \
  "v_mfma_f32_32x32x16_bf16 v[48:63], v[112:115], v[232:235], 0\n" \
  "s_add_u32 s77, s77, 4096\n" \
  "buffer_load_dwordx4 v[88:91], v240, s[84:87], s77 offen offset:0 sc1\n" \
  "buffer_load_dwordx4 v[100:103], v240, s[84:87], s77 offen offset:2048 sc1\n" \
  "s_mov_b64 exec, s[82:83]\n" \
  "buffer_load_dwordx4 v[92:95], v241, s[84:87], s77 offen offset:0 sc1\n" \
  "buffer_load_dwordx4 v[96:99], v241, s[84:87], s77 offen offset:512 sc1\n" \
  "buffer_load_dwordx4 v[104:107], v241, s[84:87], s77 offen offset:2048 sc1\n" \
  "buffer_load_dwordx4 v[108:111], v241, s[84:87], s77 offen offset:2560 sc1\n" \
  "s_mov_b64 exec, -1\n" \
  "v_exp_f32 v37, v37\n" \
  "v_exp_f32 v38, v38\n" \
  "v_exp_f32 v39, v39\n" \
  "v_exp_f32 v40, v40\n" \
  "v_exp_f32 v41, v41\n" \
  "s_lshr_b32 s62, s78, 6\n" \
  "s_bfe_u32 s61, s78, 0x30003\n" \
  "s_lshr_b32 s63, s62, 4\n" \
  "s_lshl1_add_u32 s63, s61, s63\n" \
  "s_and_b32 s64, s62, 15\n" \
  "s_lshr_b32 s65, s64, 3\n" \
  "s_lshl1_add_u32 s65, s63, s65\n" \
  "s_and_b32 s66, s62, 7\n" \
  "v_smfmac_f32_32x32x32_bf16 v[0:15], v[116:119], v[32:39], v242\n" \
  "v_exp_f32 v42, v42\n" \
  "v_exp_f32 v43, v43\n" \
  "v_exp_f32 v44, v44\n" \
  "v_exp_f32 v45, v45\n" \
  "v_exp_f32 v46, v46\n" \
  "v_exp_f32 v47, v47\n" \
  "v_exp_f32 v48, v48\n" \
  "v_exp_f32 v49, v49\n" \
  "v_smfmac_f32_32x32x32_bf16 v[0:15], v[120:123], v[40:47], v242\n" \
  "v_exp_f32 v50, v50\n" \
  "v_exp_f32 v51, v51\n" \
  "v_exp_f32 v52, v52\n" \
  "v_mfma_f32_32x32x16_bf16 v[32:47], v[124:127], v[228:231], 0\n" \
  "v_exp_f32 v53, v53\n" \
  "v_exp_f32 v54, v54\n" \
  "v_exp_f32 v55, v55\n" \
  "v_exp_f32 v56, v56\n" \
  "v_exp_f32 v57, v57\n" \
  "v_smfmac_f32_32x32x32_bf16 v[16:31], v[116:119], v[48:55], v242\n" \
  "v_exp_f32 v58, v58\n" \
  "v_exp_f32 v59, v59\n" \
  "v_exp_f32 v60, v60\n" \
  "v_exp_f32 v61, v61\n" \
  "v_exp_f32 v62, v62\n" \
  "v_exp_f32 v63, v63\n" \
  "v_exp_f32 v32, v32\n" \
  "v_exp_f32 v33, v33\n" \
  "v_smfmac_f32_32x32x32_bf16 v[16:31], v[120:123], v[56:63], v242\n" \
  "v_exp_f32 v34, v34\n" \
  "v_exp_f32 v35, v35\n" \
  "v_exp_f32 v36, v36\n" \
  "v_mfma_f32_32x32x16_bf16 v[48:63], v[124:127], v[232:235], 0\n" \
  "v_exp_f32 v37, v37\n" \
  "v_exp_f32 v38, v38\n" \
  "v_exp_f32 v39, v39\n" \
  "v_exp_f32 v40, v40\n" \
  "v_exp_f32 v41, v41\n" \
  "s_lshr_b32 s40, s56, 2\n" \
  "s_and_b32 s41, s56, 3\n" \
  "s_lshl2_add_u32 s67, s65, s41\n" \
  "s_lshl_b32 s42, s67, 14\n" \
  "s_lshl_b32 s57, s66, 11\n" \
  "s_add_u32 s43, s42, s57\n" \
  "s_lshl_b32 s44, s67, 10\n" \
  "s_lshr_b32 s60, s66, 1\n" \
  "v_smfmac_f32_32x32x32_bf16 v[0:15], v[128:131], v[32:39], v242\n" \
  "v_exp_f32 v42, v42\n" \
  "v_exp_f32 v43, v43\n" \
  "v_exp_f32 v44, v44\n" \
  "v_exp_f32 v45, v45\n" \
  "v_exp_f32 v46, v46\n" \
  "v_exp_f32 v47, v47\n" \
  "v_exp_f32 v48, v48\n" \
  "v_exp_f32 v49, v49\n" \
  "v_smfmac_f32_32x32x32_bf16 v[0:15], v[132:135], v[40:47], v242\n" \
  "v_exp_f32 v50, v50\n" \
  "v_exp_f32 v51, v51\n" \
  "v_exp_f32 v52, v52\n" \
  "s_waitcnt vmcnt(12)\n" \
  "v_mfma_f32_32x32x16_bf16 v[32:47], v[136:139], v[228:231], 0\n" \
  "v_exp_f32 v53, v53\n" \
  "v_exp_f32 v54, v54\n" \
  "v_exp_f32 v55, v55\n" \
  "v_exp_f32 v56, v56\n" \
  "v_exp_f32 v57, v57\n" \
  "v_smfmac_f32_32x32x32_bf16 v[16:31], v[128:131], v[48:55], v242\n" \
  "v_exp_f32 v58, v58\n" \
  "v_exp_f32 v59, v59\n" \
  "v_exp_f32 v60, v60\n" \
  "v_exp_f32 v61, v61\n" \
  "v_exp_f32 v62, v62\n" \
  "v_exp_f32 v63, v63\n" \
  "s_setprio 0\n" \
  "v_exp_f32 v32, v32\n" \
  "v_exp_f32 v33, v33\n" \
  "v_smfmac_f32_32x32x32_bf16 v[16:31], v[132:135], v[56:63], v242\n" \
  "v_exp_f32 v34, v34\n" \
  "v_exp_f32 v35, v35\n" \
  "v_exp_f32 v36, v36\n" \
  "v_mfma_f32_32x32x16_bf16 v[48:63], v[136:139], v[232:235], 0\n" \
  "s_add_u32 s77, s77, 4096\n" \
  "buffer_load_dwordx4 v[112:115], v240, s[84:87], s77 offen offset:0 sc1\n" \
  "buffer_load_dwordx4 v[124:127], v240, s[84:87], s77 offen offset:2048 sc1\n" \
  "s_mov_b64 exec, s[82:83]\n" \
  "buffer_load_dwordx4 v[116:119], v241, s[84:87], s77 offen offset:0 sc1\n" \
  "buffer_load_dwordx4 v[120:123], v241, s[84:87], s77 offen offset:512 sc1\n" \
  "buffer_load_dwordx4 v[128:131], v241, s[84:87], s77 offen offset:2048 sc1\n" \
  "buffer_load_dwordx4 v[132:135], v241, s[84:87], s77 offen offset:2560 sc1\n" \
  "s_mov_b64 exec, -1\n" \
  "v_exp_f32 v37, v37\n" \
  "v_exp_f32 v38, v38\n" \
  "v_exp_f32 v39, v39\n" \
  "v_exp_f32 v40, v40\n" \
  "v_exp_f32 v41, v41\n" \
  "s_lshl_b32 s60, s60, 8\n" \
  "s_add_u32 s44, s44, s60\n" \
  "s_and_b32 s60, s66, 1\n" \
  "s_lshl3_add_u32 s44, s60, s44\n" \
  "s_lshl_b32 s45, s66, 16\n" \
  "s_lshl_b32 s60, s41, 14\n" \
  "s_add_u32 s45, s45, s60\n" \
  "s_add_u32 s45, s45, 6291456\n" \
  "v_smfmac_f32_32x32x32_bf16 v[0:15], v[140:143], v[32:39], v242\n" \
  "v_exp_f32 v42, v42\n" \
  "v_exp_f32 v43, v43\n" \
  "v_exp_f32 v44, v44\n" \
  "v_exp_f32 v45, v45\n" \
  "v_exp_f32 v46, v46\n" \
  "v_exp_f32 v47, v47\n" \
  "v_exp_f32 v48, v48\n" \
  "v_exp_f32 v49, v49\n" \
  "v_smfmac_f32_32x32x32_bf16 v[0:15], v[144:147], v[40:47], v242\n" \
  "v_exp_f32 v50, v50\n" \
  "v_exp_f32 v51, v51\n" \
  "v_exp_f32 v52, v52\n" \
  "v_mfma_f32_32x32x16_bf16 v[32:47], v[148:151], v[228:231], 0\n" \
  "v_exp_f32 v53, v53\n" \
  "v_exp_f32 v54, v54\n" \
  "v_exp_f32 v55, v55\n" \
  "v_exp_f32 v56, v56\n" \
  "v_exp_f32 v57, v57\n" \
  "v_smfmac_f32_32x32x32_bf16 v[16:31], v[140:143], v[48:55], v242\n" \
  "v_exp_f32 v58, v58\n" \
  "v_exp_f32 v59, v59\n" \
  "v_exp_f32 v60, v60\n" \
  "v_exp_f32 v61, v61\n" \
  "v_exp_f32 v62, v62\n" \
  "v_exp_f32 v63, v63\n" \
  "v_exp_f32 v32, v32\n" \
  "v_exp_f32 v33, v33\n" \
  "v_smfmac_f32_32x32x32_bf16 v[16:31], v[144:147], v[56:63], v242\n" \
  "v_exp_f32 v34, v34\n" \
  "v_exp_f32 v35, v35\n" \
  "v_exp_f32 v36, v36\n" \
  "v_mfma_f32_32x32x16_bf16 v[48:63], v[148:151], v[232:235], 0\n" \
  "v_exp_f32 v37, v37\n" \
  "v_exp_f32 v38, v38\n" \
  "v_exp_f32 v39, v39\n" \
  "v_exp_f32 v40, v40\n" \
  "v_exp_f32 v41, v41\n" \
  "s_mul_i32 s46, s64, 114688\n" \
  "s_add_u32 s46, s46, 6815744\n" \
  "s_lshl1_add_u32 s60, s65, s66\n" \
  "s_add_u32 s60, s60, s56\n" \
  "s_and_b32 s60, s60, 7\n" \
  "s_mul_i32 s47, s66, 229376\n" \
  "s_lshl_b32 s61, s60, 12\n" \
  "s_add_u32 s47, s47, s61\n" \
  "v_smfmac_f32_32x32x32_bf16 v[0:15], v[152:155], v[32:39], v242\n" \
  "v_exp_f32 v42, v42\n" \
  "v_exp_f32 v43, v43\n" \
  "v_exp_f32 v44, v44\n" \
  "v_exp_f32 v45, v45\n" \
  "v_exp_f32 v46, v46\n" \
  "v_exp_f32 v47, v47\n" \
  "v_exp_f32 v48, v48\n" \
  "v_exp_f32 v49, v49\n" \
  "v_smfmac_f32_32x32x32_bf16 v[0:15], v[156:159], v[40:47], v242\n" \
  "v_exp_f32 v50, v50\n" \
  "v_exp_f32 v51, v51\n" \
  "v_exp_f32 v52, v52\n" \
  "s_waitcnt vmcnt(12)\n" \
  "v_mfma_f32_32x32x16_bf16 v[32:47], v[64:67], v[228:231], 0\n" \
  "v_exp_f32 v53, v53\n" \
  "v_exp_f32 v54, v54\n" \
  "v_exp_f32 v55, v55\n" \
  "v_exp_f32 v56, v56\n" \
  "v_exp_f32 v57, v57\n" \
  "v_smfmac_f32_32x32x32_bf16 v[16:31], v[152:155], v[48:55], v242\n" \
  "v_exp_f32 v58, v58\n" \
  "v_exp_f32 v59, v59\n" \
  "v_exp_f32 v60, v60\n" \
  "v_exp_f32 v61, v61\n" \
  "v_exp_f32 v62, v62\n" \
  "v_exp_f32 v63, v63\n" \
  "s_waitcnt vmcnt(12)\n" \
  "s_barrier\n" \
  "s_add_u32 s81, s78, 0x5000\n" \
  "s_cmp_lg_u32 s56, 4\n" \
  "s_cbranch_scc1 .La_f0\n" \
  "s_mov_b64 exec, 1\n" \
  "buffer_store_dwordx2 v[246:247], off, s[84:87], s81 sc1\n" \
  "s_mov_b64 exec, -1\n" \
  ".La_f0:\n" \
  ".La_r7_1:\n" \
  "s_setprio 3\n" \
  "v_exp_f32 v32, v32\n" \
  "v_exp_f32 v33, v33\n" \
  "v_smfmac_f32_32x32x32_bf16 v[16:31], v[156:159], v[56:63], v242\n" \
  "v_exp_f32 v34, v34\n" \
  "v_exp_f32 v35, v35\n" \
  "v_exp_f32 v36, v36\n" \
  "v_mfma_f32_32x32x16_bf16 v[48:63], v[64:67], v[232:235], 0\n" \
  "s_add_u32 s77, s77, 4096\n" \
  "buffer_load_dwordx4 v[136:139], v240, s[84:87], s77 offen offset:0 sc1\n" \
  "buffer_load_dwordx4 v[148:151], v240, s[84:87], s77 offen offset:2048 sc1\n" \
  "s_mov_b64 exec, s[82:83]\n" \
  "buffer_load_dwordx4 v[140:143], v241, s[84:87], s77 offen offset:0 sc1\n" \
  "buffer_load_dwordx4 v[144:147], v241, s[84:87], s77 offen offset:512 sc1\n" \
  "buffer_load_dwordx4 v[152:155], v241, s[84:87], s77 offen offset:2048 sc1\n" \
  "buffer_load_dwordx4 v[156:159], v241, s[84:87], s77 offen offset:2560 sc1\n" \
  "s_mov_b64 exec, -1\n" \
  "buffer_store_dwordx4 v[236:239], v248, s[84:87], s73 offen offset:0 sc1\n" \
  "v_exp_f32 v37, v37\n" \
  "v_exp_f32 v38, v38\n" \
  "v_exp_f32 v39, v39\n" \
  "v_exp_f32 v40, v40\n" \
  "v_exp_f32 v41, v41\n" \
  "s_add_u32 s47, s47, 8650752\n" \
  "s_mul_i32 s51, s65, 229376\n" \
  "s_lshl_b32 s61, s60, 10\n" \
  "s_add_u32 s51, s51, s61\n" \
  "s_add_u32 s51, s51, 18874368\n" \
  "s_and_b32 s60, s41, 1\n" \
  "s_lshl3_add_u32 s48, s63, s60\n" \
  "s_lshl1_add_u32 s48, s40, s48\n" \
  "v_smfmac_f32_32x32x32_bf16 v[0:15], v[68:71], v[32:39], v242\n" \
  "v_exp_f32 v42, v42\n" \
  "v_exp_f32 v43, v43\n" \
  "v_exp_f32 v44, v44\n" \
  "v_exp_f32 v45, v45\n" \
  "v_exp_f32 v46, v46\n" \
  "v_exp_f32 v47, v47\n" \
  "v_exp_f32 v48, v48\n" \
  "v_exp_f32 v49, v49\n" \
  "v_smfmac_f32_32x32x32_bf16 v[0:15], v[72:75], v[40:47], v242\n" \
  "v_exp_f32 v50, v50\n" \
  "v_exp_f32 v51, v51\n" \
  "v_exp_f32 v52, v52\n" \
  "v_mfma_f32_32x32x16_bf16 v[32:47], v[76:79], v[228:231], 0\n" \
  "v_exp_f32 v53, v53\n" \
  "v_exp_f32 v54, v54\n" \
  "v_exp_f32 v55, v55\n" \
  "v_exp_f32 v56, v56\n" \
  "v_exp_f32 v57, v57\n" \
  "v_smfmac_f32_32x32x32_bf16 v[16:31], v[68:71], v[48:55], v242\n" \
  "v_exp_f32 v58, v58\n" \
  "v_exp_f32 v59, v59\n" \
  "v_exp_f32 v60, v60\n" \
  "v_exp_f32 v61, v61\n" \
  "v_exp_f32 v62, v62\n" \
  "v_exp_f32 v63, v63\n" \
  "v_exp_f32 v32, v32\n" \
  "v_exp_f32 v33, v33\n" \
  "v_smfmac_f32_32x32x32_bf16 v[16:31], v[72:75], v[56:63], v242\n" \
  "v_exp_f32 v34, v34\n" \
  "v_exp_f32 v35, v35\n" \
  "v_exp_f32 v36, v36\n" \
  "v_mfma_f32_32x32x16_bf16 v[48:63], v[76:79], v[232:235], 0\n" \
  "buffer_store_dwordx4 v[236:239], v248, s[84:87], s73 offen offset:1024 sc1\n" \
  "v_exp_f32 v37, v37\n" \
  "v_exp_f32 v38, v38\n" \
  "v_exp_f32 v39, v39\n" \
  "v_exp_f32 v40, v40\n" \
  "v_exp_f32 v41, v41\n" \
  "s_lshr_b32 s60, s41, 1\n" \
  "s_lshl2_add_u32 s48, s60, s48\n" \
  "s_mul_i32 s60, s48, 57344\n" \
  "s_add_u32 s60, s60, 18874368\n" \
  "s_lshl_b32 s61, s64, 10\n" \
  "s_add_u32 s49, s60, s61\n" \
  "s_and_b32 s58, s61, 14336\n" \
  "s_lshr_b32 s61, s58, 1\n" \
  "v_smfmac_f32_32x32x32_bf16 v[0:15], v[80:83], v[32:39], v242\n" \
  "v_exp_f32 v42, v42\n" \
  "v_exp_f32 v43, v43\n" \
  "v_exp_f32 v44, v44\n" \
  "v_exp_f32 v45, v45\n" \
  "v_exp_f32 v46, v46\n" \
  "v_exp_f32 v47, v47\n" \
  "v_exp_f32 v48, v48\n" \
  "v_exp_f32 v49, v49\n" \
  "v_smfmac_f32_32x32x32_bf16 v[0:15], v[84:87], v[40:47], v242\n" \
  "v_exp_f32 v50, v50\n" \
  "v_exp_f32 v51, v51\n" \
  "v_exp_f32 v52, v52\n" \
  "s_waitcnt vmcnt(14)\n" \
  "v_mfma_f32_32x32x16_bf16 v[32:47], v[88:91], v[228:231], 0\n" \
  "v_exp_f32 v53, v53\n" \
  "v_exp_f32 v54, v54\n" \
  "v_exp_f32 v55, v55\n" \
  "v_exp_f32 v56, v56\n" \
  "v_exp_f32 v57, v57\n" \
  "v_smfmac_f32_32x32x32_bf16 v[16:31], v[80:83], v[48:55], v242\n" \
  "v_exp_f32 v58, v58\n" \
  "v_exp_f32 v59, v59\n" \
  "v_exp_f32 v60, v60\n" \
  "v_exp_f32 v61, v61\n" \
  "v_exp_f32 v62, v62\n" \
  "v_exp_f32 v63, v63\n" \
  "s_setprio 2\n" \
  "v_exp_f32 v32, v32\n" \
  "v_exp_f32 v33, v33\n" \
  "v_smfmac_f32_32x32x32_bf16 v[16:31], v[84:87], v[56:63], v242\n" \
  "v_exp_f32 v34, v34\n" \
  "v_exp_f32 v35, v35\n" \
  "v_exp_f32 v36, v36\n" \
  "v_mfma_f32_32x32x16_bf16 v[48:63], v[88:91], v[232:235], 0\n" \
  "s_add_u32 s80, s80, 16384\n" \
  "s_and_b32 s77, s80, 0xfc00\n" \
  "s_or_b32 s77, s77, s70\n" \
  "buffer_load_dwordx4 v[64:67], v240, s[84:87], s77 offen offset:0 sc1\n" \
  "buffer_load_dwordx4 v[76:79], v240, s[84:87], s77 offen offset:2048 sc1\n" \
  "s_mov_b64 exec, s[82:83]\n" \
  "buffer_load_dwordx4 v[68:71], v241, s[84:87], s77 offen offset:0 sc1\n" \
  "buffer_load_dwordx4 v[72:75], v241, s[84:87], s77 offen offset:512 sc1\n" \
  "buffer_load_dwordx4 v[80:83], v241, s[84:87], s77 offen offset:2048 sc1\n" \
  "buffer_load_dwordx4 v[84:87], v241, s[84:87], s77 offen offset:2560 sc1\n" \
  "s_mov_b64 exec, -1\n" \
  "v_exp_f32 v37, v37\n" \
  "v_exp_f32 v38, v38\n" \
  "v_exp_f32 v39, v39\n" \
  "v_exp_f32 v40, v40\n" \
  "v_exp_f32 v41, v41\n" \
  "s_add_u32 s50, s60, s61\n" \
  "s_add_u32 s50, s50, 49152\n" \
  "s_and_b32 s61, s64, 1\n" \
  "s_lshl_b32 s61, s61, 9\n" \
  "s_add_u32 s50, s50, s61\n" \
  "s_and_b32 s59, s65, 3\n" \
  "s_lshl_b32 s59, s59, 12\n" \
  "s_lshl_b32 s60, s66, 7\n" \
  "v_smfmac_f32_32x32x32_bf16 v[0:15], v[92:95], v[32:39], v242\n" \
  "v_exp_f32 v42, v42\n" \
  "v_exp_f32 v43, v43\n" \
  "v_exp_f32 v44, v44\n" \
  "v_exp_f32 v45, v45\n" \
  "v_exp_f32 v46, v46\n" \
  "v_exp_f32 v47, v47\n" \
  "v_exp_f32 v48, v48\n" \
  "v_exp_f32 v49, v49\n" \
  "v_smfmac_f32_32x32x32_bf16 v[0:15], v[96:99], v[40:47], v242\n" \
  "v_exp_f32 v50, v50\n" \
  "v_exp_f32 v51, v51\n" \
  "v_exp_f32 v52, v52\n" \
  "v_mfma_f32_32x32x16_bf16 v[32:47], v[100:103], v[228:231], 0\n" \
  "v_exp_f32 v53, v53\n" \
  "v_exp_f32 v54, v54\n" \
  "v_exp_f32 v55, v55\n" \
  "v_exp_f32 v56, v56\n" \
  "v_exp_f32 v57, v57\n" \
  "v_smfmac_f32_32x32x32_bf16 v[16:31], v[92:95], v[48:55], v242\n" \
  "v_exp_f32 v58, v58\n" \
  "v_exp_f32 v59, v59\n" \
  "v_exp_f32 v60, v60\n" \
  "v_exp_f32 v61, v61\n" \
  "v_exp_f32 v62, v62\n" \
  "v_exp_f32 v63, v63\n" \
  "v_exp_f32 v32, v32\n" \
  "v_exp_f32 v33, v33\n" \
  "v_smfmac_f32_32x32x32_bf16 v[16:31], v[96:99], v[56:63], v242\n" \
  "v_exp_f32 v34, v34\n" \
  "v_exp_f32 v35, v35\n" \
  "v_exp_f32 v36, v36\n" \
  "v_mfma_f32_32x32x16_bf16 v[48:63], v[100:103], v[232:235], 0\n" \
  "buffer_store_dwordx4 v[236:239], v248, s[84:87], s73 offen offset:2048 sc1\n" \
  "v_exp_f32 v37, v37\n" \
  "v_exp_f32 v38, v38\n" \
  "v_exp_f32 v39, v39\n" \
  "v_exp_f32 v40, v40\n" \
  "v_exp_f32 v41, v41\n" \
  "s_add_u32 s61, s42, s60\n" \
  "s_add_u32 s52, s38, s61\n" \
  "s_addc_u32 s53, s39, 0\n" \
  "s_lshl_b32 s61, s65, 16\n" \
  "s_add_u32 s61, s61, s60\n" \
  "s_add_u32 s54, s36, s61\n" \
  "s_addc_u32 s55, s37, 0\n" \
  "v_smfmac_f32_32x32x32_bf16 v[0:15], v[104:107], v[32:39], v242\n" \
  "v_exp_f32 v42, v42\n" \
  "v_exp_f32 v43, v43\n" \
  "v_exp_f32 v44, v44\n" \
  "v_exp_f32 v45, v45\n" \
  "v_exp_f32 v46, v46\n" \
  "v_exp_f32 v47, v47\n" \
  "v_exp_f32 v48, v48\n" \
  "v_exp_f32 v49, v49\n" \
  "v_smfmac_f32_32x32x32_bf16 v[0:15], v[108:111], v[40:47], v242\n" \
  "v_exp_f32 v50, v50\n" \
  "v_exp_f32 v51, v51\n" \
  "v_exp_f32 v52, v52\n" \
  "s_waitcnt vmcnt(15)\n" \
  "v_mfma_f32_32x32x16_bf16 v[32:47], v[112:115], v[228:231], 0\n" \
  "s_cmp_lt_u32 s56, 4\n" \
  "s_cbranch_scc1 .La_u11\n" \
  "ds_read_b64_tr_b16 v[168:169], v165 offset:0\n" \
  "ds_read_b64_tr_b16 v[170:171], v165 offset:512\n" \
  ".La_u11:\n" \
  "v_exp_f32 v53, v53\n" \
  "v_exp_f32 v54, v54\n" \
  "v_exp_f32 v55, v55\n" \
  "v_exp_f32 v56, v56\n" \
  "v_exp_f32 v57, v57\n" \
  "v_smfmac_f32_32x32x32_bf16 v[16:31], v[104:107], v[48:55], v242\n" \
  "v_exp_f32 v58, v58\n" \
  "v_exp_f32 v59, v59\n" \
  "v_exp_f32 v60, v60\n" \
  "v_exp_f32 v61, v61\n" \
  "v_exp_f32 v62, v62\n" \
  "v_exp_f32 v63, v63\n" \
  "s_setprio 1\n" \
  "v_exp_f32 v32, v32\n" \
  "v_exp_f32 v33, v33\n" \
  "v_smfmac_f32_32x32x32_bf16 v[16:31], v[108:111], v[56:63], v242\n" \
  "v_exp_f32 v34, v34\n" \
  "v_exp_f32 v35, v35\n" \
  "v_exp_f32 v36, v36\n" \
  "v_mfma_f32_32x32x16_bf16 v[48:63], v[112:115], v[232:235], 0\n" \
  "s_add_u32 s77, s77, 4096\n" \
  "buffer_load_dwordx4 v[88:91], v240, s[84:87], s77 offen offset:0 sc1\n" \
  "buffer_load_dwordx4 v[100:103], v240, s[84:87], s77 offen offset:2048 sc1\n" \
  "s_mov_b64 exec, s[82:83]\n" \
  "buffer_load_dwordx4 v[92:95], v241, s[84:87], s77 offen offset:0 sc1\n" \
  "buffer_load_dwordx4 v[96:99], v241, s[84:87], s77 offen offset:512 sc1\n" \
  "buffer_load_dwordx4 v[104:107], v241, s[84:87], s77 offen offset:2048 sc1\n" \
  "buffer_load_dwordx4 v[108:111], v241, s[84:87], s77 offen offset:2560 sc1\n" \
  "s_mov_b64 exec, -1\n" \
  "buffer_store_dwordx4 v[236:239], v248, s[84:87], s73 offen offset:3072 sc1\n" \
  "v_exp_f32 v37, v37\n" \
  "v_exp_f32 v38, v38\n" \
  "v_exp_f32 v39, v39\n" \
  "v_exp_f32 v40, v40\n" \
  "v_exp_f32 v41, v41\n" \
  "s_bitcmp1_b32 s66, 2\n" \
  "s_cselect_b32 s60, 2, 1\n" \
  "s_sub_i32 s61, 0, s60\n" \
  "s_bitcmp1_b32 s66, 1\n" \
  "s_cselect_b32 s60, s61, s60\n" \
  "s_lshl4_add_u32 s60, s67, s60\n" \
  "v_bfe_u32 v204, v248, 4, 4\n" \
  "s_add_u32 s61, s57, 14680064\n" \
  "v_add_u32 v204, s60, v204\n" \
  "v_smfmac_f32_32x32x32_bf16 v[0:15], v[116:119], v[32:39], v242\n" \
  "v_exp_f32 v42, v42\n" \
  "v_exp_f32 v43, v43\n" \
  "v_exp_f32 v44, v44\n" \
  "v_exp_f32 v45, v45\n" \
  "v_exp_f32 v46, v46\n" \
  "v_exp_f32 v47, v47\n" \
  "v_exp_f32 v48, v48\n" \
  "v_exp_f32 v49, v49\n" \
  "v_smfmac_f32_32x32x32_bf16 v[0:15], v[120:123], v[40:47], v242\n" \
  "v_exp_f32 v50, v50\n" \
  "v_exp_f32 v51, v51\n" \
  "v_exp_f32 v52, v52\n" \
  "v_mfma_f32_32x32x16_bf16 v[32:47], v[124:127], v[228:231], 0\n" \
  "s_cmp_lt_u32 s56, 4\n" \
  "s_cbranch_scc1 .La_u12\n" \
  "s_waitcnt lgkmcnt(0)\n" \
  "buffer_store_dwordx4 v[168:171], v160, s[84:87], 0 offen sc1\n" \
  "ds_read_b64_tr_b16 v[172:173], v165 offset:1024\n" \
  "ds_read_b64_tr_b16 v[174:175], v165 offset:1536\n" \
  ".La_u12:\n" \
  "v_exp_f32 v53, v53\n" \
  "v_exp_f32 v54, v54\n" \
  "v_exp_f32 v55, v55\n" \
  "v_exp_f32 v56, v56\n" \
  "v_exp_f32 v57, v57\n" \
  "v_smfmac_f32_32x32x32_bf16 v[16:31], v[116:119], v[48:55], v242\n" \
  "v_exp_f32 v58, v58\n" \
  "v_exp_f32 v59, v59\n" \
  "v_exp_f32 v60, v60\n" \
  "v_exp_f32 v61, v61\n" \
  "v_exp_f32 v62, v62\n" \
  "v_exp_f32 v63, v63\n" \
  "v_exp_f32 v32, v32\n" \
  "v_exp_f32 v33, v33\n" \
  "v_smfmac_f32_32x32x32_bf16 v[16:31], v[120:123], v[56:63], v242\n" \
  "v_exp_f32 v34, v34\n" \
  "v_exp_f32 v35, v35\n" \
  "v_exp_f32 v36, v36\n" \
  "v_mfma_f32_32x32x16_bf16 v[48:63], v[124:127], v[232:235], 0\n" \
  "v_exp_f32 v37, v37\n" \
  "v_exp_f32 v38, v38\n" \
  "v_exp_f32 v39, v39\n" \
  "v_exp_f32 v40, v40\n" \
  "v_exp_f32 v41, v41\n" \
  "v_and_b32 v205, 2047, v204\n" \
  "v_cmp_eq_u32 vcc, v204, v205\n" \
  "v_lshrrev_b32 v204, 4, v205\n" \
  "v_cndmask_b32_e64 v253, 0, -2, vcc\n" \
  "v_lshlrev_b32 v204, 14, v204\n" \
  "v_and_b32 v205, 15, v205\n" \
  "v_lshl_or_b32 v204, v205, 4, v204\n" \
  "v_smfmac_f32_32x32x32_bf16 v[0:15], v[128:131], v[32:39], v242\n" \
  "v_exp_f32 v42, v42\n" \
  "v_exp_f32 v43, v43\n" \
  "v_exp_f32 v44, v44\n" \
  "v_exp_f32 v45, v45\n" \
  "v_exp_f32 v46, v46\n" \
  "v_exp_f32 v47, v47\n" \
  "v_exp_f32 v48, v48\n" \
  "v_exp_f32 v49, v49\n" \
  "v_smfmac_f32_32x32x32_bf16 v[0:15], v[132:135], v[40:47], v242\n" \
  "v_exp_f32 v50, v50\n" \
  "v_exp_f32 v51, v51\n" \
  "v_exp_f32 v52, v52\n" \
  "s_waitcnt vmcnt(16)\n" \
  "v_mfma_f32_32x32x16_bf16 v[32:47], v[136:139], v[228:231], 0\n" \
  "s_cmp_lt_u32 s56, 4\n" \
  "s_cbranch_scc1 .La_u13\n" \
  "s_waitcnt lgkmcnt(0)\n" \
  "buffer_store_dwordx4 v[172:175], v161, s[84:87], 0 offen sc1\n" \
  "ds_read_b64_tr_b16 v[176:177], v165 offset:2048\n" \
  "ds_read_b64_tr_b16 v[178:179], v165 offset:2560\n" \
  ".La_u13:\n" \
  "v_exp_f32 v53, v53\n" \
  "v_exp_f32 v54, v54\n" \
  "v_exp_f32 v55, v55\n" \
  "v_exp_f32 v56, v56\n" \
  "v_exp_f32 v57, v57\n" \
  "v_smfmac_f32_32x32x32_bf16 v[16:31], v[128:131], v[48:55], v242\n" \
  "v_exp_f32 v58, v58\n" \
  "v_exp_f32 v59, v59\n" \
  "v_exp_f32 v60, v60\n" \
  "v_exp_f32 v61, v61\n" \
  "v_exp_f32 v62, v62\n" \
  "v_exp_f32 v63, v63\n" \
  "s_setprio 0\n" \
  "v_exp_f32 v32, v32\n" \
  "v_exp_f32 v33, v33\n" \
  "v_smfmac_f32_32x32x32_bf16 v[16:31], v[132:135], v[56:63], v242\n" \
  "v_exp_f32 v34, v34\n" \
  "v_exp_f32 v35, v35\n" \
  "v_exp_f32 v36, v36\n" \
  "v_mfma_f32_32x32x16_bf16 v[48:63], v[136:139], v[232:235], 0\n" \
  "s_add_u32 s77, s77, 4096\n" \
  "buffer_load_dwordx4 v[112:115], v240, s[84:87], s77 offen offset:0 sc1\n" \
  "buffer_load_dwordx4 v[124:127], v240, s[84:87], s77 offen offset:2048 sc1\n" \
  "s_mov_b64 exec, s[82:83]\n" \
  "buffer_load_dwordx4 v[116:119], v241, s[84:87], s77 offen offset:0 sc1\n" \
  "buffer_load_dwordx4 v[120:123], v241, s[84:87], s77 offen offset:512 sc1\n" \
  "buffer_load_dwordx4 v[128:131], v241, s[84:87], s77 offen offset:2048 sc1\n" \
  "buffer_load_dwordx4 v[132:135], v241, s[84:87], s77 offen offset:2560 sc1\n" \
  "s_mov_b64 exec, -1\n" \
  "buffer_store_dwordx4 v[236:239], v248, s[84:87], s74 offen offset:0 sc1\n" \
  "v_exp_f32 v37, v37\n" \
  "v_exp_f32 v38, v38\n" \
  "v_exp_f32 v39, v39\n" \
  "v_exp_f32 v40, v40\n" \
  "v_exp_f32 v41, v41\n" \
  "v_and_b32 v205, 0x300, v248\n" \
  "v_add3_u32 v252, v204, v205, s61\n" \
  "s_bfe_u32 s60, s78, 0x20004\n" \
  "s_lshr_b32 s61, s78, 8\n" \
  "s_lshl3_add_u32 s60, s60, s61\n" \
  "s_and_b32 s61, s56, 1\n" \
  "v_smfmac_f32_32x32x32_bf16 v[0:15], v[140:143], v[32:39], v242\n" \
  "v_exp_f32 v42, v42\n" \
  "v_exp_f32 v43, v43\n" \
  "v_exp_f32 v44, v44\n" \
  "v_exp_f32 v45, v45\n" \
  "v_exp_f32 v46, v46\n" \
  "v_exp_f32 v47, v47\n" \
  "v_exp_f32 v48, v48\n" \
  "v_exp_f32 v49, v49\n" \
  "v_smfmac_f32_32x32x32_bf16 v[0:15], v[144:147], v[40:47], v242\n" \
  "v_exp_f32 v50, v50\n" \
  "v_exp_f32 v51, v51\n" \
  "v_exp_f32 v52, v52\n" \
  "v_mfma_f32_32x32x16_bf16 v[32:47], v[148:151], v[228:231], 0\n" \
  "s_cmp_lt_u32 s56, 4\n" \
  "s_cbranch_scc1 .La_u14\n" \
  "s_waitcnt lgkmcnt(0)\n" \
  "buffer_store_dwordx4 v[176:179], v162, s[84:87], 0 offen sc1\n" \
  "ds_read_b64_tr_b16 v[180:181], v165 offset:3072\n" \
  "ds_read_b64_tr_b16 v[182:183], v165 offset:3584\n" \
  ".La_u14:\n" \
  "v_exp_f32 v53, v53\n" \
  "v_exp_f32 v54, v54\n" \
  "v_exp_f32 v55, v55\n" \
  "v_exp_f32 v56, v56\n" \
  "v_exp_f32 v57, v57\n" \
  "v_smfmac_f32_32x32x32_bf16 v[16:31], v[140:143], v[48:55], v242\n" \
  "v_exp_f32 v58, v58\n" \
  "v_exp_f32 v59, v59\n" \
  "v_exp_f32 v60, v60\n" \
  "v_exp_f32 v61, v61\n" \
  "v_exp_f32 v62, v62\n" \
  "v_exp_f32 v63, v63\n" \
  "v_exp_f32 v32, v32\n" \
  "v_exp_f32 v33, v33\n" \
  "v_smfmac_f32_32x32x32_bf16 v[16:31], v[144:147], v[56:63], v242\n" \
  "v_exp_f32 v34, v34\n" \
  "v_exp_f32 v35, v35\n" \
  "v_exp_f32 v36, v36\n" \
  "v_mfma_f32_32x32x16_bf16 v[48:63], v[148:151], v[232:235], 0\n" \
  "buffer_store_dwordx4 v[236:239], v248, s[84:87], s74 offen offset:1024 sc1\n" \
  "v_exp_f32 v37, v37\n" \
  "v_exp_f32 v38, v38\n" \
  "v_exp_f32 v39, v39\n" \
  "v_exp_f32 v40, v40\n" \
  "v_exp_f32 v41, v41\n" \
  "s_lshl1_add_u32 s60, s60, s61\n" \
  "s_lshl_b32 s60, s60, 15\n" \
  "s_lshr_b32 s61, s70, 17\n" \
  "s_lshl_b32 s61, s61, 10\n" \
  "s_add_u32 s60, s60, s61\n" \
  "s_and_b32 s61, s56, 2\n" \
  "s_lshl_b32 s61, s61, 8\n" \
  "s_add_u32 s60, s60, s61\n" \
  "v_smfmac_f32_32x32x32_bf16 v[0:15], v[152:155], v[32:39], v242\n" \
  "v_exp_f32 v42, v42\n" \
  "v_exp_f32 v43, v43\n" \
  "v_exp_f32 v44, v44\n" \
  "v_exp_f32 v45, v45\n" \
  "v_exp_f32 v46, v46\n" \
  "v_exp_f32 v47, v47\n" \
  "v_exp_f32 v48, v48\n" \
  "v_exp_f32 v49, v49\n" \
  "v_smfmac_f32_32x32x32_bf16 v[0:15], v[156:159], v[40:47], v242\n" \
  "v_exp_f32 v50, v50\n" \
  "v_exp_f32 v51, v51\n" \
  "v_exp_f32 v52, v52\n" \
  "s_waitcnt vmcnt(16)\n" \
  "v_mfma_f32_32x32x16_bf16 v[32:47], v[64:67], v[228:231], 0\n" \
  "s_cmp_lt_u32 s56, 4\n" \
  "s_cbranch_scc1 .La_u15\n" \
  "s_waitcnt lgkmcnt(0)\n" \
  "buffer_store_dwordx4 v[180:183], v163, s[84:87], 0 offen sc1\n" \
  ".La_u15:\n" \
  "v_exp_f32 v53, v53\n" \
  "v_exp_f32 v54, v54\n" \
  "v_exp_f32 v55, v55\n" \
  "v_exp_f32 v56, v56\n" \
  "v_exp_f32 v57, v57\n" \
  "v_smfmac_f32_32x32x32_bf16 v[16:31], v[152:155], v[48:55], v242\n" \
  "v_exp_f32 v58, v58\n" \
  "v_exp_f32 v59, v59\n" \
  "v_exp_f32 v60, v60\n" \
  "v_exp_f32 v61, v61\n" \
  "v_exp_f32 v62, v62\n" \
  "v_exp_f32 v63, v63\n" \
  "s_barrier\n" \
  ".La_r15_1:\n" \
  "s_setprio 3\n" \
  "v_exp_f32 v32, v32\n" \
  "v_exp_f32 v33, v33\n" \
  "v_smfmac_f32_32x32x32_bf16 v[16:31], v[156:159], v[56:63], v242\n" \
  "v_exp_f32 v34, v34\n" \
  "v_exp_f32 v35, v35\n" \
  "v_exp_f32 v36, v36\n" \
  "v_mfma_f32_32x32x16_bf16 v[48:63], v[64:67], v[232:235], 0\n" \
  "s_add_u32 s77, s77, 4096\n" \
  "buffer_load_dwordx4 v[136:139], v240, s[84:87], s77 offen offset:0 sc1\n" \
  "buffer_load_dwordx4 v[148:151], v240, s[84:87], s77 offen offset:2048 sc1\n" \
  "s_mov_b64 exec, s[82:83]\n" \
  "buffer_load_dwordx4 v[140:143], v241, s[84:87], s77 offen offset:0 sc1\n" \
  "buffer_load_dwordx4 v[144:147], v241, s[84:87], s77 offen offset:512 sc1\n" \
  "buffer_load_dwordx4 v[152:155], v241, s[84:87], s77 offen offset:2048 sc1\n" \
  "buffer_load_dwordx4 v[156:159], v241, s[84:87], s77 offen offset:2560 sc1\n" \
  "s_mov_b64 exec, -1\n" \
  "v_exp_f32 v37, v37\n" \
  "v_exp_f32 v38, v38\n" \
  "v_exp_f32 v39, v39\n" \
  "v_exp_f32 v40, v40\n" \
  "v_exp_f32 v41, v41\n" \
  "s_add_u32 s60, s60, 10485760\n" \
  "v_bfe_u32 v204, v248, 8, 1\n" \
  "v_bfe_u32 v205, v248, 9, 1\n" \
  "v_lshlrev_b32 v204, 14, v204\n" \
  "v_lshl_or_b32 v204, v205, 8, v204\n" \
  "v_and_b32 v205, 0xf0, v248\n" \
  "v_add3_u32 v251, v204, v205, s60\n" \
  "v_smfmac_f32_32x32x32_bf16 v[0:15], v[68:71], v[32:39], v242\n" \
  "v_exp_f32 v42, v42\n" \
  "v_exp_f32 v43, v43\n" \
  "v_exp_f32 v44, v44\n" \
  "v_exp_f32 v45, v45\n" \
  "v_exp_f32 v46, v46\n" \
  "v_exp_f32 v47, v47\n" \
  "v_exp_f32 v48, v48\n" \
  "v_exp_f32 v49, v49\n" \
  "v_smfmac_f32_32x32x32_bf16 v[0:15], v[72:75], v[40:47], v242\n" \
  "v_exp_f32 v50, v50\n" \
  "v_exp_f32 v51, v51\n" \
  "v_exp_f32 v52, v52\n" \
  "v_mfma_f32_32x32x16_bf16 v[32:47], v[76:79], v[228:231], 0\n" \
  "v_exp_f32 v53, v53\n" \
  "v_exp_f32 v54, v54\n" \
  "v_exp_f32 v55, v55\n" \
  "v_exp_f32 v56, v56\n" \
  "v_exp_f32 v57, v57\n" \
  "v_smfmac_f32_32x32x32_bf16 v[16:31], v[68:71], v[48:55], v242\n" \
  "v_exp_f32 v58, v58\n" \
  "v_exp_f32 v59, v59\n" \
  "v_exp_f32 v60, v60\n" \
  "v_exp_f32 v61, v61\n" \
  "v_exp_f32 v62, v62\n" \
  "v_exp_f32 v63, v63\n" \
  "v_exp_f32 v32, v32\n" \
  "v_exp_f32 v33, v33\n" \
  "v_smfmac_f32_32x32x32_bf16 v[16:31], v[72:75], v[56:63], v242\n" \
  "v_exp_f32 v34, v34\n" \
  "v_exp_f32 v35, v35\n" \
  "v_exp_f32 v36, v36\n" \
  "v_mfma_f32_32x32x16_bf16 v[48:63], v[76:79], v[232:235], 0\n" \
  "buffer_store_dwordx4 v[236:239], v248, s[84:87], s74 offen offset:2048 sc1\n" \
  "v_exp_f32 v37, v37\n" \
  "v_exp_f32 v38, v38\n" \
  "v_exp_f32 v39, v39\n" \
  "v_exp_f32 v40, v40\n" \
  "v_exp_f32 v41, v41\n" \
  "v_smfmac_f32_32x32x32_bf16 v[0:15], v[80:83], v[32:39], v242\n" \
  "v_exp_f32 v42, v42\n" \
  "v_exp_f32 v43, v43\n" \
  "v_exp_f32 v44, v44\n" \
  "v_exp_f32 v45, v45\n" \
  "v_exp_f32 v46, v46\n" \
  "v_exp_f32 v47, v47\n" \
  "v_exp_f32 v48, v48\n" \
  "v_exp_f32 v49, v49\n" \
  "v_smfmac_f32_32x32x32_bf16 v[0:15], v[84:87], v[40:47], v242\n" \
  "v_exp_f32 v50, v50\n" \
  "v_exp_f32 v51, v51\n" \
  "v_exp_f32 v52, v52\n" \
  "s_waitcnt vmcnt(16)\n" \
  "v_mfma_f32_32x32x16_bf16 v[32:47], v[88:91], v[228:231], 0\n" \
  "v_exp_f32 v53, v53\n" \
  "v_exp_f32 v54, v54\n" \
  "v_exp_f32 v55, v55\n" \
  "v_exp_f32 v56, v56\n" \
  "v_exp_f32 v57, v57\n" \
  "v_smfmac_f32_32x32x32_bf16 v[16:31], v[80:83], v[48:55], v242\n" \
  "v_exp_f32 v58, v58\n" \
  "v_exp_f32 v59, v59\n" \
  "v_exp_f32 v60, v60\n" \
  "v_exp_f32 v61, v61\n" \
  "v_exp_f32 v62, v62\n" \
  "v_exp_f32 v63, v63\n" \
  "s_setprio 2\n" \
  "v_exp_f32 v32, v32\n" \
  "v_exp_f32 v33, v33\n" \
  "v_smfmac_f32_32x32x32_bf16 v[16:31], v[84:87], v[56:63], v242\n" \
  "v_exp_f32 v34, v34\n" \
  "v_exp_f32 v35, v35\n" \
  "v_exp_f32 v36, v36\n" \
  "v_mfma_f32_32x32x16_bf16 v[48:63], v[88:91], v[232:235], 0\n" \
  "s_add_u32 s80, s80, 16384\n" \
  "s_and_b32 s77, s80, 0xfc00\n" \
  "s_or_b32 s77, s77, s70\n" \
  "buffer_load_dwordx4 v[64:67], v240, s[84:87], s77 offen offset:0 sc1\n" \
  "buffer_load_dwordx4 v[76:79], v240, s[84:87], s77 offen offset:2048 sc1\n" \
  "s_mov_b64 exec, s[82:83]\n" \
  "buffer_load_dwordx4 v[68:71], v241, s[84:87], s77 offen offset:0 sc1\n" \
  "buffer_load_dwordx4 v[72:75], v241, s[84:87], s77 offen offset:512 sc1\n" \
  "buffer_load_dwordx4 v[80:83], v241, s[84:87], s77 offen offset:2048 sc1\n" \
  "buffer_load_dwordx4 v[84:87], v241, s[84:87], s77 offen offset:2560 sc1\n" \
  "s_mov_b64 exec, -1\n" \
  "buffer_store_dwordx4 v[236:239], v248, s[84:87], s74 offen offset:3072 sc1\n" \
  "v_exp_f32 v37, v37\n" \
  "v_exp_f32 v38, v38\n" \
  "v_exp_f32 v39, v39\n" \
  "v_exp_f32 v40, v40\n" \
  "v_exp_f32 v41, v41\n" \
  "v_smfmac_f32_32x32x32_bf16 v[0:15], v[92:95], v[32:39], v242\n" \
  "v_exp_f32 v42, v42\n" \
  "v_exp_f32 v43, v43\n" \
  "v_exp_f32 v44, v44\n" \
  "v_exp_f32 v45, v45\n" \
  "v_exp_f32 v46, v46\n" \
  "v_exp_f32 v47, v47\n" \
  "v_exp_f32 v48, v48\n" \
  "v_exp_f32 v49, v49\n" \
  "v_smfmac_f32_32x32x32_bf16 v[0:15], v[96:99], v[40:47], v242\n" \
  "v_exp_f32 v50, v50\n" \
  "v_exp_f32 v51, v51\n" \
  "v_exp_f32 v52, v52\n" \
  "v_mfma_f32_32x32x16_bf16 v[32:47], v[100:103], v[228:231], 0\n" \
  "v_exp_f32 v53, v53\n" \
  "v_exp_f32 v54, v54\n" \
  "v_exp_f32 v55, v55\n" \
  "v_exp_f32 v56, v56\n" \
  "v_exp_f32 v57, v57\n" \
  "v_smfmac_f32_32x32x32_bf16 v[16:31], v[92:95], v[48:55], v242\n" \
  "v_exp_f32 v58, v58\n" \
  "v_exp_f32 v59, v59\n" \
  "v_exp_f32 v60, v60\n" \
  "v_exp_f32 v61, v61\n" \
  "v_exp_f32 v62, v62\n" \
  "v_exp_f32 v63, v63\n" \
  "v_exp_f32 v32, v32\n" \
  "v_exp_f32 v33, v33\n" \
  "v_smfmac_f32_32x32x32_bf16 v[16:31], v[96:99], v[56:63], v242\n" \
  "v_exp_f32 v34, v34\n" \
  "v_exp_f32 v35, v35\n" \
  "v_exp_f32 v36, v36\n" \
  "v_mfma_f32_32x32x16_bf16 v[48:63], v[100:103], v[232:235], 0\n" \
  "v_exp_f32 v37, v37\n" \
  "v_exp_f32 v38, v38\n" \
  "v_exp_f32 v39, v39\n" \
  "v_exp_f32 v40, v40\n" \
  "v_exp_f32 v41, v41\n" \
  "v_smfmac_f32_32x32x32_bf16 v[0:15], v[104:107], v[32:39], v242\n" \
  "v_exp_f32 v42, v42\n" \
  "v_exp_f32 v43, v43\n" \
  "v_exp_f32 v44, v44\n" \
  "v_exp_f32 v45, v45\n" \
  "v_exp_f32 v46, v46\n" \
  "v_exp_f32 v47, v47\n" \
  "v_exp_f32 v48, v48\n" \
  "v_exp_f32 v49, v49\n" \
  "v_smfmac_f32_32x32x32_bf16 v[0:15], v[108:111], v[40:47], v242\n" \
  "v_exp_f32 v50, v50\n" \
  "v_exp_f32 v51, v51\n" \
  "v_exp_f32 v52, v52\n" \
  "s_waitcnt vmcnt(16)\n" \
  "v_mfma_f32_32x32x16_bf16 v[32:47], v[112:115], v[228:231], 0\n" \
  "v_exp_f32 v53, v53\n" \
  "v_exp_f32 v54, v54\n" \
  "v_exp_f32 v55, v55\n" \
  "v_exp_f32 v56, v56\n" \
  "v_exp_f32 v57, v57\n" \
  "v_smfmac_f32_32x32x32_bf16 v[16:31], v[104:107], v[48:55], v242\n" \
  "v_exp_f32 v58, v58\n" \
  "v_exp_f32 v59, v59\n" \
  "v_exp_f32 v60, v60\n" \
  "v_exp_f32 v61, v61\n" \
  "v_exp_f32 v62, v62\n" \
  "v_exp_f32 v63, v63\n" \
  "s_setprio 1\n" \
  "v_exp_f32 v32, v32\n" \
  "v_exp_f32 v33, v33\n" \
  "v_smfmac_f32_32x32x32_bf16 v[16:31], v[108:111], v[56:63], v242\n" \
  "v_exp_f32 v34, v34\n" \
  "v_exp_f32 v35, v35\n" \
  "v_exp_f32 v36, v36\n" \
  "v_mfma_f32_32x32x16_bf16 v[48:63], v[112:115], v[232:235], 0\n" \
  "s_add_u32 s77, s77, 4096\n" \
  "buffer_load_dwordx4 v[88:91], v240, s[84:87], s77 offen offset:0 sc1\n" \
  "buffer_load_dwordx4 v[100:103], v240, s[84:87], s77 offen offset:2048 sc1\n" \
  "s_mov_b64 exec, s[82:83]\n" \
  "buffer_load_dwordx4 v[92:95], v241, s[84:87], s77 offen offset:0 sc1\n" \
  "buffer_load_dwordx4 v[96:99], v241, s[84:87], s77 offen offset:512 sc1\n" \
  "buffer_load_dwordx4 v[104:107], v241, s[84:87], s77 offen offset:2048 sc1\n" \
  "buffer_load_dwordx4 v[108:111], v241, s[84:87], s77 offen offset:2560 sc1\n" \
  "s_mov_b64 exec, -1\n" \
  "v_exp_f32 v37, v37\n" \
  "v_exp_f32 v38, v38\n" \
  "v_exp_f32 v39, v39\n" \
  "v_exp_f32 v40, v40\n" \
  "v_exp_f32 v41, v41\n" \
  "v_smfmac_f32_32x32x32_bf16 v[0:15], v[116:119], v[32:39], v242\n" \
  "v_exp_f32 v42, v42\n" \
  "v_exp_f32 v43, v43\n" \
  "v_exp_f32 v44, v44\n" \
  "v_exp_f32 v45, v45\n" \
  "v_exp_f32 v46, v46\n" \
  "v_exp_f32 v47, v47\n" \
  "v_exp_f32 v48, v48\n" \
  "v_exp_f32 v49, v49\n" \
  "v_smfmac_f32_32x32x32_bf16 v[0:15], v[120:123], v[40:47], v242\n" \
  "v_exp_f32 v50, v50\n" \
  "v_exp_f32 v51, v51\n" \
  "v_exp_f32 v52, v52\n" \
  "v_mfma_f32_32x32x16_bf16 v[32:47], v[124:127], v[228:231], 0\n" \
  "v_exp_f32 v53, v53\n" \
  "v_exp_f32 v54, v54\n" \
  "v_exp_f32 v55, v55\n" \
  "v_exp_f32 v56, v56\n" \
  "v_exp_f32 v57, v57\n" \
  "v_smfmac_f32_32x32x32_bf16 v[16:31], v[116:119], v[48:55], v242\n" \
  "v_exp_f32 v58, v58\n" \
  "v_exp_f32 v59, v59\n" \
  "v_exp_f32 v60, v60\n" \
  "v_exp_f32 v61, v61\n" \
  "v_exp_f32 v62, v62\n" \
  "v_exp_f32 v63, v63\n" \
  "v_exp_f32 v32, v32\n" \
  "v_exp_f32 v33, v33\n" \
  "v_smfmac_f32_32x32x32_bf16 v[16:31], v[120:123], v[56:63], v242\n" \
  "v_exp_f32 v34, v34\n" \
  "v_exp_f32 v35, v35\n" \
  "v_exp_f32 v36, v36\n" \
  "v_mfma_f32_32x32x16_bf16 v[48:63], v[124:127], v[232:235], 0\n" \
  "v_exp_f32 v37, v37\n" \
  "v_exp_f32 v38, v38\n" \
  "v_exp_f32 v39, v39\n" \
  "v_exp_f32 v40, v40\n" \
  "v_exp_f32 v41, v41\n" \
  "v_smfmac_f32_32x32x32_bf16 v[0:15], v[128:131], v[32:39], v242\n" \
  "v_exp_f32 v42, v42\n" \
  "v_exp_f32 v43, v43\n" \
  "v_exp_f32 v44, v44\n" \
  "v_exp_f32 v45, v45\n" \
  "v_exp_f32 v46, v46\n" \
  "v_exp_f32 v47, v47\n" \
  "v_exp_f32 v48, v48\n" \
  "v_exp_f32 v49, v49\n" \
  "v_smfmac_f32_32x32x32_bf16 v[0:15], v[132:135], v[40:47], v242\n" \
  "v_exp_f32 v50, v50\n" \
  "v_exp_f32 v51, v51\n" \
  "v_exp_f32 v52, v52\n" \
  "s_waitcnt vmcnt(14)\n" \
  "v_mfma_f32_32x32x16_bf16 v[32:47], v[136:139], v[228:231], 0\n" \
  "s_lshl_b32 s93, s56, 8\n" \
  "s_add_u32 s93, s93, 20480\n" \
  "buffer_load_dword v244, v245, s[84:87], s93 offen sc1\n" \
  "v_exp_f32 v53, v53\n" \
  "v_exp_f32 v54, v54\n" \
  "v_exp_f32 v55, v55\n" \
  "v_exp_f32 v56, v56\n" \
  "v_exp_f32 v57, v57\n" \
  "v_smfmac_f32_32x32x32_bf16 v[16:31], v[128:131], v[48:55], v242\n" \
  "v_exp_f32 v58, v58\n" \
  "v_exp_f32 v59, v59\n" \
  "v_exp_f32 v60, v60\n" \
  "v_exp_f32 v61, v61\n" \
  "v_exp_f32 v62, v62\n" \
  "v_exp_f32 v63, v63\n" \
  "s_setprio 0\n" \
  "v_exp_f32 v32, v32\n" \
  "v_exp_f32 v33, v33\n" \
  "v_smfmac_f32_32x32x32_bf16 v[16:31], v[132:135], v[56:63], v242\n" \
  "v_exp_f32 v34, v34\n" \
  "v_exp_f32 v35, v35\n" \
  "v_exp_f32 v36, v36\n" \
  "v_mfma_f32_32x32x16_bf16 v[48:63], v[136:139], v[232:235], 0\n" \
  "s_add_u32 s77, s77, 4096\n" \
  "buffer_load_dwordx4 v[112:115], v240, s[84:87], s77 offen offset:0 sc1\n" \
  "buffer_load_dwordx4 v[124:127], v240, s[84:87], s77 offen offset:2048 sc1\n" \
  "s_mov_b64 exec, s[82:83]\n" \
  "buffer_load_dwordx4 v[116:119], v241, s[84:87], s77 offen offset:0 sc1\n" \
  "buffer_load_dwordx4 v[120:123], v241, s[84:87], s77 offen offset:512 sc1\n" \
  "buffer_load_dwordx4 v[128:131], v241, s[84:87], s77 offen offset:2048 sc1\n" \
  "buffer_load_dwordx4 v[132:135], v241, s[84:87], s77 offen offset:2560 sc1\n" \
  "s_mov_b64 exec, -1\n" \
  "v_exp_f32 v37, v37\n" \
  "v_exp_f32 v38, v38\n" \
  "v_exp_f32 v39, v39\n" \
  "v_exp_f32 v40, v40\n" \
  "v_exp_f32 v41, v41\n" \
  "v_smfmac_f32_32x32x32_bf16 v[0:15], v[140:143], v[32:39], v242\n" \
  "v_exp_f32 v42, v42\n" \
  "v_exp_f32 v43, v43\n" \
  "v_exp_f32 v44, v44\n" \
  "v_exp_f32 v45, v45\n" \
  "v_exp_f32 v46, v46\n" \
  "v_exp_f32 v47, v47\n" \
  "v_exp_f32 v48, v48\n" \
  "v_exp_f32 v49, v49\n" \
  "v_smfmac_f32_32x32x32_bf16 v[0:15], v[144:147], v[40:47], v242\n" \
  "v_exp_f32 v50, v50\n" \
  "v_exp_f32 v51, v51\n" \
  "v_exp_f32 v52, v52\n" \
  "v_mfma_f32_32x32x16_bf16 v[32:47], v[148:151], v[228:231], 0\n" \
  "v_exp_f32 v53, v53\n" \
  "v_exp_f32 v54, v54\n" \
  "v_exp_f32 v55, v55\n" \
  "v_exp_f32 v56, v56\n" \
  "v_exp_f32 v57, v57\n" \
  "v_smfmac_f32_32x32x32_bf16 v[16:31], v[140:143], v[48:55], v242\n" \
  "v_exp_f32 v58, v58\n" \
  "v_exp_f32 v59, v59\n" \
  "v_exp_f32 v60, v60\n" \
  "v_exp_f32 v61, v61\n" \
  "v_exp_f32 v62, v62\n" \
  "v_exp_f32 v63, v63\n" \
  "v_exp_f32 v32, v32\n" \
  "v_exp_f32 v33, v33\n" \
  "v_smfmac_f32_32x32x32_bf16 v[16:31], v[144:147], v[56:63], v242\n" \
  "v_exp_f32 v34, v34\n" \
  "v_exp_f32 v35, v35\n" \
  "v_exp_f32 v36, v36\n" \
  "v_mfma_f32_32x32x16_bf16 v[48:63], v[148:151], v[232:235], 0\n" \
  "v_exp_f32 v37, v37\n" \
  "v_exp_f32 v38, v38\n" \
  "v_exp_f32 v39, v39\n" \
  "v_exp_f32 v40, v40\n" \
  "v_exp_f32 v41, v41\n" \
  "v_smfmac_f32_32x32x32_bf16 v[0:15], v[152:155], v[32:39], v242\n" \
  "v_exp_f32 v42, v42\n" \
  "v_exp_f32 v43, v43\n" \
  "v_exp_f32 v44, v44\n" \
  "v_exp_f32 v45, v45\n" \
  "v_exp_f32 v46, v46\n" \
  "v_exp_f32 v47, v47\n" \
  "v_exp_f32 v48, v48\n" \
  "v_exp_f32 v49, v49\n" \
  "v_smfmac_f32_32x32x32_bf16 v[0:15], v[156:159], v[40:47], v242\n" \
  "v_exp_f32 v50, v50\n" \
  "v_exp_f32 v51, v51\n" \
  "v_exp_f32 v52, v52\n" \
  "s_waitcnt vmcnt(14)\n" \
  "v_mfma_f32_32x32x16_bf16 v[32:47], v[64:67], v[228:231], 0\n" \
  "v_exp_f32 v53, v53\n" \
  "v_exp_f32 v54, v54\n" \
  "v_exp_f32 v55, v55\n" \
  "v_exp_f32 v56, v56\n" \
  "v_exp_f32 v57, v57\n" \
  "v_smfmac_f32_32x32x32_bf16 v[16:31], v[152:155], v[48:55], v242\n" \
  "v_exp_f32 v58, v58\n" \
  "v_exp_f32 v59, v59\n" \
  "v_exp_f32 v60, v60\n" \
  "v_exp_f32 v61, v61\n" \
  "v_exp_f32 v62, v62\n" \
  "v_exp_f32 v63, v63\n" \
  "s_waitcnt vmcnt(13)\n" \
  "s_waitcnt vmcnt(6)\n" \
  "v_cmp_ne_u32 vcc, v243, v244\n" \
  "s_cbranch_vccnz .La_s0\n" \
  ".La_b0:\n" \
  "s_barrier\n" \
  "s_add_u32 s81, s78, 0x4000\n" \
  "s_cmp_lg_u32 s56, 0\n" \
  "s_cbranch_scc1 .La_f2\n" \
  "s_mov_b64 exec, 1\n" \
  "buffer_store_dwordx2 v[246:247], off, s[84:87], s81 sc1\n" \
  "s_mov_b64 exec, -1\n" \
  ".La_f2:\n" \
  "s_mov_b32 s81, 0\n" \
  "s_or_b32 s88, s78, s56\n" \
  "s_cbranch_scc1 .La_f1\n" \
  "s_mov_b64 exec, 1\n" \
  "buffer_store_dwordx2 v[246:247], off, s[84:87], s81 sc1\n" \
  "s_mov_b64 exec, -1\n" \
  ".La_f1:\n" \
  ".La_r23_1:\n" \
  "s_setprio 3\n" \
  "v_exp_f32 v32, v32\n" \
  "v_exp_f32 v33, v33\n" \
  "v_smfmac_f32_32x32x32_bf16 v[16:31], v[156:159], v[56:63], v242\n" \
  "v_exp_f32 v34, v34\n" \
  "v_exp_f32 v35, v35\n" \
  "v_exp_f32 v36, v36\n" \
  "v_mfma_f32_32x32x16_bf16 v[48:63], v[64:67], v[232:235], 0\n" \
  "s_add_u32 s77, s77, 4096\n" \
  "buffer_load_dwordx4 v[136:139], v240, s[84:87], s77 offen offset:0 sc1\n" \
  "buffer_load_dwordx4 v[148:151], v240, s[84:87], s77 offen offset:2048 sc1\n" \
  "s_mov_b64 exec, s[82:83]\n" \
  "buffer_load_dwordx4 v[140:143], v241, s[84:87], s77 offen offset:0 sc1\n" \
  "buffer_load_dwordx4 v[144:147], v241, s[84:87], s77 offen offset:512 sc1\n" \
  "buffer_load_dwordx4 v[152:155], v241, s[84:87], s77 offen offset:2048 sc1\n" \
  "buffer_load_dwordx4 v[156:159], v241, s[84:87], s77 offen offset:2560 sc1\n" \
  "s_mov_b64 exec, -1\n" \
  "v_exp_f32 v37, v37\n" \
  "v_exp_f32 v38, v38\n" \
  "v_exp_f32 v39, v39\n" \
  "v_exp_f32 v40, v40\n" \
  "v_exp_f32 v41, v41\n" \
  "v_smfmac_f32_32x32x32_bf16 v[0:15], v[68:71], v[32:39], v242\n" \
  "v_exp_f32 v42, v42\n" \
  "v_exp_f32 v43, v43\n" \
  "v_exp_f32 v44, v44\n" \
  "v_exp_f32 v45, v45\n" \
  "v_exp_f32 v46, v46\n" \
  "v_exp_f32 v47, v47\n" \
  "v_exp_f32 v48, v48\n" \
  "v_exp_f32 v49, v49\n" \
  "v_smfmac_f32_32x32x32_bf16 v[0:15], v[72:75], v[40:47], v242\n" \
  "v_exp_f32 v50, v50\n" \
  "v_exp_f32 v51, v51\n" \
  "v_exp_f32 v52, v52\n" \
  "v_mfma_f32_32x32x16_bf16 v[32:47], v[76:79], v[228:231], 0\n" \
  "v_exp_f32 v53, v53\n" \
  "v_exp_f32 v54, v54\n" \
  "v_exp_f32 v55, v55\n" \
  "v_exp_f32 v56, v56\n" \
  "v_exp_f32 v57, v57\n" \
  "v_smfmac_f32_32x32x32_bf16 v[16:31], v[68:71], v[48:55], v242\n" \
  "v_exp_f32 v58, v58\n" \
  "v_exp_f32 v59, v59\n" \
  "v_exp_f32 v60, v60\n" \
  "v_exp_f32 v61, v61\n" \
  "v_exp_f32 v62, v62\n" \
  "v_exp_f32 v63, v63\n" \
  "v_exp_f32 v32, v32\n" \
  "v_exp_f32 v33, v33\n" \
  "v_smfmac_f32_32x32x32_bf16 v[16:31], v[72:75], v[56:63], v242\n" \
  "v_exp_f32 v34, v34\n" \
  "v_exp_f32 v35, v35\n" \
  "v_exp_f32 v36, v36\n" \
  "v_mfma_f32_32x32x16_bf16 v[48:63], v[76:79], v[232:235], 0\n" \
  "s_add_u32 m0, s91, 0\n" \
  "s_add_u32 s81, s75, 0\n" \
  "buffer_load_dwordx4 v248, s[84:87], s81 offen offset:0 sc1 lds\n" \
  "buffer_load_dwordx4 v248, s[84:87], s81 offen offset:1024 sc1 lds\n" \
  "buffer_load_dwordx4 v248, s[84:87], s81 offen offset:2048 sc1 lds\n" \
  "buffer_load_dwordx4 v248, s[84:87], s81 offen offset:3072 sc1 lds\n" \
  "v_exp_f32 v37, v37\n" \
  "v_exp_f32 v38, v38\n" \
  "v_exp_f32 v39, v39\n" \
  "v_exp_f32 v40, v40\n" \
  "v_exp_f32 v41, v41\n" \
  "v_smfmac_f32_32x32x32_bf16 v[0:15], v[80:83], v[32:39], v242\n" \
  "v_exp_f32 v42, v42\n" \
  "v_exp_f32 v43, v43\n" \
  "v_exp_f32 v44, v44\n" \
  "v_exp_f32 v45, v45\n" \
  "v_exp_f32 v46, v46\n" \
  "v_exp_f32 v47, v47\n" \
  "v_exp_f32 v48, v48\n" \
  "v_exp_f32 v49, v49\n" \
  "v_smfmac_f32_32x32x32_bf16 v[0:15], v[84:87], v[40:47], v242\n" \
  "v_exp_f32 v50, v50\n" \
  "v_exp_f32 v51, v51\n" \
  "v_exp_f32 v52, v52\n" \
  "v_mfma_f32_32x32x16_bf16 v[32:47], v[88:91], v[228:231], 0\n" \
  "v_exp_f32 v53, v53\n" \
  "v_exp_f32 v54, v54\n" \
  "v_exp_f32 v55, v55\n" \
  "v_exp_f32 v56, v56\n" \
  "v_exp_f32 v57, v57\n" \
  "v_smfmac_f32_32x32x32_bf16 v[16:31], v[80:83], v[48:55], v242\n" \
  "v_exp_f32 v58, v58\n" \
  "v_exp_f32 v59, v59\n" \
  "v_exp_f32 v60, v60\n" \
  "v_exp_f32 v61, v61\n" \
  "v_exp_f32 v62, v62\n" \
  "v_exp_f32 v63, v63\n" \
  "s_setprio 2\n" \
  "v_exp_f32 v32, v32\n" \
  "v_exp_f32 v33, v33\n" \
  "v_smfmac_f32_32x32x32_bf16 v[16:31], v[84:87], v[56:63], v242\n" \
  "v_exp_f32 v34, v34\n" \
  "v_exp_f32 v35, v35\n" \
  "v_exp_f32 v36, v36\n" \
  "v_mfma_f32_32x32x16_bf16 v[48:63], v[88:91], v[232:235], 0\n" \
  "s_add_u32 m0, s91, 4096\n" \
  "s_add_u32 s81, s75, 4096\n" \
  "buffer_load_dwordx4 v248, s[84:87], s81 offen offset:0 sc1 lds\n" \
  "buffer_load_dwordx4 v248, s[84:87], s81 offen offset:1024 sc1 lds\n" \
  "buffer_load_dwordx4 v248, s[84:87], s81 offen offset:2048 sc1 lds\n" \
  "buffer_load_dwordx4 v248, s[84:87], s81 offen offset:3072 sc1 lds\n" \
  "v_exp_f32 v37, v37\n" \
  "v_exp_f32 v38, v38\n" \
  "v_exp_f32 v39, v39\n" \
  "v_exp_f32 v40, v40\n" \
  "v_exp_f32 v41, v41\n" \
  "v_smfmac_f32_32x32x32_bf16 v[0:15], v[92:95], v[32:39], v242\n" \
  "v_exp_f32 v42, v42\n" \
  "v_exp_f32 v43, v43\n" \
  "v_exp_f32 v44, v44\n" \
  "v_exp_f32 v45, v45\n" \
  "v_exp_f32 v46, v46\n" \
  "v_exp_f32 v47, v47\n" \
  "v_exp_f32 v48, v48\n" \
  "v_exp_f32 v49, v49\n" \
  "v_smfmac_f32_32x32x32_bf16 v[0:15], v[96:99], v[40:47], v242\n" \
  "v_exp_f32 v50, v50\n" \
  "v_exp_f32 v51, v51\n" \
  "v_exp_f32 v52, v52\n" \
  "v_mfma_f32_32x32x16_bf16 v[32:47], v[100:103], v[228:231], 0\n" \
  "v_exp_f32 v53, v53\n" \
  "v_exp_f32 v54, v54\n" \
  "v_exp_f32 v55, v55\n" \
  "v_exp_f32 v56, v56\n" \
  "v_exp_f32 v57, v57\n" \
  "v_smfmac_f32_32x32x32_bf16 v[16:31], v[92:95], v[48:55], v242\n" \
  "v_exp_f32 v58, v58\n" \
  "v_exp_f32 v59, v59\n" \
  "v_exp_f32 v60, v60\n" \
  "v_exp_f32 v61, v61\n" \
  "v_exp_f32 v62, v62\n" \
  "v_exp_f32 v63, v63\n" \
  "v_exp_f32 v32, v32\n" \
  "v_exp_f32 v33, v33\n" \
  "v_smfmac_f32_32x32x32_bf16 v[16:31], v[96:99], v[56:63], v242\n" \
  "v_exp_f32 v34, v34\n" \
  "v_exp_f32 v35, v35\n" \
  "v_exp_f32 v36, v36\n" \
  "v_mfma_f32_32x32x16_bf16 v[48:63], v[100:103], v[232:235], 0\n" \
  "s_add_u32 m0, s91, 65536\n" \
  "s_add_u32 s81, s75, 524288\n" \
  "buffer_load_dwordx4 v248, s[84:87], s81 offen offset:0 sc1 lds\n" \
  "buffer_load_dwordx4 v248, s[84:87], s81 offen offset:1024 sc1 lds\n" \
  "buffer_load_dwordx4 v248, s[84:87], s81 offen offset:2048 sc1 lds\n" \
  "buffer_load_dwordx4 v248, s[84:87], s81 offen offset:3072 sc1 lds\n" \
  "v_exp_f32 v37, v37\n" \
  "v_exp_f32 v38, v38\n" \
  "v_exp_f32 v39, v39\n" \
  "v_exp_f32 v40, v40\n" \
  "v_exp_f32 v41, v41\n" \
  "v_smfmac_f32_32x32x32_bf16 v[0:15], v[104:107], v[32:39], v242\n" \
  "v_exp_f32 v42, v42\n" \
  "v_exp_f32 v43, v43\n" \
  "v_exp_f32 v44, v44\n" \
  "v_exp_f32 v45, v45\n" \
  "v_exp_f32 v46, v46\n" \
  "v_exp_f32 v47, v47\n" \
  "v_exp_f32 v48, v48\n" \
  "v_exp_f32 v49, v49\n" \
  "v_smfmac_f32_32x32x32_bf16 v[0:15], v[108:111], v[40:47], v242\n" \
  "v_exp_f32 v50, v50\n" \
  "v_exp_f32 v51, v51\n" \
  "v_exp_f32 v52, v52\n" \
  "s_waitcnt vmcnt(18)\n" \
  "v_mfma_f32_32x32x16_bf16 v[32:47], v[112:115], v[228:231], 0\n" \
  "v_exp_f32 v53, v53\n" \
  "v_exp_f32 v54, v54\n" \
  "v_exp_f32 v55, v55\n" \
  "v_exp_f32 v56, v56\n" \
  "v_exp_f32 v57, v57\n" \
  "v_smfmac_f32_32x32x32_bf16 v[16:31], v[104:107], v[48:55], v242\n" \
  "v_exp_f32 v58, v58\n" \
  "v_exp_f32 v59, v59\n" \
  "v_exp_f32 v60, v60\n" \
  "v_exp_f32 v61, v61\n" \
  "v_exp_f32 v62, v62\n" \
  "v_exp_f32 v63, v63\n" \
  "s_setprio 1\n" \
  "v_exp_f32 v32, v32\n" \
  "v_exp_f32 v33, v33\n" \
  "v_smfmac_f32_32x32x32_bf16 v[16:31], v[108:111], v[56:63], v242\n" \
  "v_exp_f32 v34, v34\n" \
  "v_exp_f32 v35, v35\n" \
  "v_exp_f32 v36, v36\n" \
  "v_mfma_f32_32x32x16_bf16 v[48:63], v[112:115], v[232:235], 0\n" \
  "s_add_u32 m0, s91, 69632\n" \
  "s_add_u32 s81, s75, 528384\n" \
  "buffer_load_dwordx4 v248, s[84:87], s81 offen offset:0 sc1 lds\n" \
  "buffer_load_dwordx4 v248, s[84:87], s81 offen offset:1024 sc1 lds\n" \
  "buffer_load_dwordx4 v248, s[84:87], s81 offen offset:2048 sc1 lds\n" \
  "buffer_load_dwordx4 v248, s[84:87], s81 offen offset:3072 sc1 lds\n" \
  "v_exp_f32 v37, v37\n" \
  "v_exp_f32 v38, v38\n" \
  "v_exp_f32 v39, v39\n" \
  "v_exp_f32 v40, v40\n" \
  "v_exp_f32 v41, v41\n" \
  "v_smfmac_f32_32x32x32_bf16 v[0:15], v[116:119], v[32:39], v242\n" \
  "v_exp_f32 v42, v42\n" \
  "v_exp_f32 v43, v43\n" \
  "v_exp_f32 v44, v44\n" \
  "v_exp_f32 v45, v45\n" \
  "v_exp_f32 v46, v46\n" \
  "v_exp_f32 v47, v47\n" \
  "v_exp_f32 v48, v48\n" \
  "v_exp_f32 v49, v49\n" \
  "v_smfmac_f32_32x32x32_bf16 v[0:15], v[120:123], v[40:47], v242\n" \
  "v_exp_f32 v50, v50\n" \
  "v_exp_f32 v51, v51\n" \
  "v_exp_f32 v52, v52\n" \
  "v_mfma_f32_32x32x16_bf16 v[32:47], v[124:127], v[228:231], 0\n" \
  "v_and_b32 v168, 0xf0, v248\n" \
  "v_lshrrev_b32 v169, 8, v248\n" \
  "v_lshlrev_b32 v168, 6, v168\n" \
  "v_lshl_or_b32 v168, v169, 5, v168\n" \
  "global_load_dwordx4 v[160:163], v168, s[52:53]\n" \
  "global_load_dwordx4 v[164:167], v168, s[52:53] offset:16\n" \
  "v_mov_b32 v170, 131584\n" \
  "v_mov_b64 v[172:173], 0\n" \
  "v_mov_b64 v[174:175], 0\n" \
  "ds_write_b128 v170, v[172:175]\n" \
  "ds_write_b128 v170, v[172:175] offset:16\n" \
  "v_exp_f32 v53, v53\n" \
  "v_exp_f32 v54, v54\n" \
  "v_exp_f32 v55, v55\n" \
  "v_exp_f32 v56, v56\n" \
  "v_exp_f32 v57, v57\n" \
  "v_smfmac_f32_32x32x32_bf16 v[16:31], v[116:119], v[48:55], v242\n" \
  "v_exp_f32 v58, v58\n" \
  "v_exp_f32 v59, v59\n" \
  "v_exp_f32 v60, v60\n" \
  "v_exp_f32 v61, v61\n" \
  "v_exp_f32 v62, v62\n" \
  "v_exp_f32 v63, v63\n" \
  "v_exp_f32 v32, v32\n" \
  "v_exp_f32 v33, v33\n" \
  "v_smfmac_f32_32x32x32_bf16 v[16:31], v[120:123], v[56:63], v242\n" \
  "v_exp_f32 v34, v34\n" \
  "v_exp_f32 v35, v35\n" \
  "v_exp_f32 v36, v36\n" \
  "v_mfma_f32_32x32x16_bf16 v[48:63], v[124:127], v[232:235], 0\n" \
  "v_exp_f32 v37, v37\n" \
  "v_exp_f32 v38, v38\n" \
  "v_exp_f32 v39, v39\n" \
  "v_exp_f32 v40, v40\n" \
  "v_exp_f32 v41, v41\n" \
  "v_smfmac_f32_32x32x32_bf16 v[0:15], v[128:131], v[32:39], v242\n" \
  "v_exp_f32 v42, v42\n" \
  "v_exp_f32 v43, v43\n" \
  "v_exp_f32 v44, v44\n" \
  "v_exp_f32 v45, v45\n" \
  "v_exp_f32 v46, v46\n" \
  "v_exp_f32 v47, v47\n" \
  "v_exp_f32 v48, v48\n" \
  "v_exp_f32 v49, v49\n" \
  "v_smfmac_f32_32x32x32_bf16 v[0:15], v[132:135], v[40:47], v242\n" \
  "v_exp_f32 v50, v50\n" \
  "v_exp_f32 v51, v51\n" \
  "v_exp_f32 v52, v52\n" \
  "s_waitcnt vmcnt(18)\n" \
  "v_mfma_f32_32x32x16_bf16 v[32:47], v[136:139], v[228:231], 0\n" \
  "v_exp_f32 v53, v53\n" \
  "v_exp_f32 v54, v54\n" \
  "v_exp_f32 v55, v55\n" \
  "v_exp_f32 v56, v56\n" \
  "v_exp_f32 v57, v57\n" \
  "v_smfmac_f32_32x32x32_bf16 v[16:31], v[128:131], v[48:55], v242\n" \
  "v_exp_f32 v58, v58\n" \
  "v_exp_f32 v59, v59\n" \
  "v_exp_f32 v60, v60\n" \
  "v_exp_f32 v61, v61\n" \
  "v_exp_f32 v62, v62\n" \
  "v_exp_f32 v63, v63\n" \
  "s_setprio 0\n" \
  "v_exp_f32 v32, v32\n" \
  "v_exp_f32 v33, v33\n" \
  "v_smfmac_f32_32x32x32_bf16 v[16:31], v[132:135], v[56:63], v242\n" \
  "v_exp_f32 v34, v34\n" \
  "v_exp_f32 v35, v35\n" \
  "v_exp_f32 v36, v36\n" \
  "v_mfma_f32_32x32x16_bf16 v[48:63], v[136:139], v[232:235], 0\n" \
  "v_exp_f32 v37, v37\n" \
  "v_exp_f32 v38, v38\n" \
  "v_exp_f32 v39, v39\n" \
  "v_exp_f32 v40, v40\n" \
  "v_exp_f32 v41, v41\n" \
  "v_smfmac_f32_32x32x32_bf16 v[0:15], v[140:143], v[32:39], v242\n" \
  "v_exp_f32 v42, v42\n" \
  "v_exp_f32 v43, v43\n" \
  "v_exp_f32 v44, v44\n" \
  "v_exp_f32 v45, v45\n" \
  "v_exp_f32 v46, v46\n" \
  "v_exp_f32 v47, v47\n" \
  "v_exp_f32 v48, v48\n" \
  "v_exp_f32 v49, v49\n" \
  "v_smfmac_f32_32x32x32_bf16 v[0:15], v[144:147], v[40:47], v242\n" \
  "v_exp_f32 v50, v50\n" \
  "v_exp_f32 v51, v51\n" \
  "v_exp_f32 v52, v52\n" \
  "v_mfma_f32_32x32x16_bf16 v[32:47], v[148:151], v[228:231], 0\n" \
  "s_lshl_b32 s93, s56, 8\n" \
  "s_add_u32 s93, s93, 16384\n" \
  "buffer_load_dword v244, v245, s[84:87], s93 offen sc1\n" \
  "v_exp_f32 v53, v53\n" \
  "v_exp_f32 v54, v54\n" \
  "v_exp_f32 v55, v55\n" \
  "v_exp_f32 v56, v56\n" \
  "v_exp_f32 v57, v57\n" \
  "v_smfmac_f32_32x32x32_bf16 v[16:31], v[140:143], v[48:55], v242\n" \
  "v_exp_f32 v58, v58\n" \
  "v_exp_f32 v59, v59\n" \
  "v_exp_f32 v60, v60\n" \
  "v_exp_f32 v61, v61\n" \
  "v_exp_f32 v62, v62\n" \
  "v_exp_f32 v63, v63\n" \
  "v_exp_f32 v32, v32\n" \
  "v_exp_f32 v33, v33\n" \
  "v_smfmac_f32_32x32x32_bf16 v[16:31], v[144:147], v[56:63], v242\n" \
  "v_exp_f32 v34, v34\n" \
  "v_exp_f32 v35, v35\n" \
  "v_exp_f32 v36, v36\n" \
  "v_mfma_f32_32x32x16_bf16 v[48:63], v[148:151], v[232:235], 0\n" \
  "v_exp_f32 v37, v37\n" \
  "v_exp_f32 v38, v38\n" \
  "v_exp_f32 v39, v39\n" \
  "v_exp_f32 v40, v40\n" \
  "v_exp_f32 v41, v41\n" \
  "v_smfmac_f32_32x32x32_bf16 v[0:15], v[152:155], v[32:39], v242\n" \
  "v_exp_f32 v42, v42\n" \
  "v_exp_f32 v43, v43\n" \
  "v_exp_f32 v44, v44\n" \
  "v_exp_f32 v45, v45\n" \
  "v_exp_f32 v46, v46\n" \
  "v_exp_f32 v47, v47\n" \
  "v_exp_f32 v48, v48\n" \
  "v_exp_f32 v49, v49\n" \
  "v_smfmac_f32_32x32x32_bf16 v[0:15], v[156:159], v[40:47], v242\n" \
  "v_exp_f32 v50, v50\n" \
  "v_exp_f32 v51, v51\n" \
  "v_exp_f32 v52, v52\n" \
  "v_exp_f32 v53, v53\n" \
  "v_exp_f32 v54, v54\n" \
  "v_exp_f32 v55, v55\n" \
  "v_exp_f32 v56, v56\n" \
  "v_exp_f32 v57, v57\n" \
  "v_smfmac_f32_32x32x32_bf16 v[16:31], v[152:155], v[48:55], v242\n" \
  "v_exp_f32 v58, v58\n" \
  "v_exp_f32 v59, v59\n" \
  "v_exp_f32 v60, v60\n" \
  "v_exp_f32 v61, v61\n" \
  "v_exp_f32 v62, v62\n" \
  "v_exp_f32 v63, v63\n" \
  "s_nop 1\n" \
  "v_smfmac_f32_32x32x32_bf16 v[16:31], v[156:159], v[56:63], v242\n" \
  "s_mul_i32 s88, s56, 2304\n" \
  "s_xor_b32 s89, s56, 1\n" \
  "s_mul_i32 s89, s89, 2304\n" \
  "v_add_u32 v246, 132096, v248\n" \
  "v_add_u32 v247, 134144, v245\n" \
  "v_add_u32 v32, s88, v246\n" \
  "v_add_u32 v33, s88, v247\n" \
  "v_add_u32 v246, s89, v246\n" \
  "v_add_u32 v247, s89, v247\n" \
  "s_waitcnt vmcnt(0)\n" \
  "v_cmp_ne_u32 vcc, v243, v244\n" \
  "s_cbranch_vccnz .La_s1\n" \
  ".La_b1:\n" \
  "s_bitcmp1_b32 s56, 0\n" \
  "s_cbranch_scc1 .La_k1\n" \
  "s_nop 9\n" \
  "ds_write_b128 v32, v[16:19]\n" \
  "ds_write_b128 v32, v[20:23] offset:1024\n" \
  "ds_write_b32 v33, v24\n" \
  "s_waitcnt lgkmcnt(0)\n" \
  "s_barrier\n" \
  "ds_read_b128 v[36:39], v246\n" \
  "ds_read_b128 v[40:43], v246 offset:1024\n" \
  "ds_read_b32 v44, v247\n" \
  "s_movk_i32 s92, 0x1f8\n" \
  "s_mov_b32 s93, 0x53800000\n" \
  "s_waitcnt lgkmcnt(0)\n" \
  "v_add_f32 v8, v8, v44\n" \
  "v_pk_add_f32 v[0:1], v[0:1], v[36:37]\n" \
  "v_pk_add_f32 v[2:3], v[2:3], v[38:39]\n" \
  "v_rcp_f32 v46, v8\n" \
  "v_pk_add_f32 v[4:5], v[4:5], v[40:41]\n" \
  "v_pk_add_f32 v[6:7], v[6:7], v[42:43]\n" \
  "v_pk_add_f32 v[10:11], v[0:1], v[2:3]\n" \
  "v_pk_mul_f32 v[48:49], v[46:47], v[0:1] op_sel_hi:[0,1]\n" \
  "v_pk_add_f32 v[36:37], v[4:5], v[6:7]\n" \
  "v_pk_mul_f32 v[50:51], v[46:47], v[2:3] op_sel_hi:[0,1]\n" \
  "v_cmp_lt_f32 vcc, 0x2b800000, v8\n" \
  "v_pk_add_f32 v[10:11], v[10:11], v[36:37]\n" \
  "v_pk_mul_f32 v[52:53], v[46:47], v[4:5] op_sel_hi:[0,1]\n" \
  "v_cmp_gt_f32_e64 s[96:97], s93, v8\n" \
  "v_add_f32 v10, v10, v11\n" \
  "v_pk_mul_f32 v[54:55], v[46:47], v[6:7] op_sel_hi:[0,1]\n" \
  "v_cmp_class_f32_e64 s[88:89], v10, s92\n" \
  "v_cvt_pk_bf16_f32 v12, v48, v49\n" \
  "v_cvt_pk_bf16_f32 v13, v50, v51\n" \
  "s_and_b64 s[96:97], s[96:97], vcc\n" \
  "v_cvt_pk_bf16_f32 v14, v52, v53\n" \
  "v_cvt_pk_bf16_f32 v15, v54, v55\n" \
  "s_and_b64 s[88:89], s[88:89], s[96:97]\n" \
  "v_min_u32 v12, -2, v12\n" \
  "v_min_u32 v13, -2, v13\n" \
  "s_cmp_eq_u64 s[88:89], -1\n" \
  "v_min_u32 v14, -2, v14\n" \
  "v_min_u32 v15, -2, v15\n" \
  "s_cbranch_scc0 .La_end\n" \
  "buffer_store_dwordx4 v[12:15], v251, s[84:87], 0 offen sc1\n" \
  "s_branch .La_end\n" \
  "; STUBS\n" \
  ".La_s9:\n" \
  "s_mov_b32 s94, 200000\n" \
  ".La_l9:\n" \
  "s_sleep 8\n" \
  "buffer_load_dwordx2 v[196:197], v206, s[84:87], 0 offen sc1 ; RF\n" \
  "s_waitcnt vmcnt(0) ; RF\n" \
  "v_cmp_ne_u64 vcc, s[68:69], v[196:197] ; RF\n" \
  "s_and_b64 s[88:89], vcc, exec\n" \
  "s_cbranch_scc0 .La_b9\n" \
  "s_sub_u32 s94, s94, 1\n" \
  "s_cbranch_scc0 .La_l9\n" \
  "s_branch .La_b9\n" \
  ".La_s0:\n" \
  "s_mov_b32 s92, 200000\n" \
  ".La_l0:\n" \
  "s_sleep 4\n" \
  "buffer_load_dword v244, v245, s[84:87], s93 offen sc1 ; RF\n" \
  "s_waitcnt vmcnt(0) ; RF\n" \
  "v_cmp_ne_u32 vcc, v243, v244 ; RF\n" \
  "s_cbranch_vccz .La_b0\n" \
  "s_sub_u32 s92, s92, 1\n" \
  "s_cbranch_scc0 .La_l0\n" \
  "s_branch .La_b0\n" \
  ".La_s1:\n" \
  "s_mov_b32 s92, 200000\n" \
  ".La_l1:\n" \
  "s_sleep 4\n" \
  "buffer_load_dword v244, v245, s[84:87], s93 offen sc1 ; RF\n" \
  "s_waitcnt vmcnt(0) ; RF\n" \
  "v_cmp_ne_u32 vcc, v243, v244 ; RF\n" \
  "s_cbranch_vccz .La_b1\n" \
  "s_sub_u32 s92, s92, 1\n" \
  "s_cbranch_scc0 .La_l1\n" \
  "s_branch .La_b1\n" \
  ".La_k1: ; ENDSTUBS\n" \
  "ds_write_b128 v32, v[0:3]\n" \
  "ds_write_b128 v32, v[4:7] offset:1024\n" \
  "ds_write_b32 v33, v8\n" \
  "v_mov_b64 v[228:229], v[232:233]\n" \
  "v_mov_b64 v[230:231], v[234:235]\n" \
  "s_waitcnt lgkmcnt(0)\n" \
  "s_barrier\n" \
  "ds_read_b128 v[36:39], v246\n" \
  "ds_read_b128 v[40:43], v246 offset:1024\n" \
  "ds_read_b32 v44, v247\n" \
  "s_movk_i32 s92, 0x1f8\n" \
  "s_mov_b32 s93, 0x53800000\n" \
  "s_waitcnt lgkmcnt(0)\n" \
  "v_add_f32 v8, v24, v44\n" \
  "v_pk_add_f32 v[0:1], v[16:17], v[36:37]\n" \
  "v_pk_add_f32 v[2:3], v[18:19], v[38:39]\n" \
  "v_rcp_f32 v46, v8\n" \
  "v_pk_add_f32 v[4:5], v[20:21], v[40:41]\n" \
  "v_pk_add_f32 v[6:7], v[22:23], v[42:43]\n" \
  "v_pk_add_f32 v[10:11], v[0:1], v[2:3]\n" \
  "v_pk_mul_f32 v[48:49], v[46:47], v[0:1] op_sel_hi:[0,1]\n" \
  "v_pk_add_f32 v[36:37], v[4:5], v[6:7]\n" \
  "v_pk_mul_f32 v[50:51], v[46:47], v[2:3] op_sel_hi:[0,1]\n" \
  "v_cmp_lt_f32 vcc, 0x2b800000, v8\n" \
  "v_pk_add_f32 v[10:11], v[10:11], v[36:37]\n" \
  "v_pk_mul_f32 v[52:53], v[46:47], v[4:5] op_sel_hi:[0,1]\n" \
  "v_cmp_gt_f32_e64 s[96:97], s93, v8\n" \
  "v_add_f32 v10, v10, v11\n" \
  "v_pk_mul_f32 v[54:55], v[46:47], v[6:7] op_sel_hi:[0,1]\n" \
  "v_cmp_class_f32_e64 s[88:89], v10, s92\n" \
  "v_cvt_pk_bf16_f32 v12, v48, v49\n" \
  "v_cvt_pk_bf16_f32 v13, v50, v51\n" \
  "s_and_b64 s[96:97], s[96:97], vcc\n" \
  "v_cvt_pk_bf16_f32 v14, v52, v53\n" \
  "v_cvt_pk_bf16_f32 v15, v54, v55\n" \
  "s_and_b64 s[88:89], s[88:89], s[96:97]\n" \
  "v_min_u32 v12, -2, v12\n" \
  "v_min_u32 v13, -2, v13\n" \
  "s_cmp_eq_u64 s[88:89], -1\n" \
  "v_min_u32 v14, -2, v14\n" \
  "v_min_u32 v15, -2, v15\n" \
  "s_cbranch_scc0 .La_end\n" \
  "buffer_store_dwordx4 v[12:15], v251, s[84:87], 0 offen sc1\n" \
  ".La_end:\n"
#define ATT_CLOB "v16", "v17", "v18", "v19", "v20", "v21", "v22", "v23", "v24", "v25", "v26", "v27", "v28", "v29", "v30", "v31", "v32", "v33", "v34", "v35", "v36", "v37", "v38", "v39", "v40", "v41", "v42", "v43", "v44", "v45", "v46", "v47", "v48", "v49", "v50", "v51", "v52", "v53", "v54", "v55", "v56", "v57", "v58", "v59", "v60", "v61", "v62", "v63", "v64", "v65", "v66", "v67", "v68", "v69", "v70", "v71", "v72", "v73", "v74", "v75", "v76", "v77", "v78", "v79", "v80", "v82", "v83", "v84", "v85", "v86", "v87", "v88", "v89", "v90", "v91", "v92", "v93", "v94", "v95", "v96", "v97", "v98", "v99", "v100", "v101", "v102", "v103", "v104", "v105", "v106", "v107", "v108", "v109", "v110", "v111", "v112", "v113", "v114", "v115", "v116", "v117", "v118", "v119", "v120", "v121", "v122", "v123", "v124", "v125", "v126", "v127", "v128", "v129", "v130", "v131", "v132", "v133", "v134", "v135", "v136", "v137", "v138", "v139", "v140", "v141", "v142", "v143", "v144", "v145", "v146", "v147", "v148", "v149", "v150", "v151", "v152", "v153", "v154", "v155", "v156", "v157", "v158", "v159", "v196", "v197", "v198", "v199", "v200", "v201", "v202", "v203", "v204", "v205", "v206", "v207", "v208", "v209", "v210", "v211", "v212", "v213", "v214", "v215", "v216", "v217", "v218", "v219", "v220", "v221", "v222", "v223", "v224", "v225", "v226", "v227", "v232", "v233", "v234", "v235", "v236", "v237", "v238", "v239", "v242", "v243", "v244", "v245", "v246", "v247", "s77", "s80", "s81", "s82", "s83", "s90", "s91", "s92", "s93", "s94", "s95", "s96", "s97", "s98", "s99", "s40", "s41", "s42", "s43", "s44", "s45", "s46", "s47", "s48", "s49", "s50", "s51", "s52", "s53", "s54", "s55", "s57", "s58", "s59", "s60", "s61", "s62", "s63", "s64", "s65", "s66", "s67"
#define POST_ASM \
  "v_add_u32 v249, 0x10000, v248\n" \
  "s_cmp_lg_u32 s40, 0\n" \
  "s_cbranch_scc1 .Lp_e\n" \
  "v_lshlrev_b32 v0, 16, v160\n" \
  "v_and_b32 v1, 0xffff0000, v160\n" \
  "v_lshlrev_b32 v2, 16, v161\n" \
  "v_and_b32 v3, 0xffff0000, v161\n" \
  "v_lshlrev_b32 v4, 16, v162\n" \
  "v_and_b32 v5, 0xffff0000, v162\n" \
  "v_lshlrev_b32 v6, 16, v163\n" \
  "v_and_b32 v7, 0xffff0000, v163\n" \
  "v_lshlrev_b32 v8, 16, v164\n" \
  "v_and_b32 v9, 0xffff0000, v164\n" \
  "v_lshlrev_b32 v10, 16, v165\n" \
  "v_and_b32 v11, 0xffff0000, v165\n" \
  "v_lshlrev_b32 v12, 16, v166\n" \
  "v_and_b32 v13, 0xffff0000, v166\n" \
  "v_lshlrev_b32 v14, 16, v167\n" \
  "v_and_b32 v15, 0xffff0000, v167\n" \
  "ds_read_b128 v[84:87], v248 offset:0\n" \
  "ds_read_b128 v[88:91], v248 offset:1024\n" \
  "ds_read_b128 v[92:95], v248 offset:2048\n" \
  "ds_read_b128 v[96:99], v248 offset:3072\n" \
  "ds_read_b128 v[100:103], v248 offset:4096\n" \
  "ds_read_b128 v[104:107], v248 offset:5120\n" \
  "ds_read_b128 v[108:111], v248 offset:6144\n" \
  "ds_read_b128 v[112:115], v248 offset:7168\n" \
  "ds_read_b128 v[116:119], v248 offset:8192\n" \
  "ds_read_b128 v[120:123], v248 offset:9216\n" \
  "ds_read_b128 v[124:127], v248 offset:10240\n" \
  "ds_read_b128 v[128:131], v248 offset:11264\n" \
  "ds_read_b128 v[132:135], v248 offset:12288\n" \
  "ds_read_b128 v[136:139], v248 offset:13312\n" \
  "ds_read_b128 v[140:143], v248 offset:14336\n" \
  "ds_read_b128 v[144:147], v248 offset:15360\n" \
  "ds_read_b128 v[148:151], v248 offset:16384\n" \
  "ds_read_b128 v[152:155], v248 offset:17408\n" \
  "ds_read_b128 v[156:159], v248 offset:18432\n" \
  "ds_read_b128 v[160:163], v248 offset:19456\n" \
  "ds_read_b128 v[164:167], v248 offset:20480\n" \
  "ds_read_b128 v[168:171], v248 offset:21504\n" \
  "ds_read_b128 v[172:175], v248 offset:22528\n" \
  "ds_read_b128 v[176:179], v248 offset:23552\n" \
  "ds_read_b128 v[180:183], v248 offset:24576\n" \
  "ds_read_b128 v[184:187], v248 offset:25600\n" \
  "ds_read_b128 v[188:191], v248 offset:26624\n" \
  "ds_read_b128 v[192:195], v248 offset:27648\n" \
  "ds_read_b128 v[196:199], v248 offset:28672\n" \
  "ds_read_b128 v[200:203], v248 offset:29696\n" \
  "ds_read_b128 v[204:207], v248 offset:30720\n" \
  "ds_read_b128 v[208:211], v248 offset:31744\n" \
  "v_mov_b32 v80, 0\n" \
  "v_mov_b32 v82, 0\n" \
  "s_add_u32 s94, s42, 10485760\n" \
  "v_add_u32 v255, s94, v248\n" \
  "s_mov_b32 s88, s57\n" \
  "s_lshr_b32 s67, s42, 2\n" \
  "s_add_u32 s67, s67, 27262976\n" \
  "s_lshr_b32 s66, s57, 4\n" \
  "s_add_u32 s66, s66, s67\n" \
  "s_lshl_b32 s65, s48, 12\n" \
  "s_add_u32 s65, s65, 27262976\n" \
  "s_and_b32 s64, s67, 0xffffc000\n" \
  "s_lshr_b32 s61, s57, 4\n" \
  "s_add_u32 s60, s61, 128\n" \
  "s_and_b32 s60, s60, 0x380\n" \
  "v_bfe_i32 v32, v248, 7, 1\n" \
  "v_bfe_u32 v33, v248, 5, 1\n" \
  "v_lshl_add_u32 v33, v33, 1, -1\n" \
  "v_and_b32 v33, v33, v32\n" \
  "s_lshr_b32 s63, s42, 14\n" \
  "v_add_u32 v33, s63, v33\n" \
  "v_and_b32 v33, 127, v33\n" \
  "v_and_b32 v32, 0x70, v248\n" \
  "v_lshlrev_b32 v32, 3, v32\n" \
  "v_lshl_or_b32 v250, v33, 12, v32\n" \
  "v_add_u32 v250, 27262976, v250\n" \
  "s_waitcnt vmcnt(0)\n" \
  "s_sleep 12\n" \
  "s_mov_b32 s94, s88\n" \
  "buffer_load_dwordx4 v[16:19], v255, s[84:87], s94 offen offset:0 sc1\n" \
  "s_nop 3\n" \
  "buffer_load_dwordx4 v[20:23], v255, s[84:87], s94 offen offset:1024 sc1\n" \
  "s_nop 3\n" \
  "s_add_u32 s94, s94, 2048\n" \
  "s_and_b32 s94, s94, 0x3fff\n" \
  "buffer_load_dwordx4 v[24:27], v255, s[84:87], s94 offen offset:0 sc1\n" \
  "s_nop 3\n" \
  "buffer_load_dwordx4 v[28:31], v255, s[84:87], s94 offen offset:1024 sc1\n" \
  "s_nop 3\n" \
  "s_add_u32 s94, s94, 2048\n" \
  "s_and_b32 s94, s94, 0x3fff\n" \
  "buffer_load_dwordx4 v[32:35], v255, s[84:87], s94 offen offset:0 sc1\n" \
  "s_nop 3\n" \
  "buffer_load_dwordx4 v[36:39], v255, s[84:87], s94 offen offset:1024 sc1\n" \
  "s_nop 3\n" \
  "s_add_u32 s94, s94, 2048\n" \
  "s_and_b32 s94, s94, 0x3fff\n" \
  "buffer_load_dwordx4 v[40:43], v255, s[84:87], s94 offen offset:0 sc1\n" \
  "s_nop 3\n" \
  "buffer_load_dwordx4 v[44:47], v255, s[84:87], s94 offen offset:1024 sc1\n" \
  "s_nop 3\n" \
  "s_add_u32 s94, s94, 2048\n" \
  "s_and_b32 s94, s94, 0x3fff\n" \
  "buffer_load_dwordx4 v[48:51], v255, s[84:87], s94 offen offset:0 sc1\n" \
  "s_nop 3\n" \
  "buffer_load_dwordx4 v[52:55], v255, s[84:87], s94 offen offset:1024 sc1\n" \
  "s_nop 3\n" \
  "s_add_u32 s94, s94, 2048\n" \
  "s_and_b32 s94, s94, 0x3fff\n" \
  "buffer_load_dwordx4 v[56:59], v255, s[84:87], s94 offen offset:0 sc1\n" \
  "s_nop 3\n" \
  "buffer_load_dwordx4 v[60:63], v255, s[84:87], s94 offen offset:1024 sc1\n" \
  "s_waitcnt lgkmcnt(0)\n" \
  "s_waitcnt vmcnt(11)\n" \
  "v_max_u32 v251, v16, v18\n" \
  "v_cmp_eq_u32 vcc, -1, v251\n" \
  "s_cbranch_vccnz .Lp_s0\n" \
  ".Lp_b0:\n" \
  "v_mfma_f32_16x16x32_bf16 v[0:3], v[84:87], v[16:19], v[0:3]\n" \
  "ds_read_b128 v[84:87], v248 offset:32768\n" \
  "s_add_u32 s94, s94, 2048\n" \
  "s_and_b32 s94, s94, 0x3fff\n" \
  "buffer_load_dwordx4 v[64:67], v255, s[84:87], s94 offen offset:0 sc1\n" \
  "v_mfma_f32_16x16x32_bf16 v[4:7], v[88:91], v[16:19], v[4:7]\n" \
  "ds_read_b128 v[88:91], v248 offset:33792\n" \
  "s_waitcnt vmcnt(11)\n" \
  "v_max_u32 v251, v20, v22\n" \
  "v_mfma_f32_16x16x32_bf16 v[8:11], v[92:95], v[16:19], v[8:11]\n" \
  "v_cmp_eq_u32 vcc, -1, v251\n" \
  "v_mfma_f32_16x16x32_bf16 v[12:15], v[96:99], v[16:19], v[12:15]\n" \
  "ds_read_b128 v[92:95], v248 offset:34816\n" \
  "s_cbranch_vccnz .Lp_s1\n" \
  ".Lp_b1:\n" \
  "v_mfma_f32_16x16x32_bf16 v[0:3], v[100:103], v[20:23], v[0:3]\n" \
  "ds_read_b128 v[96:99], v248 offset:35840\n" \
  "buffer_load_dwordx4 v[68:71], v255, s[84:87], s94 offen offset:1024 sc1\n" \
  "v_mfma_f32_16x16x32_bf16 v[4:7], v[104:107], v[20:23], v[4:7]\n" \
  "s_waitcnt vmcnt(11)\n" \
  "v_max_u32 v251, v24, v26\n" \
  "v_mfma_f32_16x16x32_bf16 v[8:11], v[108:111], v[20:23], v[8:11]\n" \
  "ds_read_b128 v[100:103], v248 offset:36864\n" \
  "v_cmp_eq_u32 vcc, -1, v251\n" \
  "v_mfma_f32_16x16x32_bf16 v[12:15], v[112:115], v[20:23], v[12:15]\n" \
  "ds_read_b128 v[104:107], v248 offset:37888\n" \
  "s_cbranch_vccnz .Lp_s2\n" \
  ".Lp_b2:\n" \
  "v_mfma_f32_16x16x32_bf16 v[0:3], v[116:119], v[24:27], v[0:3]\n" \
  "s_add_u32 s94, s94, 2048\n" \
  "s_and_b32 s94, s94, 0x3fff\n" \
  "buffer_load_dwordx4 v[72:75], v255, s[84:87], s94 offen offset:0 sc1\n" \
  "v_mfma_f32_16x16x32_bf16 v[4:7], v[120:123], v[24:27], v[4:7]\n" \
  "ds_read_b128 v[108:111], v248 offset:38912\n" \
  "s_waitcnt vmcnt(11)\n" \
  "v_max_u32 v251, v28, v30\n" \
  "v_mfma_f32_16x16x32_bf16 v[8:11], v[124:127], v[24:27], v[8:11]\n" \
  "ds_read_b128 v[112:115], v248 offset:39936\n" \
  "v_cmp_eq_u32 vcc, -1, v251\n" \
  "v_mfma_f32_16x16x32_bf16 v[12:15], v[128:131], v[24:27], v[12:15]\n" \
  "s_cbranch_vccnz .Lp_s3\n" \
  ".Lp_b3:\n" \
  "v_mfma_f32_16x16x32_bf16 v[0:3], v[132:135], v[28:31], v[0:3]\n" \
  "ds_read_b128 v[116:119], v248 offset:40960\n" \
  "buffer_load_dwordx4 v[76:79], v255, s[84:87], s94 offen offset:1024 sc1\n" \
  "v_mfma_f32_16x16x32_bf16 v[4:7], v[136:139], v[28:31], v[4:7]\n" \
  "ds_read_b128 v[120:123], v248 offset:41984\n" \
  "s_waitcnt vmcnt(11)\n" \
  "v_max_u32 v251, v32, v34\n" \
  "v_mfma_f32_16x16x32_bf16 v[8:11], v[140:143], v[28:31], v[8:11]\n" \
  "v_cmp_eq_u32 vcc, -1, v251\n" \
  "v_mfma_f32_16x16x32_bf16 v[12:15], v[144:147], v[28:31], v[12:15]\n" \
  "ds_read_b128 v[124:127], v248 offset:43008\n" \
  "s_cbranch_vccnz .Lp_s4\n" \
  ".Lp_b4:\n" \
  "v_mfma_f32_16x16x32_bf16 v[0:3], v[148:151], v[32:35], v[0:3]\n" \
  "ds_read_b128 v[128:131], v248 offset:44032\n" \
  "v_mfma_f32_16x16x32_bf16 v[4:7], v[152:155], v[32:35], v[4:7]\n" \
  "s_waitcnt vmcnt(10)\n" \
  "v_max_u32 v251, v36, v38\n" \
  "v_mfma_f32_16x16x32_bf16 v[8:11], v[156:159], v[32:35], v[8:11]\n" \
  "ds_read_b128 v[132:135], v248 offset:45056\n" \
  "v_cmp_eq_u32 vcc, -1, v251\n" \
  "v_mfma_f32_16x16x32_bf16 v[12:15], v[160:163], v[32:35], v[12:15]\n" \
  "ds_read_b128 v[136:139], v248 offset:46080\n" \
  "s_cbranch_vccnz .Lp_s5\n" \
  ".Lp_b5:\n" \
  "v_mfma_f32_16x16x32_bf16 v[0:3], v[164:167], v[36:39], v[0:3]\n" \
  "v_mfma_f32_16x16x32_bf16 v[4:7], v[168:171], v[36:39], v[4:7]\n" \
  "ds_read_b128 v[140:143], v248 offset:47104\n" \
  "s_waitcnt vmcnt(9)\n" \
  "v_max_u32 v251, v40, v42\n" \
  "v_mfma_f32_16x16x32_bf16 v[8:11], v[172:175], v[36:39], v[8:11]\n" \
  "ds_read_b128 v[144:147], v248 offset:48128\n" \
  "v_cmp_eq_u32 vcc, -1, v251\n" \
  "v_mfma_f32_16x16x32_bf16 v[12:15], v[176:179], v[36:39], v[12:15]\n" \
  "s_cbranch_vccnz .Lp_s6\n" \
  ".Lp_b6:\n" \
  "v_mfma_f32_16x16x32_bf16 v[0:3], v[180:183], v[40:43], v[0:3]\n" \
  "ds_read_b128 v[148:151], v248 offset:49152\n" \
  "v_mfma_f32_16x16x32_bf16 v[4:7], v[184:187], v[40:43], v[4:7]\n" \
  "ds_read_b128 v[152:155], v248 offset:50176\n" \
  "s_waitcnt vmcnt(8)\n" \
  "v_max_u32 v251, v44, v46\n" \
  "v_mfma_f32_16x16x32_bf16 v[8:11], v[188:191], v[40:43], v[8:11]\n" \
  "v_cmp_eq_u32 vcc, -1, v251\n" \
  "v_mfma_f32_16x16x32_bf16 v[12:15], v[192:195], v[40:43], v[12:15]\n" \
  "ds_read_b128 v[156:159], v248 offset:51200\n" \
  "s_cbranch_vccnz .Lp_s7\n" \
  ".Lp_b7:\n" \
  "v_mfma_f32_16x16x32_bf16 v[0:3], v[196:199], v[44:47], v[0:3]\n" \
  "ds_read_b128 v[160:163], v248 offset:52224\n" \
  "v_mfma_f32_16x16x32_bf16 v[4:7], v[200:203], v[44:47], v[4:7]\n" \
  "s_waitcnt vmcnt(7) lgkmcnt(15)\n" \
  "v_max_u32 v251, v48, v50\n" \
  "v_mfma_f32_16x16x32_bf16 v[8:11], v[204:207], v[44:47], v[8:11]\n" \
  "ds_read_b128 v[164:167], v248 offset:53248\n" \
  "v_cmp_eq_u32 vcc, -1, v251\n" \
  "v_mfma_f32_16x16x32_bf16 v[12:15], v[208:211], v[44:47], v[12:15]\n" \
  "ds_read_b128 v[168:171], v248 offset:54272\n" \
  "s_cbranch_vccnz .Lp_s8\n" \
  ".Lp_b8:\n" \
  "v_mfma_f32_16x16x32_bf16 v[0:3], v[84:87], v[48:51], v[0:3]\n" \
  "v_mfma_f32_16x16x32_bf16 v[4:7], v[88:91], v[48:51], v[4:7]\n" \
  "ds_read_b128 v[172:175], v248 offset:55296\n" \
  "s_waitcnt vmcnt(6) lgkmcnt(15)\n" \
  "v_max_u32 v251, v52, v54\n" \
  "v_mfma_f32_16x16x32_bf16 v[8:11], v[92:95], v[48:51], v[8:11]\n" \
  "ds_read_b128 v[176:179], v248 offset:56320\n" \
  "v_cmp_eq_u32 vcc, -1, v251\n" \
  "v_mfma_f32_16x16x32_bf16 v[12:15], v[96:99], v[48:51], v[12:15]\n" \
  "s_cbranch_vccnz .Lp_s9\n" \
  ".Lp_b9:\n" \
  "v_mfma_f32_16x16x32_bf16 v[0:3], v[100:103], v[52:55], v[0:3]\n" \
  "ds_read_b128 v[180:183], v248 offset:57344\n" \
  "v_mfma_f32_16x16x32_bf16 v[4:7], v[104:107], v[52:55], v[4:7]\n" \
  "ds_read_b128 v[184:187], v248 offset:58368\n" \
  "s_waitcnt vmcnt(5) lgkmcnt(14)\n" \
  "v_max_u32 v251, v56, v58\n" \
  "v_mfma_f32_16x16x32_bf16 v[8:11], v[108:111], v[52:55], v[8:11]\n" \
  "v_cmp_eq_u32 vcc, -1, v251\n" \
  "v_mfma_f32_16x16x32_bf16 v[12:15], v[112:115], v[52:55], v[12:15]\n" \
  "ds_read_b128 v[188:191], v248 offset:59392\n" \
  "s_cbranch_vccnz .Lp_s10\n" \
  ".Lp_b10:\n" \
  "v_mfma_f32_16x16x32_bf16 v[0:3], v[116:119], v[56:59], v[0:3]\n" \
  "ds_read_b128 v[192:195], v248 offset:60416\n" \
  "v_mov_b32 v16, s68\n" \
  "s_mov_b64 exec, 255\n" \
  "buffer_store_dwordx4 v[16:19], v248, s[84:87], s66 offen offset:1024 sc1\n" \
  "s_mov_b64 exec, -1\n" \
  "v_mfma_f32_16x16x32_bf16 v[4:7], v[120:123], v[56:59], v[4:7]\n" \
  "s_waitcnt vmcnt(5) lgkmcnt(12)\n" \
  "v_max_u32 v251, v60, v62\n" \
  "v_mfma_f32_16x16x32_bf16 v[8:11], v[124:127], v[56:59], v[8:11]\n" \
  "ds_read_b128 v[196:199], v248 offset:61440\n" \
  "v_cmp_eq_u32 vcc, -1, v251\n" \
  "v_mfma_f32_16x16x32_bf16 v[12:15], v[128:131], v[56:59], v[12:15]\n" \
  "ds_read_b128 v[200:203], v248 offset:62464\n" \
  "s_cbranch_vccnz .Lp_s11\n" \
  ".Lp_b11:\n" \
  "v_mfma_f32_16x16x32_bf16 v[0:3], v[132:135], v[60:63], v[0:3]\n" \
  "v_mfma_f32_16x16x32_bf16 v[4:7], v[136:139], v[60:63], v[4:7]\n" \
  "ds_read_b128 v[204:207], v248 offset:63488\n" \
  "s_waitcnt vmcnt(4) lgkmcnt(11)\n" \
  "v_max_u32 v251, v64, v66\n" \
  "v_mfma_f32_16x16x32_bf16 v[8:11], v[140:143], v[60:63], v[8:11]\n" \
  "ds_read_b128 v[208:211], v248 offset:64512\n" \
  "v_cmp_eq_u32 vcc, -1, v251\n" \
  "v_mfma_f32_16x16x32_bf16 v[12:15], v[144:147], v[60:63], v[12:15]\n" \
  "s_cbranch_vccnz .Lp_s12\n" \
  ".Lp_b12:\n" \
  "v_mfma_f32_16x16x32_bf16 v[0:3], v[148:151], v[64:67], v[0:3]\n" \
  "v_mfma_f32_16x16x32_bf16 v[4:7], v[152:155], v[64:67], v[4:7]\n" \
  "s_waitcnt vmcnt(3) lgkmcnt(8)\n" \
  "v_max_u32 v251, v68, v70\n" \
  "v_mfma_f32_16x16x32_bf16 v[8:11], v[156:159], v[64:67], v[8:11]\n" \
  "v_cmp_eq_u32 vcc, -1, v251\n" \
  "v_mov_b32 v212, 131584\n" \
  "v_mov_b32 v213, 1\n" \
  "s_mov_b64 exec, 1\n" \
  "ds_add_u32 v212, v213 offset:4\n" \
  "s_mov_b64 exec, -1\n" \
  "v_mfma_f32_16x16x32_bf16 v[12:15], v[160:163], v[64:67], v[12:15]\n" \
  "s_cbranch_vccnz .Lp_s13\n" \
  ".Lp_b13:\n" \
  "v_mfma_f32_16x16x32_bf16 v[0:3], v[164:167], v[68:71], v[0:3]\n" \
  "v_mfma_f32_16x16x32_bf16 v[4:7], v[168:171], v[68:71], v[4:7]\n" \
  "s_waitcnt vmcnt(2) lgkmcnt(5)\n" \
  "v_max_u32 v251, v72, v74\n" \
  "v_mfma_f32_16x16x32_bf16 v[8:11], v[172:175], v[68:71], v[8:11]\n" \
  "v_cmp_eq_u32 vcc, -1, v251\n" \
  "v_mfma_f32_16x16x32_bf16 v[12:15], v[176:179], v[68:71], v[12:15]\n" \
  "s_cbranch_vccnz .Lp_s14\n" \
  ".Lp_b14:\n" \
  "v_mfma_f32_16x16x32_bf16 v[0:3], v[180:183], v[72:75], v[0:3]\n" \
  "v_mfma_f32_16x16x32_bf16 v[4:7], v[184:187], v[72:75], v[4:7]\n" \
  "s_waitcnt vmcnt(1) lgkmcnt(1)\n" \
  "v_max_u32 v251, v76, v78\n" \
  "v_mfma_f32_16x16x32_bf16 v[8:11], v[188:191], v[72:75], v[8:11]\n" \
  "v_cmp_eq_u32 vcc, -1, v251\n" \
  "v_mfma_f32_16x16x32_bf16 v[12:15], v[192:195], v[72:75], v[12:15]\n" \
  "s_cbranch_vccnz .Lp_s15\n" \
  ".Lp_b15:\n" \
  "v_mfma_f32_16x16x32_bf16 v[0:3], v[196:199], v[76:79], v[0:3]\n" \
  "v_mov_b32 v212, 131584\n" \
  "ds_read_b32 v213, v212 offset:0\n" \
  "v_mfma_f32_16x16x32_bf16 v[4:7], v[200:203], v[76:79], v[4:7]\n" \
  "v_mfma_f32_16x16x32_bf16 v[8:11], v[204:207], v[76:79], v[8:11]\n" \
  "v_mfma_f32_16x16x32_bf16 v[12:15], v[208:211], v[76:79], v[12:15]\n" \
  "s_add_u32 s94, s43, 12582912\n" \
  "s_nop 1\n" \
  "v_cvt_pk_bf16_f32 v16, v0, v1\n" \
  "v_cvt_pk_bf16_f32 v17, v2, v3\n" \
  "s_nop 0\n" \
  "v_cvt_pk_bf16_f32 v18, v4, v5\n" \
  "v_cvt_pk_bf16_f32 v19, v6, v7\n" \
  "v_min_u32 v16, -2, v16\n" \
  "v_min_u32 v17, -2, v17\n" \
  "v_min_u32 v18, -2, v18\n" \
  "v_min_u32 v19, -2, v19\n" \
  "buffer_store_dwordx4 v[16:19], v248, s[84:87], s94 offen offset:0 sc1\n" \
  "v_cvt_pk_bf16_f32 v20, v8, v9\n" \
  "v_cvt_pk_bf16_f32 v21, v10, v11\n" \
  "v_cvt_pk_bf16_f32 v22, v12, v13\n" \
  "v_cvt_pk_bf16_f32 v23, v14, v15\n" \
  "v_min_u32 v20, -2, v20\n" \
  "v_min_u32 v21, -2, v21\n" \
  "v_min_u32 v22, -2, v22\n" \
  "v_min_u32 v23, -2, v23\n" \
  "buffer_store_dwordx4 v[20:23], v248, s[84:87], s94 offen offset:1024 sc1\n" \
  "v_pk_add_f32 v[24:25], v[0:1], v[2:3]\n" \
  "v_pk_mul_f32 v[28:29], v[0:1], v[0:1]\n" \
  "v_pk_add_f32 v[26:27], v[4:5], v[6:7]\n" \
  "v_pk_mul_f32 v[30:31], v[2:3], v[2:3]\n" \
  "v_pk_fma_f32 v[28:29], v[4:5], v[4:5], v[28:29]\n" \
  "v_pk_fma_f32 v[30:31], v[6:7], v[6:7], v[30:31]\n" \
  "v_pk_fma_f32 v[28:29], v[8:9], v[8:9], v[28:29]\n" \
  "v_pk_add_f32 v[24:25], v[24:25], v[8:9]\n" \
  "v_pk_fma_f32 v[30:31], v[10:11], v[10:11], v[30:31]\n" \
  "v_pk_add_f32 v[26:27], v[26:27], v[10:11]\n" \
  "v_pk_fma_f32 v[28:29], v[12:13], v[12:13], v[28:29]\n" \
  "v_pk_add_f32 v[24:25], v[24:25], v[12:13]\n" \
  "v_pk_fma_f32 v[30:31], v[14:15], v[14:15], v[30:31]\n" \
  "v_pk_add_f32 v[26:27], v[26:27], v[14:15]\n" \
  "v_pk_add_f32 v[24:25], v[24:25], v[26:27]\n" \
  "v_pk_add_f32 v[28:29], v[28:29], v[30:31]\n" \
  "v_add_f32 v24, v24, v25\n" \
  "v_add_f32 v25, v28, v29\n" \
  "s_add_u32 s94, s44, 26214400\n" \
  "s_nop 0\n" \
  "v_permlane16_swap_b32 v24, v25\n" \
  "v_add_f32 v24, v24, v25\n" \
  "v_mov_b32 v25, v24\n" \
  "s_nop 1\n" \
  "v_permlane32_swap_b32 v24, v25\n" \
  "v_add_f32 v24, v24, v25\n" \
  "v_mov_b32 v25, v24\n" \
  "s_nop 1\n" \
  "v_permlane16_swap_b32 v24, v25\n" \
  "v_min_u32 v24, -2, v24\n" \
  "v_min_u32 v25, -2, v25\n" \
  "s_mov_b64 exec, 0xffff\n" \
  "buffer_store_dwordx2 v[24:25], v248, s[84:87], s94 offen sc1\n" \
  "s_mov_b64 exec, -1\n" \
  "s_waitcnt lgkmcnt(0)\n" \
  "v_cmp_gt_u32 vcc, 4, v213\n" \
  "s_cbranch_vccz .Lp_c0_x\n" \
  "s_mov_b32 s92, 2000000\n" \
  ".Lp_c0_l:\n" \
  "s_sleep 1\n" \
  "ds_read_b32 v213, v212 offset:0\n" \
  "s_waitcnt lgkmcnt(0)\n" \
  "v_cmp_gt_u32 vcc, 4, v213\n" \
  "s_cbranch_vccz .Lp_c0_x\n" \
  "s_sub_u32 s92, s92, 1\n" \
  "s_cbranch_scc0 .Lp_c0_l\n" \
  ".Lp_c0_x:\n" \
  "ds_read_b128 v[84:87], v249 offset:0\n" \
  "ds_read_b128 v[88:91], v249 offset:1024\n" \
  "ds_read_b128 v[92:95], v249 offset:2048\n" \
  "ds_read_b128 v[96:99], v249 offset:3072\n" \
  "ds_read_b128 v[100:103], v249 offset:4096\n" \
  "ds_read_b128 v[104:107], v249 offset:5120\n" \
  "ds_read_b128 v[108:111], v249 offset:6144\n" \
  "ds_read_b128 v[112:115], v249 offset:7168\n" \
  "ds_read_b128 v[116:119], v249 offset:8192\n" \
  "ds_read_b128 v[120:123], v249 offset:9216\n" \
  "ds_read_b128 v[124:127], v249 offset:10240\n" \
  "ds_read_b128 v[128:131], v249 offset:11264\n" \
  "ds_read_b128 v[132:135], v249 offset:12288\n" \
  "ds_read_b128 v[136:139], v249 offset:13312\n" \
  "ds_read_b128 v[140:143], v249 offset:14336\n" \
  "ds_read_b128 v[144:147], v249 offset:15360\n" \
  "ds_read_b128 v[148:151], v249 offset:16384\n" \
  "ds_read_b128 v[152:155], v249 offset:17408\n" \
  "ds_read_b128 v[156:159], v249 offset:18432\n" \
  "ds_read_b128 v[160:163], v249 offset:19456\n" \
  "ds_read_b128 v[164:167], v249 offset:20480\n" \
  "ds_read_b128 v[168:171], v249 offset:21504\n" \
  "ds_read_b128 v[172:175], v249 offset:22528\n" \
  "ds_read_b128 v[176:179], v249 offset:23552\n" \
  "ds_read_b128 v[180:183], v249 offset:24576\n" \
  "ds_read_b128 v[184:187], v249 offset:25600\n" \
  "ds_read_b128 v[188:191], v249 offset:26624\n" \
  "ds_read_b128 v[192:195], v249 offset:27648\n" \
  "ds_read_b128 v[196:199], v249 offset:28672\n" \
  "ds_read_b128 v[200:203], v249 offset:29696\n" \
  "ds_read_b128 v[204:207], v249 offset:30720\n" \
  "ds_read_b128 v[208:211], v249 offset:31744\n" \
  "v_lshrrev_b32 v251, 4, v248\n" \
  "v_and_b32 v251, 0x30, v251\n" \
  "v_add_u32 v251, 0x20000, v251\n" \
  "v_mov_b64 v[228:229], v[0:1]\n" \
  "v_mov_b64 v[230:231], v[2:3]\n" \
  "v_mov_b64 v[232:233], v[4:5]\n" \
  "v_mov_b64 v[234:235], v[6:7]\n" \
  "v_mov_b64 v[236:237], v[8:9]\n" \
  "v_mov_b64 v[238:239], v[10:11]\n" \
  "v_mov_b64 v[240:241], v[12:13]\n" \
  "v_mov_b64 v[242:243], v[14:15]\n" \
  "ds_read_b128 v[212:215], v251 offset:0\n" \
  "ds_read_b128 v[216:219], v251 offset:64\n" \
  "ds_read_b128 v[220:223], v251 offset:128\n" \
  "ds_read_b128 v[224:227], v251 offset:192\n" \
  "v_mov_b64 v[0:1], 0\n" \
  "v_mov_b64 v[2:3], 0\n" \
  "v_mov_b64 v[4:5], 0\n" \
  "v_mov_b64 v[6:7], 0\n" \
  "v_mov_b64 v[8:9], 0\n" \
  "v_mov_b64 v[10:11], 0\n" \
  "v_mov_b64 v[12:13], 0\n" \
  "v_mov_b64 v[14:15], 0\n" \
  "s_add_u32 s94, s42, 12582912\n" \
  "v_add_u32 v255, s94, v248\n" \
  "s_lshr_b32 s89, s42, 4\n" \
  "s_add_u32 s89, s89, 26214400\n" \
  "v_and_b32 v33, 0x70, v248\n" \
  "v_lshlrev_b32 v33, 3, v33\n" \
  "s_mov_b32 s92, 2000\n" \
  ".Lp_q1_l:\n" \
  "buffer_load_dword v44, v33, s[84:87], s67 offen offset:1024 sc1 ; RF\n" \
  "s_waitcnt vmcnt(0) ; RF\n" \
  "v_cmp_ne_u32 vcc, s68, v44 ; RF\n" \
  "s_cbranch_vccz .Lp_q1_g\n" \
  "s_sleep 4\n" \
  "s_sub_u32 s92, s92, 1\n" \
  "s_cbranch_scc0 .Lp_q1_l\n" \
  ".Lp_q1_g:\n" \
  "s_mov_b32 s94, s88\n" \
  "s_add_u32 s94, s94, 2048\n" \
  "s_and_b32 s94, s94, 0x3fff\n" \
  "buffer_load_dwordx4 v[24:27], v255, s[84:87], s94 offen offset:0 sc1\n" \
  "s_nop 3\n" \
  "buffer_load_dwordx4 v[28:31], v255, s[84:87], s94 offen offset:1024 sc1\n" \
  "s_nop 3\n" \
  "s_add_u32 s94, s94, 2048\n" \
  "s_and_b32 s94, s94, 0x3fff\n" \
  "buffer_load_dwordx4 v[32:35], v255, s[84:87], s94 offen offset:0 sc1\n" \
  "s_nop 3\n" \
  "buffer_load_dwordx4 v[36:39], v255, s[84:87], s94 offen offset:1024 sc1\n" \
  "s_nop 3\n" \
  "s_add_u32 s94, s94, 2048\n" \
  "s_and_b32 s94, s94, 0x3fff\n" \
  "buffer_load_dwordx4 v[40:43], v255, s[84:87], s94 offen offset:0 sc1\n" \
  "s_nop 3\n" \
  "buffer_load_dwordx4 v[44:47], v255, s[84:87], s94 offen offset:1024 sc1\n" \
  "s_nop 3\n" \
  "buffer_load_dwordx4 v[80:83], v248, s[84:87], s89 offen offset:0 sc1\n" \
  "s_add_u32 s94, s94, 2048\n" \
  "s_and_b32 s94, s94, 0x3fff\n" \
  "buffer_load_dwordx4 v[48:51], v255, s[84:87], s94 offen offset:0 sc1\n" \
  "s_waitcnt lgkmcnt(0)\n" \
  "v_mfma_f32_16x16x32_bf16 v[0:3], v[84:87], v[16:19], v[0:3]\n" \
  "ds_read_b128 v[84:87], v249 offset:32768\n" \
  "buffer_load_dwordx4 v[52:55], v255, s[84:87], s94 offen offset:1024 sc1\n" \
  "v_mfma_f32_16x16x32_bf16 v[4:7], v[88:91], v[16:19], v[4:7]\n" \
  "ds_read_b128 v[88:91], v249 offset:33792\n" \
  "v_mfma_f32_16x16x32_bf16 v[8:11], v[92:95], v[16:19], v[8:11]\n" \
  "v_mfma_f32_16x16x32_bf16 v[12:15], v[96:99], v[16:19], v[12:15]\n" \
  "ds_read_b128 v[92:95], v249 offset:34816\n" \
  "v_mfma_f32_16x16x32_bf16 v[0:3], v[100:103], v[20:23], v[0:3]\n" \
  "ds_read_b128 v[96:99], v249 offset:35840\n" \
  "s_add_u32 s94, s94, 2048\n" \
  "s_and_b32 s94, s94, 0x3fff\n" \
  "buffer_load_dwordx4 v[56:59], v255, s[84:87], s94 offen offset:0 sc1\n" \
  "v_mfma_f32_16x16x32_bf16 v[4:7], v[104:107], v[20:23], v[4:7]\n" \
  "s_waitcnt vmcnt(9)\n" \
  "v_max_u32 v251, v24, v26\n" \
  "v_mfma_f32_16x16x32_bf16 v[8:11], v[108:111], v[20:23], v[8:11]\n" \
  "ds_read_b128 v[100:103], v249 offset:36864\n" \
  "v_cmp_eq_u32 vcc, -1, v251\n" \
  "v_mfma_f32_16x16x32_bf16 v[12:15], v[112:115], v[20:23], v[12:15]\n" \
  "ds_read_b128 v[104:107], v249 offset:37888\n" \
  "s_cbranch_vccnz .Lp_s16\n" \
  ".Lp_b16:\n" \
  "v_mfma_f32_16x16x32_bf16 v[0:3], v[116:119], v[24:27], v[0:3]\n" \
  "buffer_load_dwordx4 v[60:63], v255, s[84:87], s94 offen offset:1024 sc1\n" \
  "v_mfma_f32_16x16x32_bf16 v[4:7], v[120:123], v[24:27], v[4:7]\n" \
  "ds_read_b128 v[108:111], v249 offset:38912\n" \
  "s_waitcnt vmcnt(9)\n" \
  "v_max_u32 v251, v28, v30\n" \
  "v_mfma_f32_16x16x32_bf16 v[8:11], v[124:127], v[24:27], v[8:11]\n" \
  "ds_read_b128 v[112:115], v249 offset:39936\n" \
  "v_cmp_eq_u32 vcc, -1, v251\n" \
  "v_mfma_f32_16x16x32_bf16 v[12:15], v[128:131], v[24:27], v[12:15]\n" \
  "s_cbranch_vccnz .Lp_s17\n" \
  ".Lp_b17:\n" \
  "v_mfma_f32_16x16x32_bf16 v[0:3], v[132:135], v[28:31], v[0:3]\n" \
  "ds_read_b128 v[116:119], v249 offset:40960\n" \
  "s_add_u32 s94, s94, 2048\n" \
  "s_and_b32 s94, s94, 0x3fff\n" \
  "buffer_load_dwordx4 v[64:67], v255, s[84:87], s94 offen offset:0 sc1\n" \
  "v_mfma_f32_16x16x32_bf16 v[4:7], v[136:139], v[28:31], v[4:7]\n" \
  "ds_read_b128 v[120:123], v249 offset:41984\n" \
  "s_waitcnt vmcnt(9)\n" \
  "v_max_u32 v251, v32, v34\n" \
  "v_mfma_f32_16x16x32_bf16 v[8:11], v[140:143], v[28:31], v[8:11]\n" \
  "v_cmp_eq_u32 vcc, -1, v251\n" \
  "v_mfma_f32_16x16x32_bf16 v[12:15], v[144:147], v[28:31], v[12:15]\n" \
  "ds_read_b128 v[124:127], v249 offset:43008\n" \
  "s_cbranch_vccnz .Lp_s18\n" \
  ".Lp_b18:\n" \
  "v_mfma_f32_16x16x32_bf16 v[0:3], v[148:151], v[32:35], v[0:3]\n" \
  "ds_read_b128 v[128:131], v249 offset:44032\n" \
  "buffer_load_dwordx4 v[68:71], v255, s[84:87], s94 offen offset:1024 sc1\n" \
  "v_mfma_f32_16x16x32_bf16 v[4:7], v[152:155], v[32:35], v[4:7]\n" \
  "s_waitcnt vmcnt(9)\n" \
  "v_max_u32 v251, v36, v38\n" \
  "v_mfma_f32_16x16x32_bf16 v[8:11], v[156:159], v[32:35], v[8:11]\n" \
  "ds_read_b128 v[132:135], v249 offset:45056\n" \
  "v_cmp_eq_u32 vcc, -1, v251\n" \
  "v_mfma_f32_16x16x32_bf16 v[12:15], v[160:163], v[32:35], v[12:15]\n" \
  "ds_read_b128 v[136:139], v249 offset:46080\n" \
  "s_cbranch_vccnz .Lp_s19\n" \
  ".Lp_b19:\n" \
  "v_mfma_f32_16x16x32_bf16 v[0:3], v[164:167], v[36:39], v[0:3]\n" \
  "s_add_u32 s94, s94, 2048\n" \
  "s_and_b32 s94, s94, 0x3fff\n" \
  "buffer_load_dwordx4 v[72:75], v255, s[84:87], s94 offen offset:0 sc1\n" \
  "v_mfma_f32_16x16x32_bf16 v[4:7], v[168:171], v[36:39], v[4:7]\n" \
  "ds_read_b128 v[140:143], v249 offset:47104\n" \
  "s_waitcnt vmcnt(9)\n" \
  "v_max_u32 v251, v40, v42\n" \
  "v_mfma_f32_16x16x32_bf16 v[8:11], v[172:175], v[36:39], v[8:11]\n" \
  "ds_read_b128 v[144:147], v249 offset:48128\n" \
  "v_cmp_eq_u32 vcc, -1, v251\n" \
  "v_mfma_f32_16x16x32_bf16 v[12:15], v[176:179], v[36:39], v[12:15]\n" \
  "s_cbranch_vccnz .Lp_s20\n" \
  ".Lp_b20:\n" \
  "v_mfma_f32_16x16x32_bf16 v[0:3], v[180:183], v[40:43], v[0:3]\n" \
  "ds_read_b128 v[148:151], v249 offset:49152\n" \
  "buffer_load_dwordx4 v[76:79], v255, s[84:87], s94 offen offset:1024 sc1\n" \
  "v_mfma_f32_16x16x32_bf16 v[4:7], v[184:187], v[40:43], v[4:7]\n" \
  "ds_read_b128 v[152:155], v249 offset:50176\n" \
  "s_waitcnt vmcnt(9)\n" \
  "v_max_u32 v251, v44, v46\n" \
  "v_mfma_f32_16x16x32_bf16 v[8:11], v[188:191], v[40:43], v[8:11]\n" \
  "v_cmp_eq_u32 vcc, -1, v251\n" \
  "v_mfma_f32_16x16x32_bf16 v[12:15], v[192:195], v[40:43], v[12:15]\n" \
  "ds_read_b128 v[156:159], v249 offset:51200\n" \
  "s_cbranch_vccnz .Lp_s21\n" \
  ".Lp_b21:\n" \
  "v_mfma_f32_16x16x32_bf16 v[0:3], v[196:199], v[44:47], v[0:3]\n" \
  "ds_read_b128 v[160:163], v249 offset:52224\n" \
  "v_mfma_f32_16x16x32_bf16 v[4:7], v[200:203], v[44:47], v[4:7]\n" \
  "s_waitcnt vmcnt(7) lgkmcnt(15)\n" \
  "v_max_u32 v251, v48, v50\n" \
  "v_mfma_f32_16x16x32_bf16 v[8:11], v[204:207], v[44:47], v[8:11]\n" \
  "ds_read_b128 v[164:167], v249 offset:53248\n" \
  "v_cmp_eq_u32 vcc, -1, v251\n" \
  "v_mfma_f32_16x16x32_bf16 v[12:15], v[208:211], v[44:47], v[12:15]\n" \
  "ds_read_b128 v[168:171], v249 offset:54272\n" \
  "s_cbranch_vccnz .Lp_s22\n" \
  ".Lp_b22:\n" \
  "v_mfma_f32_16x16x32_bf16 v[0:3], v[84:87], v[48:51], v[0:3]\n" \
  "v_max_u32 v251, v80, v82\n" \
  "v_cmp_eq_u32 vcc, -1, v251\n" \
  "s_cbranch_vccnz .Lp_s23\n" \
  ".Lp_b23:\n" \
  "v_mfma_f32_16x16x32_bf16 v[4:7], v[88:91], v[48:51], v[4:7]\n" \
  "ds_read_b128 v[172:175], v249 offset:55296\n" \
  "s_waitcnt vmcnt(6) lgkmcnt(15)\n" \
  "v_max_u32 v251, v52, v54\n" \
  "v_mfma_f32_16x16x32_bf16 v[8:11], v[92:95], v[48:51], v[8:11]\n" \
  "ds_read_b128 v[176:179], v249 offset:56320\n" \
  "v_cmp_eq_u32 vcc, -1, v251\n" \
  "v_mfma_f32_16x16x32_bf16 v[12:15], v[96:99], v[48:51], v[12:15]\n" \
  "s_cbranch_vccnz .Lp_s24\n" \
  ".Lp_b24:\n" \
  "v_mfma_f32_16x16x32_bf16 v[0:3], v[100:103], v[52:55], v[0:3]\n" \
  "ds_read_b128 v[180:183], v249 offset:57344\n" \
  "v_add_f32 v84, v80, v82\n" \
  "v_mfma_f32_16x16x32_bf16 v[4:7], v[104:107], v[52:55], v[4:7]\n" \
  "ds_read_b128 v[184:187], v249 offset:58368\n" \
  "s_waitcnt vmcnt(5) lgkmcnt(14)\n" \
  "v_max_u32 v251, v56, v58\n" \
  "v_mfma_f32_16x16x32_bf16 v[8:11], v[108:111], v[52:55], v[8:11]\n" \
  "v_cmp_eq_u32 vcc, -1, v251\n" \
  "v_add_f32 v85, v81, v83\n" \
  "v_mfma_f32_16x16x32_bf16 v[12:15], v[112:115], v[52:55], v[12:15]\n" \
  "ds_read_b128 v[188:191], v249 offset:59392\n" \
  "v_permlane16_swap_b32 v84, v85\n" \
  "s_cbranch_vccnz .Lp_s25\n" \
  ".Lp_b25:\n" \
  "v_mfma_f32_16x16x32_bf16 v[0:3], v[116:119], v[56:59], v[0:3]\n" \
  "ds_read_b128 v[192:195], v249 offset:60416\n" \
  "v_add_f32 v84, v84, v85\n" \
  "v_mfma_f32_16x16x32_bf16 v[4:7], v[120:123], v[56:59], v[4:7]\n" \
  "s_waitcnt vmcnt(4) lgkmcnt(12)\n" \
  "v_max_u32 v251, v60, v62\n" \
  "v_mfma_f32_16x16x32_bf16 v[8:11], v[124:127], v[56:59], v[8:11]\n" \
  "ds_read_b128 v[196:199], v249 offset:61440\n" \
  "v_cmp_eq_u32 vcc, -1, v251\n" \
  "v_mov_b32 v85, v84\n" \
  "v_mfma_f32_16x16x32_bf16 v[12:15], v[128:131], v[56:59], v[12:15]\n" \
  "ds_read_b128 v[200:203], v249 offset:62464\n" \
  "v_permlane32_swap_b32 v84, v85\n" \
  "s_cbranch_vccnz .Lp_s26\n" \
  ".Lp_b26:\n" \
  "v_mfma_f32_16x16x32_bf16 v[0:3], v[132:135], v[60:63], v[0:3]\n" \
  "v_add_f32 v84, v84, v85\n" \
  "v_mfma_f32_16x16x32_bf16 v[4:7], v[136:139], v[60:63], v[4:7]\n" \
  "ds_read_b128 v[204:207], v249 offset:63488\n" \
  "s_waitcnt vmcnt(3) lgkmcnt(11)\n" \
  "v_max_u32 v251, v64, v66\n" \
  "v_mfma_f32_16x16x32_bf16 v[8:11], v[140:143], v[60:63], v[8:11]\n" \
  "ds_read_b128 v[208:211], v249 offset:64512\n" \
  "v_cmp_eq_u32 vcc, -1, v251\n" \
  "v_mov_b32 v85, v84\n" \
  "v_mfma_f32_16x16x32_bf16 v[12:15], v[144:147], v[60:63], v[12:15]\n" \
  "s_nop 0\n" \
  "v_permlane16_swap_b32 v84, v85\n" \
  "s_cbranch_vccnz .Lp_s27\n" \
  ".Lp_b27:\n" \
  "v_mfma_f32_16x16x32_bf16 v[0:3], v[148:151], v[64:67], v[0:3]\n" \
  "v_mul_f32 v247, 0x3b000000, v84\n" \
  "v_mul_f32 v85, 0x3b000000, v85\n" \
  "v_mov_b32 v16, s68\n" \
  "s_mov_b64 exec, 255\n" \
  "buffer_store_dwordx4 v[16:19], v248, s[84:87], s66 offen offset:2048 sc1\n" \
  "s_mov_b64 exec, -1\n" \
  "v_mfma_f32_16x16x32_bf16 v[4:7], v[152:155], v[64:67], v[4:7]\n" \
  "s_waitcnt vmcnt(3) lgkmcnt(8)\n" \
  "v_max_u32 v251, v68, v70\n" \
  "v_mov_b32 v92, 131584\n" \
  "v_mov_b32 v93, 1\n" \
  "s_mov_b64 exec, 1\n" \
  "ds_add_u32 v92, v93 offset:12\n" \
  "s_mov_b64 exec, -1\n" \
  "v_mfma_f32_16x16x32_bf16 v[8:11], v[156:159], v[64:67], v[8:11]\n" \
  "v_cmp_eq_u32 vcc, -1, v251\n" \
  "v_fma_f32 v85, -v247, v247, v85\n" \
  "v_max_f32 v85, 0, v85\n" \
  "v_mfma_f32_16x16x32_bf16 v[12:15], v[160:163], v[64:67], v[12:15]\n" \
  "v_add_f32 v86, 0x3727c5ac, v85\n" \
  "v_rsq_f32 v90, v86\n" \
  "s_cbranch_vccnz .Lp_s28\n" \
  ".Lp_b28:\n" \
  "v_mfma_f32_16x16x32_bf16 v[0:3], v[164:167], v[68:71], v[0:3]\n" \
  "v_mul_f32 v87, v85, v90\n" \
  "v_fmaak_f32 v87, v87, v90, 0x3727c5ac\n" \
  "v_mfma_f32_16x16x32_bf16 v[4:7], v[168:171], v[68:71], v[4:7]\n" \
  "s_waitcnt vmcnt(2) lgkmcnt(5)\n" \
  "v_max_u32 v251, v72, v74\n" \
  "v_mfma_f32_16x16x32_bf16 v[8:11], v[172:175], v[68:71], v[8:11]\n" \
  "v_cmp_eq_u32 vcc, -1, v251\n" \
  "v_rsq_f32 v88, v87\n" \
  "v_mul_f32 v86, v86, v90\n" \
  "v_mfma_f32_16x16x32_bf16 v[12:15], v[176:179], v[68:71], v[12:15]\n" \
  "v_mul_f32 v89, v90, v88\n" \
  "v_mul_f32 v87, v87, v88\n" \
  "s_cbranch_vccnz .Lp_s29\n" \
  ".Lp_b29:\n" \
  "v_mfma_f32_16x16x32_bf16 v[0:3], v[180:183], v[72:75], v[0:3]\n" \
  "v_mul_f32 v244, 0xc0135761, v89\n" \
  "v_mul_f32 v246, v86, v87\n" \
  "v_mfma_f32_16x16x32_bf16 v[4:7], v[184:187], v[72:75], v[4:7]\n" \
  "s_waitcnt vmcnt(1) lgkmcnt(1)\n" \
  "v_max_u32 v251, v76, v78\n" \
  "v_mfma_f32_16x16x32_bf16 v[8:11], v[188:191], v[72:75], v[8:11]\n" \
  "v_cmp_eq_u32 vcc, -1, v251\n" \
  "v_mul_f32 v89, v89, v89\n" \
  "v_mul_f32 v89, 0x3d372713, v89\n" \
  "v_mfma_f32_16x16x32_bf16 v[12:15], v[192:195], v[72:75], v[12:15]\n" \
  "v_mul_f32 v245, v244, v89\n" \
  "v_mul_f32_e64 v91, -v247, v90\n" \
  "s_cbranch_vccnz .Lp_s30\n" \
  ".Lp_b30:\n" \
  "v_mfma_f32_16x16x32_bf16 v[0:3], v[196:199], v[76:79], v[0:3]\n" \
  "v_mfma_f32_16x16x32_bf16 v[4:7], v[200:203], v[76:79], v[4:7]\n" \
  "v_mfma_f32_16x16x32_bf16 v[8:11], v[204:207], v[76:79], v[8:11]\n" \
  "v_mfma_f32_16x16x32_bf16 v[12:15], v[208:211], v[76:79], v[12:15]\n" \
  "v_mov_b32 v92, 131584\n" \
  "ds_read_b32 v93, v92 offset:8\n" \
  "s_nop 2\n" \
  "v_pk_fma_f32 v[0:1], v[246:247], v[212:213], v[0:1] op_sel:[1,0,0] op_sel_hi:[1,1,1] neg_lo:[1,0,0] neg_hi:[1,0,0]\n" \
  "v_pk_fma_f32 v[4:5], v[246:247], v[216:217], v[4:5] op_sel:[1,0,0] op_sel_hi:[1,1,1] neg_lo:[1,0,0] neg_hi:[1,0,0]\n" \
  "v_pk_fma_f32 v[2:3], v[246:247], v[214:215], v[2:3] op_sel:[1,0,0] op_sel_hi:[1,1,1] neg_lo:[1,0,0] neg_hi:[1,0,0]\n" \
  "v_pk_fma_f32 v[6:7], v[246:247], v[218:219], v[6:7] op_sel:[1,0,0] op_sel_hi:[1,1,1] neg_lo:[1,0,0] neg_hi:[1,0,0]\n" \
  "v_pk_mul_f32 v[24:25], v[0:1], v[0:1]\n" \
  "v_pk_mul_f32 v[28:29], v[4:5], v[4:5]\n" \
  "v_pk_mul_f32 v[26:27], v[2:3], v[2:3]\n" \
  "v_pk_mul_f32 v[30:31], v[6:7], v[6:7]\n" \
  "v_pk_fma_f32 v[24:25], v[244:245], v[24:25], v[244:245] op_sel:[1,0,0] op_sel_hi:[1,1,0]\n" \
  "v_pk_fma_f32 v[28:29], v[244:245], v[28:29], v[244:245] op_sel:[1,0,0] op_sel_hi:[1,1,0]\n" \
  "v_pk_fma_f32 v[26:27], v[244:245], v[26:27], v[244:245] op_sel:[1,0,0] op_sel_hi:[1,1,0]\n" \
  "v_pk_fma_f32 v[30:31], v[244:245], v[30:31], v[244:245] op_sel:[1,0,0] op_sel_hi:[1,1,0]\n" \
  "v_pk_mul_f32 v[24:25], v[24:25], v[0:1]\n" \
  "v_pk_mul_f32 v[28:29], v[28:29], v[4:5]\n" \
  "v_pk_mul_f32 v[26:27], v[26:27], v[2:3]\n" \
  "v_pk_mul_f32 v[30:31], v[30:31], v[6:7]\n" \
  "v_exp_f32 v24, v24\n" \
  "v_exp_f32 v28, v28\n" \
  "v_exp_f32 v25, v25\n" \
  "v_exp_f32 v29, v29\n" \
  "v_exp_f32 v26, v26\n" \
  "v_exp_f32 v30, v30\n" \
  "v_exp_f32 v27, v27\n" \
  "v_exp_f32 v31, v31\n" \
  "v_pk_fma_f32 v[24:25], v[24:25], v[246:247], v[246:247] op_sel:[0,0,0] op_sel_hi:[1,0,0]\n" \
  "v_pk_fma_f32 v[28:29], v[28:29], v[246:247], v[246:247] op_sel:[0,0,0] op_sel_hi:[1,0,0]\n" \
  "v_pk_fma_f32 v[26:27], v[26:27], v[246:247], v[246:247] op_sel:[0,0,0] op_sel_hi:[1,0,0]\n" \
  "v_pk_fma_f32 v[30:31], v[30:31], v[246:247], v[246:247] op_sel:[0,0,0] op_sel_hi:[1,0,0]\n" \
  "v_rcp_f32 v24, v24\n" \
  "v_rcp_f32 v28, v28\n" \
  "v_rcp_f32 v25, v25\n" \
  "v_rcp_f32 v29, v29\n" \
  "v_rcp_f32 v26, v26\n" \
  "v_rcp_f32 v30, v30\n" \
  "v_rcp_f32 v27, v27\n" \
  "v_rcp_f32 v31, v31\n" \
  "v_pk_mul_f32 v[24:25], v[24:25], v[0:1]\n" \
  "v_pk_mul_f32 v[28:29], v[28:29], v[4:5]\n" \
  "v_pk_mul_f32 v[26:27], v[26:27], v[2:3]\n" \
  "v_pk_mul_f32 v[30:31], v[30:31], v[6:7]\n" \
  "v_cvt_pk_bf16_f32 v16, v24, v25\n" \
  "v_cvt_pk_bf16_f32 v18, v28, v29\n" \
  "v_cvt_pk_bf16_f32 v17, v26, v27\n" \
  "v_cvt_pk_bf16_f32 v19, v30, v31\n" \
  "v_min_u32 v16, v253, v16\n" \
  "v_min_u32 v18, v253, v18\n" \
  "v_min_u32 v17, v253, v17\n" \
  "v_min_u32 v19, v253, v19\n" \
  "buffer_store_dwordx4 v[16:19], v252, s[84:87], 0 offen offset:0 sc1\n" \
  "v_pk_fma_f32 v[8:9], v[246:247], v[220:221], v[8:9] op_sel:[1,0,0] op_sel_hi:[1,1,1] neg_lo:[1,0,0] neg_hi:[1,0,0]\n" \
  "v_pk_fma_f32 v[12:13], v[246:247], v[224:225], v[12:13] op_sel:[1,0,0] op_sel_hi:[1,1,1] neg_lo:[1,0,0] neg_hi:[1,0,0]\n" \
  "v_pk_fma_f32 v[10:11], v[246:247], v[222:223], v[10:11] op_sel:[1,0,0] op_sel_hi:[1,1,1] neg_lo:[1,0,0] neg_hi:[1,0,0]\n" \
  "v_pk_fma_f32 v[14:15], v[246:247], v[226:227], v[14:15] op_sel:[1,0,0] op_sel_hi:[1,1,1] neg_lo:[1,0,0] neg_hi:[1,0,0]\n" \
  "v_pk_mul_f32 v[24:25], v[8:9], v[8:9]\n" \
  "v_pk_mul_f32 v[28:29], v[12:13], v[12:13]\n" \
  "v_pk_mul_f32 v[26:27], v[10:11], v[10:11]\n" \
  "v_pk_mul_f32 v[30:31], v[14:15], v[14:15]\n" \
  "v_pk_fma_f32 v[24:25], v[244:245], v[24:25], v[244:245] op_sel:[1,0,0] op_sel_hi:[1,1,0]\n" \
  "v_pk_fma_f32 v[28:29], v[244:245], v[28:29], v[244:245] op_sel:[1,0,0] op_sel_hi:[1,1,0]\n" \
  "v_pk_fma_f32 v[26:27], v[244:245], v[26:27], v[244:245] op_sel:[1,0,0] op_sel_hi:[1,1,0]\n" \
  "v_pk_fma_f32 v[30:31], v[244:245], v[30:31], v[244:245] op_sel:[1,0,0] op_sel_hi:[1,1,0]\n" \
  "v_pk_mul_f32 v[24:25], v[24:25], v[8:9]\n" \
  "v_pk_mul_f32 v[28:29], v[28:29], v[12:13]\n" \
  "v_pk_mul_f32 v[26:27], v[26:27], v[10:11]\n" \
  "v_pk_mul_f32 v[30:31], v[30:31], v[14:15]\n" \
  "v_exp_f32 v24, v24\n" \
  "v_exp_f32 v28, v28\n" \
  "v_exp_f32 v25, v25\n" \
  "v_exp_f32 v29, v29\n" \
  "v_exp_f32 v26, v26\n" \
  "v_exp_f32 v30, v30\n" \
  "v_exp_f32 v27, v27\n" \
  "v_exp_f32 v31, v31\n" \
  "v_pk_fma_f32 v[24:25], v[24:25], v[246:247], v[246:247] op_sel:[0,0,0] op_sel_hi:[1,0,0]\n" \
  "v_pk_fma_f32 v[28:29], v[28:29], v[246:247], v[246:247] op_sel:[0,0,0] op_sel_hi:[1,0,0]\n" \
  "v_pk_fma_f32 v[26:27], v[26:27], v[246:247], v[246:247] op_sel:[0,0,0] op_sel_hi:[1,0,0]\n" \
  "v_pk_fma_f32 v[30:31], v[30:31], v[246:247], v[246:247] op_sel:[0,0,0] op_sel_hi:[1,0,0]\n" \
  "v_rcp_f32 v24, v24\n" \
  "v_rcp_f32 v28, v28\n" \
  "v_rcp_f32 v25, v25\n" \
  "v_rcp_f32 v29, v29\n" \
  "v_rcp_f32 v26, v26\n" \
  "v_rcp_f32 v30, v30\n" \
  "v_rcp_f32 v27, v27\n" \
  "v_rcp_f32 v31, v31\n" \
  "v_pk_mul_f32 v[24:25], v[24:25], v[8:9]\n" \
  "v_pk_mul_f32 v[28:29], v[28:29], v[12:13]\n" \
  "v_pk_mul_f32 v[26:27], v[26:27], v[10:11]\n" \
  "v_pk_mul_f32 v[30:31], v[30:31], v[14:15]\n" \
  "v_cvt_pk_bf16_f32 v20, v24, v25\n" \
  "v_cvt_pk_bf16_f32 v22, v28, v29\n" \
  "v_cvt_pk_bf16_f32 v21, v26, v27\n" \
  "v_cvt_pk_bf16_f32 v23, v30, v31\n" \
  "v_min_u32 v20, v253, v20\n" \
  "v_min_u32 v22, v253, v22\n" \
  "v_min_u32 v21, v253, v21\n" \
  "v_min_u32 v23, v253, v23\n" \
  "buffer_store_dwordx4 v[20:23], v252, s[84:87], 0 offen offset:1024 sc1\n" \
  "v_pk_fma_f32 v[0:1], v[90:91], v[228:229], v[90:91] op_sel:[0,0,1] op_sel_hi:[0,1,1]\n" \
  "v_pk_fma_f32 v[2:3], v[90:91], v[230:231], v[90:91] op_sel:[0,0,1] op_sel_hi:[0,1,1]\n" \
  "v_pk_fma_f32 v[4:5], v[90:91], v[232:233], v[90:91] op_sel:[0,0,1] op_sel_hi:[0,1,1]\n" \
  "v_pk_fma_f32 v[6:7], v[90:91], v[234:235], v[90:91] op_sel:[0,0,1] op_sel_hi:[0,1,1]\n" \
  "v_pk_fma_f32 v[8:9], v[90:91], v[236:237], v[90:91] op_sel:[0,0,1] op_sel_hi:[0,1,1]\n" \
  "v_pk_fma_f32 v[10:11], v[90:91], v[238:239], v[90:91] op_sel:[0,0,1] op_sel_hi:[0,1,1]\n" \
  "v_pk_fma_f32 v[12:13], v[90:91], v[240:241], v[90:91] op_sel:[0,0,1] op_sel_hi:[0,1,1]\n" \
  "v_pk_fma_f32 v[14:15], v[90:91], v[242:243], v[90:91] op_sel:[0,0,1] op_sel_hi:[0,1,1]\n" \
  "s_waitcnt lgkmcnt(0)\n" \
  "v_cmp_gt_u32 vcc, 4, v93\n" \
  "s_cbranch_vccz .Lp_c2_x\n" \
  "s_mov_b32 s92, 2000000\n" \
  ".Lp_c2_l:\n" \
  "s_sleep 1\n" \
  "ds_read_b32 v93, v92 offset:8\n" \
  "s_waitcnt lgkmcnt(0)\n" \
  "v_cmp_gt_u32 vcc, 4, v93\n" \
  "s_cbranch_vccz .Lp_c2_x\n" \
  "s_sub_u32 s92, s92, 1\n" \
  "s_cbranch_scc0 .Lp_c2_l\n" \
  ".Lp_c2_x:\n" \
  "ds_read_b128 v[84:87], v248 offset:0\n" \
  "ds_read_b128 v[88:91], v248 offset:1024\n" \
  "ds_read_b128 v[92:95], v248 offset:2048\n" \
  "ds_read_b128 v[96:99], v248 offset:3072\n" \
  "ds_read_b128 v[100:103], v248 offset:4096\n" \
  "ds_read_b128 v[104:107], v248 offset:5120\n" \
  "ds_read_b128 v[108:111], v248 offset:6144\n" \
  "ds_read_b128 v[112:115], v248 offset:7168\n" \
  "ds_read_b128 v[116:119], v248 offset:8192\n" \
  "ds_read_b128 v[120:123], v248 offset:9216\n" \
  "ds_read_b128 v[124:127], v248 offset:10240\n" \
  "ds_read_b128 v[128:131], v248 offset:11264\n" \
  "ds_read_b128 v[132:135], v248 offset:12288\n" \
  "ds_read_b128 v[136:139], v248 offset:13312\n" \
  "ds_read_b128 v[140:143], v248 offset:14336\n" \
  "ds_read_b128 v[144:147], v248 offset:15360\n" \
  "ds_read_b128 v[148:151], v248 offset:16384\n" \
  "ds_read_b128 v[152:155], v248 offset:17408\n" \
  "ds_read_b128 v[156:159], v248 offset:18432\n" \
  "ds_read_b128 v[160:163], v248 offset:19456\n" \
  "ds_read_b128 v[164:167], v248 offset:20480\n" \
  "ds_read_b128 v[168:171], v248 offset:21504\n" \
  "ds_read_b128 v[172:175], v248 offset:22528\n" \
  "ds_read_b128 v[176:179], v248 offset:23552\n" \
  "ds_read_b128 v[180:183], v248 offset:24576\n" \
  "ds_read_b128 v[184:187], v248 offset:25600\n" \
  "ds_read_b128 v[188:191], v248 offset:26624\n" \
  "ds_read_b128 v[192:195], v248 offset:27648\n" \
  "ds_read_b128 v[196:199], v248 offset:28672\n" \
  "ds_read_b128 v[200:203], v248 offset:29696\n" \
  "ds_read_b128 v[204:207], v248 offset:30720\n" \
  "ds_read_b128 v[208:211], v248 offset:31744\n" \
  "s_add_u32 s94, s42, 14680064\n" \
  "v_add_u32 v255, s94, v248\n" \
  "s_mov_b32 s92, 2000\n" \
  ".Lp_q2_l:\n" \
  "buffer_load_dword v44, v250, s[84:87], 0 offen offset:2048 sc1 ; RF\n" \
  "s_waitcnt vmcnt(0) ; RF\n" \
  "v_cmp_ne_u32 vcc, s68, v44 ; RF\n" \
  "s_cbranch_vccz .Lp_q2_g\n" \
  "s_sleep 4\n" \
  "s_sub_u32 s92, s92, 1\n" \
  "s_cbranch_scc0 .Lp_q2_l\n" \
  ".Lp_q2_g:\n" \
  "s_mov_b32 s94, s88\n" \
  "buffer_load_dwordx4 v[16:19], v255, s[84:87], s94 offen offset:0 sc1\n" \
  "s_nop 3\n" \
  "buffer_load_dwordx4 v[20:23], v255, s[84:87], s94 offen offset:1024 sc1\n" \
  "s_nop 3\n" \
  "s_add_u32 s94, s94, 2048\n" \
  "s_and_b32 s94, s94, 0x3fff\n" \
  "buffer_load_dwordx4 v[24:27], v255, s[84:87], s94 offen offset:0 sc1\n" \
  "s_nop 3\n" \
  "buffer_load_dwordx4 v[28:31], v255, s[84:87], s94 offen offset:1024 sc1\n" \
  "s_nop 3\n" \
  "s_add_u32 s94, s94, 2048\n" \
  "s_and_b32 s94, s94, 0x3fff\n" \
  "buffer_load_dwordx4 v[32:35], v255, s[84:87], s94 offen offset:0 sc1\n" \
  "s_nop 3\n" \
  "buffer_load_dwordx4 v[36:39], v255, s[84:87], s94 offen offset:1024 sc1\n" \
  "s_nop 3\n" \
  "s_add_u32 s94, s94, 2048\n" \
  "s_and_b32 s94, s94, 0x3fff\n" \
  "buffer_load_dwordx4 v[40:43], v255, s[84:87], s94 offen offset:0 sc1\n" \
  "s_nop 3\n" \
  "buffer_load_dwordx4 v[44:47], v255, s[84:87], s94 offen offset:1024 sc1\n" \
  "s_waitcnt lgkmcnt(0)\n" \
  "s_lshr_b32 s64, s48, 2\n" \
  "s_lshr_b32 s62, s42, 16\n" \
  "s_cmp_eq_u32 s64, s62\n" \
  "s_cselect_b32 s64, s60, s61\n" \
  "s_waitcnt vmcnt(7)\n" \
  "v_max_u32 v251, v16, v18\n" \
  "v_cmp_eq_u32 vcc, -1, v251\n" \
  "s_cbranch_vccnz .Lp_s31\n" \
  ".Lp_b31:\n" \
  "v_mfma_f32_16x16x32_bf16 v[0:3], v[84:87], v[16:19], v[0:3]\n" \
  "ds_read_b128 v[84:87], v248 offset:32768\n" \
  "s_add_u32 s94, s94, 2048\n" \
  "s_and_b32 s94, s94, 0x3fff\n" \
  "buffer_load_dwordx4 v[48:51], v255, s[84:87], s94 offen offset:0 sc1\n" \
  "v_mfma_f32_16x16x32_bf16 v[4:7], v[88:91], v[16:19], v[4:7]\n" \
  "ds_read_b128 v[88:91], v248 offset:33792\n" \
  "s_waitcnt vmcnt(7)\n" \
  "v_max_u32 v251, v20, v22\n" \
  "v_mfma_f32_16x16x32_bf16 v[8:11], v[92:95], v[16:19], v[8:11]\n" \
  "v_cmp_eq_u32 vcc, -1, v251\n" \
  "v_mfma_f32_16x16x32_bf16 v[12:15], v[96:99], v[16:19], v[12:15]\n" \
  "ds_read_b128 v[92:95], v248 offset:34816\n" \
  "s_cbranch_vccnz .Lp_s32\n" \
  ".Lp_b32:\n" \
  "v_mfma_f32_16x16x32_bf16 v[0:3], v[100:103], v[20:23], v[0:3]\n" \
  "ds_read_b128 v[96:99], v248 offset:35840\n" \
  "buffer_load_dwordx4 v[52:55], v255, s[84:87], s94 offen offset:1024 sc1\n" \
  "v_mfma_f32_16x16x32_bf16 v[4:7], v[104:107], v[20:23], v[4:7]\n" \
  "s_waitcnt vmcnt(7)\n" \
  "v_max_u32 v251, v24, v26\n" \
  "v_mfma_f32_16x16x32_bf16 v[8:11], v[108:111], v[20:23], v[8:11]\n" \
  "ds_read_b128 v[100:103], v248 offset:36864\n" \
  "v_cmp_eq_u32 vcc, -1, v251\n" \
  "v_mfma_f32_16x16x32_bf16 v[12:15], v[112:115], v[20:23], v[12:15]\n" \
  "ds_read_b128 v[104:107], v248 offset:37888\n" \
  "s_cbranch_vccnz .Lp_s33\n" \
  ".Lp_b33:\n" \
  "v_mfma_f32_16x16x32_bf16 v[0:3], v[116:119], v[24:27], v[0:3]\n" \
  "s_add_u32 s94, s94, 2048\n" \
  "s_and_b32 s94, s94, 0x3fff\n" \
  "buffer_load_dwordx4 v[56:59], v255, s[84:87], s94 offen offset:0 sc1\n" \
  "v_mfma_f32_16x16x32_bf16 v[4:7], v[120:123], v[24:27], v[4:7]\n" \
  "ds_read_b128 v[108:111], v248 offset:38912\n" \
  "s_waitcnt vmcnt(7)\n" \
  "v_max_u32 v251, v28, v30\n" \
  "v_mfma_f32_16x16x32_bf16 v[8:11], v[124:127], v[24:27], v[8:11]\n" \
  "ds_read_b128 v[112:115], v248 offset:39936\n" \
  "v_cmp_eq_u32 vcc, -1, v251\n" \
  "v_mfma_f32_16x16x32_bf16 v[12:15], v[128:131], v[24:27], v[12:15]\n" \
  "s_cbranch_vccnz .Lp_s34\n" \
  ".Lp_b34:\n" \
  "v_mfma_f32_16x16x32_bf16 v[0:3], v[132:135], v[28:31], v[0:3]\n" \
  "ds_read_b128 v[116:119], v248 offset:40960\n" \
  "buffer_load_dwordx4 v[60:63], v255, s[84:87], s94 offen offset:1024 sc1\n" \
  "v_mfma_f32_16x16x32_bf16 v[4:7], v[136:139], v[28:31], v[4:7]\n" \
  "ds_read_b128 v[120:123], v248 offset:41984\n" \
  "s_waitcnt vmcnt(7)\n" \
  "v_max_u32 v251, v32, v34\n" \
  "v_mfma_f32_16x16x32_bf16 v[8:11], v[140:143], v[28:31], v[8:11]\n" \
  "v_cmp_eq_u32 vcc, -1, v251\n" \
  "v_mfma_f32_16x16x32_bf16 v[12:15], v[144:147], v[28:31], v[12:15]\n" \
  "ds_read_b128 v[124:127], v248 offset:43008\n" \
  "s_cbranch_vccnz .Lp_s35\n" \
  ".Lp_b35:\n" \
  "v_mfma_f32_16x16x32_bf16 v[0:3], v[148:151], v[32:35], v[0:3]\n" \
  "ds_read_b128 v[128:131], v248 offset:44032\n" \
  "s_add_u32 s94, s94, 2048\n" \
  "s_and_b32 s94, s94, 0x3fff\n" \
  "buffer_load_dwordx4 v[64:67], v255, s[84:87], s94 offen offset:0 sc1\n" \
  "v_mfma_f32_16x16x32_bf16 v[4:7], v[152:155], v[32:35], v[4:7]\n" \
  "s_waitcnt vmcnt(7)\n" \
  "v_max_u32 v251, v36, v38\n" \
  "v_mfma_f32_16x16x32_bf16 v[8:11], v[156:159], v[32:35], v[8:11]\n" \
  "ds_read_b128 v[132:135], v248 offset:45056\n" \
  "v_cmp_eq_u32 vcc, -1, v251\n" \
  "v_mfma_f32_16x16x32_bf16 v[12:15], v[160:163], v[32:35], v[12:15]\n" \
  "ds_read_b128 v[136:139], v248 offset:46080\n" \
  "s_cbranch_vccnz .Lp_s36\n" \
  ".Lp_b36:\n" \
  "v_mfma_f32_16x16x32_bf16 v[0:3], v[164:167], v[36:39], v[0:3]\n" \
  "buffer_load_dwordx4 v[68:71], v255, s[84:87], s94 offen offset:1024 sc1\n" \
  "v_mfma_f32_16x16x32_bf16 v[4:7], v[168:171], v[36:39], v[4:7]\n" \
  "ds_read_b128 v[140:143], v248 offset:47104\n" \
  "s_waitcnt vmcnt(7)\n" \
  "v_max_u32 v251, v40, v42\n" \
  "v_mfma_f32_16x16x32_bf16 v[8:11], v[172:175], v[36:39], v[8:11]\n" \
  "ds_read_b128 v[144:147], v248 offset:48128\n" \
  "v_cmp_eq_u32 vcc, -1, v251\n" \
  "v_mfma_f32_16x16x32_bf16 v[12:15], v[176:179], v[36:39], v[12:15]\n" \
  "s_cbranch_vccnz .Lp_s37\n" \
  ".Lp_b37:\n" \
  "v_mfma_f32_16x16x32_bf16 v[0:3], v[180:183], v[40:43], v[0:3]\n" \
  "ds_read_b128 v[148:151], v248 offset:49152\n" \
  "s_add_u32 s94, s94, 2048\n" \
  "s_and_b32 s94, s94, 0x3fff\n" \
  "buffer_load_dwordx4 v[72:75], v255, s[84:87], s94 offen offset:0 sc1\n" \
  "v_mfma_f32_16x16x32_bf16 v[4:7], v[184:187], v[40:43], v[4:7]\n" \
  "ds_read_b128 v[152:155], v248 offset:50176\n" \
  "s_waitcnt vmcnt(7)\n" \
  "v_max_u32 v251, v44, v46\n" \
  "v_mfma_f32_16x16x32_bf16 v[8:11], v[188:191], v[40:43], v[8:11]\n" \
  "v_cmp_eq_u32 vcc, -1, v251\n" \
  "v_mfma_f32_16x16x32_bf16 v[12:15], v[192:195], v[40:43], v[12:15]\n" \
  "ds_read_b128 v[156:159], v248 offset:51200\n" \
  "s_cbranch_vccnz .Lp_s38\n" \
  ".Lp_b38:\n" \
  "v_mfma_f32_16x16x32_bf16 v[0:3], v[196:199], v[44:47], v[0:3]\n" \
  "ds_read_b128 v[160:163], v248 offset:52224\n" \
  "buffer_load_dwordx4 v[76:79], v255, s[84:87], s94 offen offset:1024 sc1\n" \
  "v_mfma_f32_16x16x32_bf16 v[4:7], v[200:203], v[44:47], v[4:7]\n" \
  "s_waitcnt vmcnt(7) lgkmcnt(15)\n" \
  "v_max_u32 v251, v48, v50\n" \
  "v_mfma_f32_16x16x32_bf16 v[8:11], v[204:207], v[44:47], v[8:11]\n" \
  "ds_read_b128 v[164:167], v248 offset:53248\n" \
  "v_cmp_eq_u32 vcc, -1, v251\n" \
  "v_mfma_f32_16x16x32_bf16 v[12:15], v[208:211], v[44:47], v[12:15]\n" \
  "ds_read_b128 v[168:171], v248 offset:54272\n" \
  "s_cbranch_vccnz .Lp_s39\n" \
  ".Lp_b39:\n" \
  "v_mfma_f32_16x16x32_bf16 v[0:3], v[84:87], v[48:51], v[0:3]\n" \
  "v_mfma_f32_16x16x32_bf16 v[4:7], v[88:91], v[48:51], v[4:7]\n" \
  "ds_read_b128 v[172:175], v248 offset:55296\n" \
  "s_waitcnt vmcnt(6) lgkmcnt(15)\n" \
  "v_max_u32 v251, v52, v54\n" \
  "v_mfma_f32_16x16x32_bf16 v[8:11], v[92:95], v[48:51], v[8:11]\n" \
  "ds_read_b128 v[176:179], v248 offset:56320\n" \
  "v_cmp_eq_u32 vcc, -1, v251\n" \
  "v_mfma_f32_16x16x32_bf16 v[12:15], v[96:99], v[48:51], v[12:15]\n" \
  "s_cbranch_vccnz .Lp_s40\n" \
  ".Lp_b40:\n" \
  "v_mfma_f32_16x16x32_bf16 v[0:3], v[100:103], v[52:55], v[0:3]\n" \
  "ds_read_b128 v[180:183], v248 offset:57344\n" \
  "v_mfma_f32_16x16x32_bf16 v[4:7], v[104:107], v[52:55], v[4:7]\n" \
  "ds_read_b128 v[184:187], v248 offset:58368\n" \
  "s_waitcnt vmcnt(5) lgkmcnt(14)\n" \
  "v_max_u32 v251, v56, v58\n" \
  "v_mfma_f32_16x16x32_bf16 v[8:11], v[108:111], v[52:55], v[8:11]\n" \
  "v_cmp_eq_u32 vcc, -1, v251\n" \
  "v_mfma_f32_16x16x32_bf16 v[12:15], v[112:115], v[52:55], v[12:15]\n" \
  "ds_read_b128 v[188:191], v248 offset:59392\n" \
  "s_cbranch_vccnz .Lp_s41\n" \
  ".Lp_b41:\n" \
  "v_mfma_f32_16x16x32_bf16 v[0:3], v[116:119], v[56:59], v[0:3]\n" \
  "ds_read_b128 v[192:195], v248 offset:60416\n" \
  "v_mov_b32 v16, s68\n" \
  "s_mov_b64 exec, 255\n" \
  "buffer_store_dwordx4 v[16:19], v248, s[84:87], s66 offen offset:3072 sc1\n" \
  "s_mov_b64 exec, -1\n" \
  "v_mfma_f32_16x16x32_bf16 v[4:7], v[120:123], v[56:59], v[4:7]\n" \
  "s_waitcnt vmcnt(5) lgkmcnt(12)\n" \
  "v_max_u32 v251, v60, v62\n" \
  "v_mfma_f32_16x16x32_bf16 v[8:11], v[124:127], v[56:59], v[8:11]\n" \
  "ds_read_b128 v[196:199], v248 offset:61440\n" \
  "v_cmp_eq_u32 vcc, -1, v251\n" \
  "v_mfma_f32_16x16x32_bf16 v[12:15], v[128:131], v[56:59], v[12:15]\n" \
  "ds_read_b128 v[200:203], v248 offset:62464\n" \
  "s_cbranch_vccnz .Lp_s42\n" \
  ".Lp_b42:\n" \
  "v_mfma_f32_16x16x32_bf16 v[0:3], v[132:135], v[60:63], v[0:3]\n" \
  "v_mfma_f32_16x16x32_bf16 v[4:7], v[136:139], v[60:63], v[4:7]\n" \
  "ds_read_b128 v[204:207], v248 offset:63488\n" \
  "s_waitcnt vmcnt(4) lgkmcnt(11)\n" \
  "v_max_u32 v251, v64, v66\n" \
  "s_add_u32 s94, s47, 0\n" \
  "buffer_load_dwordx4 v[132:135], v248, s[84:87], s94 offen offset:0 sc1\n" \
  "v_mfma_f32_16x16x32_bf16 v[8:11], v[140:143], v[60:63], v[8:11]\n" \
  "ds_read_b128 v[208:211], v248 offset:64512\n" \
  "v_cmp_eq_u32 vcc, -1, v251\n" \
  "v_mfma_f32_16x16x32_bf16 v[12:15], v[144:147], v[60:63], v[12:15]\n" \
  "s_add_u32 s94, s47, 0\n" \
  "buffer_load_dwordx4 v[136:139], v248, s[84:87], s94 offen offset:1024 sc1\n" \
  "s_cbranch_vccnz .Lp_s43\n" \
  ".Lp_b43:\n" \
  "v_mfma_f32_16x16x32_bf16 v[0:3], v[148:151], v[64:67], v[0:3]\n" \
  "v_mfma_f32_16x16x32_bf16 v[4:7], v[152:155], v[64:67], v[4:7]\n" \
  "s_waitcnt vmcnt(5) lgkmcnt(8)\n" \
  "v_max_u32 v251, v68, v70\n" \
  "s_add_u32 s94, s47, 0\n" \
  "buffer_load_dwordx4 v[140:143], v248, s[84:87], s94 offen offset:2048 sc1\n" \
  "v_mfma_f32_16x16x32_bf16 v[8:11], v[156:159], v[64:67], v[8:11]\n" \
  "v_cmp_eq_u32 vcc, -1, v251\n" \
  "v_mov_b32 v244, 131584\n" \
  "v_mov_b32 v245, 1\n" \
  "s_mov_b64 exec, 1\n" \
  "ds_add_u32 v244, v245 offset:16\n" \
  "s_mov_b64 exec, -1\n" \
  "v_mfma_f32_16x16x32_bf16 v[12:15], v[160:163], v[64:67], v[12:15]\n" \
  "s_add_u32 s94, s47, 0\n" \
  "buffer_load_dwordx4 v[144:147], v248, s[84:87], s94 offen offset:3072 sc1\n" \
  "s_cbranch_vccnz .Lp_s44\n" \
  ".Lp_b44:\n" \
  "v_mfma_f32_16x16x32_bf16 v[0:3], v[164:167], v[68:71], v[0:3]\n" \
  "v_mfma_f32_16x16x32_bf16 v[4:7], v[168:171], v[68:71], v[4:7]\n" \
  "s_waitcnt vmcnt(6) lgkmcnt(5)\n" \
  "v_max_u32 v251, v72, v74\n" \
  "s_add_u32 s94, s47, 32768\n" \
  "buffer_load_dwordx4 v[148:151], v248, s[84:87], s94 offen offset:0 sc1\n" \
  "v_mfma_f32_16x16x32_bf16 v[8:11], v[172:175], v[68:71], v[8:11]\n" \
  "v_cmp_eq_u32 vcc, -1, v251\n" \
  "v_mfma_f32_16x16x32_bf16 v[12:15], v[176:179], v[68:71], v[12:15]\n" \
  "s_add_u32 s94, s47, 32768\n" \
  "buffer_load_dwordx4 v[152:155], v248, s[84:87], s94 offen offset:1024 sc1\n" \
  "s_cbranch_vccnz .Lp_s45\n" \
  ".Lp_b45:\n" \
  "v_mfma_f32_16x16x32_bf16 v[0:3], v[180:183], v[72:75], v[0:3]\n" \
  "v_mfma_f32_16x16x32_bf16 v[4:7], v[184:187], v[72:75], v[4:7]\n" \
  "s_waitcnt vmcnt(7) lgkmcnt(1)\n" \
  "v_max_u32 v251, v76, v78\n" \
  "s_add_u32 s94, s47, 32768\n" \
  "buffer_load_dwordx4 v[156:159], v248, s[84:87], s94 offen offset:2048 sc1\n" \
  "v_mfma_f32_16x16x32_bf16 v[8:11], v[188:191], v[72:75], v[8:11]\n" \
  "v_cmp_eq_u32 vcc, -1, v251\n" \
  "v_mfma_f32_16x16x32_bf16 v[12:15], v[192:195], v[72:75], v[12:15]\n" \
  "s_add_u32 s94, s47, 32768\n" \
  "buffer_load_dwordx4 v[160:163], v248, s[84:87], s94 offen offset:3072 sc1\n" \
  "s_cbranch_vccnz .Lp_s46\n" \
  ".Lp_b46:\n" \
  "v_mfma_f32_16x16x32_bf16 v[0:3], v[196:199], v[76:79], v[0:3]\n" \
  "v_mfma_f32_16x16x32_bf16 v[4:7], v[200:203], v[76:79], v[4:7]\n" \
  "v_mfma_f32_16x16x32_bf16 v[8:11], v[204:207], v[76:79], v[8:11]\n" \
  "v_mfma_f32_16x16x32_bf16 v[12:15], v[208:211], v[76:79], v[12:15]\n" \
  "s_add_u32 s94, s43, 16777216\n" \
  "s_nop 3\n" \
  "v_cvt_pk_bf16_f32 v16, v0, v1\n" \
  "v_cvt_pk_bf16_f32 v17, v2, v3\n" \
  "v_cvt_pk_bf16_f32 v18, v4, v5\n" \
  "v_cvt_pk_bf16_f32 v19, v6, v7\n" \
  "v_min_u32 v16, -2, v16\n" \
  "v_min_u32 v17, -2, v17\n" \
  "v_min_u32 v18, -2, v18\n" \
  "v_min_u32 v19, -2, v19\n" \
  "buffer_store_dwordx4 v[16:19], v248, s[84:87], s94 offen offset:0 sc1\n" \
  "v_cvt_pk_bf16_f32 v20, v8, v9\n" \
  "v_cvt_pk_bf16_f32 v21, v10, v11\n" \
  "v_cvt_pk_bf16_f32 v22, v12, v13\n" \
  "v_cvt_pk_bf16_f32 v23, v14, v15\n" \
  "v_min_u32 v20, -2, v20\n" \
  "v_min_u32 v21, -2, v21\n" \
  "v_min_u32 v22, -2, v22\n" \
  "v_min_u32 v23, -2, v23\n" \
  "buffer_store_dwordx4 v[20:23], v248, s[84:87], s94 offen offset:1024 sc1\n" \
  "v_pk_add_f32 v[24:25], v[0:1], v[2:3]\n" \
  "v_pk_mul_f32 v[28:29], v[0:1], v[0:1]\n" \
  "v_pk_add_f32 v[26:27], v[4:5], v[6:7]\n" \
  "v_pk_mul_f32 v[30:31], v[2:3], v[2:3]\n" \
  "v_pk_fma_f32 v[28:29], v[4:5], v[4:5], v[28:29]\n" \
  "v_pk_fma_f32 v[30:31], v[6:7], v[6:7], v[30:31]\n" \
  "v_pk_fma_f32 v[28:29], v[8:9], v[8:9], v[28:29]\n" \
  "v_pk_add_f32 v[24:25], v[24:25], v[8:9]\n" \
  "v_pk_fma_f32 v[30:31], v[10:11], v[10:11], v[30:31]\n" \
  "v_pk_add_f32 v[26:27], v[26:27], v[10:11]\n" \
  "v_pk_fma_f32 v[28:29], v[12:13], v[12:13], v[28:29]\n" \
  "v_pk_add_f32 v[24:25], v[24:25], v[12:13]\n" \
  "v_pk_fma_f32 v[30:31], v[14:15], v[14:15], v[30:31]\n" \
  "v_pk_add_f32 v[26:27], v[26:27], v[14:15]\n" \
  "v_pk_add_f32 v[24:25], v[24:25], v[26:27]\n" \
  "v_pk_add_f32 v[28:29], v[28:29], v[30:31]\n" \
  "v_add_f32 v24, v24, v25\n" \
  "v_add_f32 v25, v28, v29\n" \
  "s_add_u32 s94, s44, 26345472\n" \
  "s_nop 0\n" \
  "v_permlane16_swap_b32 v24, v25\n" \
  "v_add_f32 v24, v24, v25\n" \
  "v_mov_b32 v25, v24\n" \
  "s_nop 1\n" \
  "v_permlane32_swap_b32 v24, v25\n" \
  "v_add_f32 v24, v24, v25\n" \
  "v_mov_b32 v25, v24\n" \
  "s_nop 1\n" \
  "v_permlane16_swap_b32 v24, v25\n" \
  "v_min_u32 v24, -2, v24\n" \
  "v_min_u32 v25, -2, v25\n" \
  "s_mov_b64 exec, 0xffff\n" \
  "buffer_store_dwordx2 v[24:25], v248, s[84:87], s94 offen sc1\n" \
  "s_mov_b64 exec, -1\n" \
  "s_barrier\n" \
  "s_lshl_b32 s60, s56, 12\n" \
  "s_add_u32 m0, s60, 16384\n" \
  "s_add_u32 s94, s47, 196608\n" \
  "buffer_load_dwordx4 v248, s[84:87], s94 offen offset:0 sc1 lds\n" \
  "buffer_load_dwordx4 v248, s[84:87], s94 offen offset:1024 sc1 lds\n" \
  "buffer_load_dwordx4 v248, s[84:87], s94 offen offset:2048 sc1 lds\n" \
  "buffer_load_dwordx4 v248, s[84:87], s94 offen offset:3072 sc1 lds\n" \
  "s_add_u32 s94, s47, 65536\n" \
  "buffer_load_dwordx4 v[164:167], v248, s[84:87], s94 offen offset:0 sc1\n" \
  "buffer_load_dwordx4 v[168:171], v248, s[84:87], s94 offen offset:1024 sc1\n" \
  "buffer_load_dwordx4 v[172:175], v248, s[84:87], s94 offen offset:2048 sc1\n" \
  "buffer_load_dwordx4 v[176:179], v248, s[84:87], s94 offen offset:3072 sc1\n" \
  "v_pk_add_f32 v[228:229], v[228:229], v[0:1]\n" \
  "v_pk_add_f32 v[230:231], v[230:231], v[2:3]\n" \
  "v_pk_add_f32 v[232:233], v[232:233], v[4:5]\n" \
  "v_pk_add_f32 v[234:235], v[234:235], v[6:7]\n" \
  "v_pk_add_f32 v[236:237], v[236:237], v[8:9]\n" \
  "v_pk_add_f32 v[238:239], v[238:239], v[10:11]\n" \
  "v_pk_add_f32 v[240:241], v[240:241], v[12:13]\n" \
  "v_pk_add_f32 v[242:243], v[242:243], v[14:15]\n" \
  "v_add_u32 v251, s60, v248\n" \
  "ds_write_b128 v251, v[228:231] offset:49152\n" \
  "ds_write_b128 v251, v[232:235] offset:50176\n" \
  "ds_write_b128 v251, v[236:239] offset:51200\n" \
  "v_and_b32 v33, 0x70, v248\n" \
  "v_lshlrev_b32 v33, 3, v33\n" \
  "s_mov_b32 s92, 2000\n" \
  ".Lp_q3_l:\n" \
  "buffer_load_dword v44, v33, s[84:87], s65 offen offset:3072 sc1 ; RF\n" \
  "s_waitcnt vmcnt(0) ; RF\n" \
  "v_cmp_ne_u32 vcc, s68, v44 ; RF\n" \
  "s_cbranch_vccz .Lp_q3_g\n" \
  "s_sleep 4\n" \
  "s_sub_u32 s92, s92, 1\n" \
  "s_cbranch_scc0 .Lp_q3_l\n" \
  ".Lp_q3_g:\n" \
  "s_branch .Lp_G\n" \
  ".Lp_e:\n" \
  "v_mov_b32 v0, 0x3f803f80\n" \
  "v_mov_b32 v1, 0x3f803f80\n" \
  "v_mov_b32 v2, 0x3f803f80\n" \
  "v_mov_b32 v3, 0x3f803f80\n" \
  "s_lshl_b32 s61, s41, 10\n" \
  "v_add_u32 v251, s61, v249\n" \
  "ds_read_b128 v[84:87], v251 offset:0\n" \
  "ds_read_b128 v[88:91], v251 offset:4096\n" \
  "ds_read_b128 v[92:95], v251 offset:8192\n" \
  "ds_read_b128 v[96:99], v251 offset:12288\n" \
  "ds_read_b128 v[100:103], v251 offset:16384\n" \
  "ds_read_b128 v[104:107], v251 offset:20480\n" \
  "ds_read_b128 v[108:111], v251 offset:24576\n" \
  "ds_read_b128 v[112:115], v251 offset:28672\n" \
  "s_waitcnt lgkmcnt(4)\n" \
  "v_mfma_f32_16x16x32_bf16 v[4:7], v[0:3], v[84:87], 0\n" \
  "v_mfma_f32_16x16x32_bf16 v[4:7], v[0:3], v[88:91], v[4:7]\n" \
  "v_mfma_f32_16x16x32_bf16 v[4:7], v[0:3], v[92:95], v[4:7]\n" \
  "v_mfma_f32_16x16x32_bf16 v[4:7], v[0:3], v[96:99], v[4:7]\n" \
  "s_waitcnt lgkmcnt(0)\n" \
  "v_mfma_f32_16x16x32_bf16 v[4:7], v[0:3], v[100:103], v[4:7]\n" \
  "v_mfma_f32_16x16x32_bf16 v[4:7], v[0:3], v[104:107], v[4:7]\n" \
  "v_mfma_f32_16x16x32_bf16 v[4:7], v[0:3], v[108:111], v[4:7]\n" \
  "v_mfma_f32_16x16x32_bf16 v[4:7], v[0:3], v[112:115], v[4:7]\n" \
  "ds_read_b128 v[84:87], v251 offset:32768\n" \
  "ds_read_b128 v[88:91], v251 offset:36864\n" \
  "ds_read_b128 v[92:95], v251 offset:40960\n" \
  "ds_read_b128 v[96:99], v251 offset:45056\n" \
  "ds_read_b128 v[100:103], v251 offset:49152\n" \
  "ds_read_b128 v[104:107], v251 offset:53248\n" \
  "ds_read_b128 v[108:111], v251 offset:57344\n" \
  "ds_read_b128 v[112:115], v251 offset:61440\n" \
  "s_waitcnt lgkmcnt(4)\n" \
  "v_mfma_f32_16x16x32_bf16 v[4:7], v[0:3], v[84:87], v[4:7]\n" \
  "v_mfma_f32_16x16x32_bf16 v[4:7], v[0:3], v[88:91], v[4:7]\n" \
  "v_mfma_f32_16x16x32_bf16 v[4:7], v[0:3], v[92:95], v[4:7]\n" \
  "v_mfma_f32_16x16x32_bf16 v[4:7], v[0:3], v[96:99], v[4:7]\n" \
  "s_waitcnt lgkmcnt(0)\n" \
  "v_mfma_f32_16x16x32_bf16 v[4:7], v[0:3], v[100:103], v[4:7]\n" \
  "v_mfma_f32_16x16x32_bf16 v[4:7], v[0:3], v[104:107], v[4:7]\n" \
  "v_mfma_f32_16x16x32_bf16 v[4:7], v[0:3], v[108:111], v[4:7]\n" \
  "v_mfma_f32_16x16x32_bf16 v[4:7], v[0:3], v[112:115], v[4:7]\n" \
  "s_lshl_b32 s62, s41, 6\n" \
  "v_lshrrev_b32 v252, 2, v248\n" \
  "v_add_u32 v252, s62, v252\n" \
  "v_add_u32 v251, 0x20000, v252\n" \
  "s_mov_b64 exec, 0xffff\n" \
  "s_nop 2\n" \
  "ds_write_b32 v251, v4\n" \
  "s_mov_b64 exec, -1\n" \
  "v_mov_b32 v8, 131584\n" \
  "v_mov_b32 v9, 1\n" \
  "s_mov_b64 exec, 1\n" \
  "ds_add_u32 v8, v9 offset:0\n" \
  "s_mov_b64 exec, -1\n" \
  "s_lshl_b32 s61, s41, 14\n" \
  "s_add_u32 s64, s46, s61\n" \
  "s_add_u32 s94, s47, 0\n" \
  "buffer_load_dwordx4 v[132:135], v248, s[84:87], s94 offen offset:0 sc1\n" \
  "buffer_load_dwordx4 v[136:139], v248, s[84:87], s94 offen offset:1024 sc1\n" \
  "buffer_load_dwordx4 v[140:143], v248, s[84:87], s94 offen offset:2048 sc1\n" \
  "buffer_load_dwordx4 v[144:147], v248, s[84:87], s94 offen offset:3072 sc1\n" \
  "s_add_u32 s94, s47, 32768\n" \
  "buffer_load_dwordx4 v[148:151], v248, s[84:87], s94 offen offset:0 sc1\n" \
  "buffer_load_dwordx4 v[152:155], v248, s[84:87], s94 offen offset:1024 sc1\n" \
  "buffer_load_dwordx4 v[156:159], v248, s[84:87], s94 offen offset:2048 sc1\n" \
  "buffer_load_dwordx4 v[160:163], v248, s[84:87], s94 offen offset:3072 sc1\n" \
  "v_mov_b32 v8, 131584\n" \
  "s_mov_b32 s92, 2000000\n" \
  ".Lp_c1_l:\n" \
  "s_sleep 1\n" \
  "ds_read_b32 v9, v8 offset:4\n" \
  "s_waitcnt lgkmcnt(0)\n" \
  "v_cmp_gt_u32 vcc, 4, v9\n" \
  "s_cbranch_vccz .Lp_c1_x\n" \
  "s_sub_u32 s92, s92, 1\n" \
  "s_cbranch_scc0 .Lp_c1_l\n" \
  ".Lp_c1_x:\n" \
  "s_sleep 6\n" \
  "s_add_u32 s94, s45, 0\n" \
  "buffer_load_dwordx4 v[16:19], v248, s[84:87], s94 offen offset:0 sc1\n" \
  "buffer_load_dwordx4 v[20:23], v248, s[84:87], s94 offen offset:1024 sc1\n" \
  "buffer_load_dwordx4 v[24:27], v248, s[84:87], s94 offen offset:2048 sc1\n" \
  "buffer_load_dwordx4 v[28:31], v248, s[84:87], s94 offen offset:3072 sc1\n" \
  "s_add_u32 s94, s45, 4096\n" \
  "buffer_load_dwordx4 v[32:35], v248, s[84:87], s94 offen offset:0 sc1\n" \
  "buffer_load_dwordx4 v[36:39], v248, s[84:87], s94 offen offset:1024 sc1\n" \
  "buffer_load_dwordx4 v[40:43], v248, s[84:87], s94 offen offset:2048 sc1\n" \
  "buffer_load_dwordx4 v[44:47], v248, s[84:87], s94 offen offset:3072 sc1\n" \
  "s_add_u32 s94, s45, 8192\n" \
  "buffer_load_dwordx4 v[48:51], v248, s[84:87], s94 offen offset:0 sc1\n" \
  "buffer_load_dwordx4 v[52:55], v248, s[84:87], s94 offen offset:1024 sc1\n" \
  "buffer_load_dwordx4 v[56:59], v248, s[84:87], s94 offen offset:2048 sc1\n" \
  "buffer_load_dwordx4 v[60:63], v248, s[84:87], s94 offen offset:3072 sc1\n" \
  "s_add_u32 s94, s45, 12288\n" \
  "buffer_load_dwordx4 v[64:67], v248, s[84:87], s94 offen offset:0 sc1\n" \
  "buffer_load_dwordx4 v[68:71], v248, s[84:87], s94 offen offset:1024 sc1\n" \
  "buffer_load_dwordx4 v[72:75], v248, s[84:87], s94 offen offset:2048 sc1\n" \
  "buffer_load_dwordx4 v[76:79], v248, s[84:87], s94 offen offset:3072 sc1\n" \
  "s_add_u32 s94, s47, 65536\n" \
  "buffer_load_dwordx4 v[164:167], v248, s[84:87], s94 offen offset:0 sc1\n" \
  "buffer_load_dwordx4 v[168:171], v248, s[84:87], s94 offen offset:1024 sc1\n" \
  "buffer_load_dwordx4 v[172:175], v248, s[84:87], s94 offen offset:2048 sc1\n" \
  "buffer_load_dwordx4 v[176:179], v248, s[84:87], s94 offen offset:3072 sc1\n" \
  "s_add_u32 s94, s47, 98304\n" \
  "buffer_load_dwordx4 v[180:183], v248, s[84:87], s94 offen offset:0 sc1\n" \
  "buffer_load_dwordx4 v[184:187], v248, s[84:87], s94 offen offset:1024 sc1\n" \
  "buffer_load_dwordx4 v[188:191], v248, s[84:87], s94 offen offset:2048 sc1\n" \
  "buffer_load_dwordx4 v[192:195], v248, s[84:87], s94 offen offset:3072 sc1\n" \
  "s_add_u32 s94, s47, 131072\n" \
  "buffer_load_dwordx4 v[196:199], v248, s[84:87], s94 offen offset:0 sc1\n" \
  "buffer_load_dwordx4 v[200:203], v248, s[84:87], s94 offen offset:1024 sc1\n" \
  "buffer_load_dwordx4 v[204:207], v248, s[84:87], s94 offen offset:2048 sc1\n" \
  "buffer_load_dwordx4 v[208:211], v248, s[84:87], s94 offen offset:3072 sc1\n" \
  "v_add_u32 v251, s61, v248\n" \
  "s_waitcnt vmcnt(24)\n" \
  "ds_write_b128 v251, v[16:19] offset:0\n" \
  "ds_write_b128 v251, v[20:23] offset:1024\n" \
  "ds_write_b128 v251, v[24:27] offset:2048\n" \
  "ds_write_b128 v251, v[28:31] offset:3072\n" \
  "s_waitcnt vmcnt(20)\n" \
  "ds_write_b128 v251, v[32:35] offset:4096\n" \
  "ds_write_b128 v251, v[36:39] offset:5120\n" \
  "ds_write_b128 v251, v[40:43] offset:6144\n" \
  "ds_write_b128 v251, v[44:47] offset:7168\n" \
  "s_waitcnt vmcnt(16)\n" \
  "ds_write_b128 v251, v[48:51] offset:8192\n" \
  "ds_write_b128 v251, v[52:55] offset:9216\n" \
  "ds_write_b128 v251, v[56:59] offset:10240\n" \
  "ds_write_b128 v251, v[60:63] offset:11264\n" \
  "s_waitcnt vmcnt(12)\n" \
  "ds_write_b128 v251, v[64:67] offset:12288\n" \
  "ds_write_b128 v251, v[68:71] offset:13312\n" \
  "ds_write_b128 v251, v[72:75] offset:14336\n" \
  "ds_write_b128 v251, v[76:79] offset:15360\n" \
  "v_mov_b32 v8, 131584\n" \
  "v_mov_b32 v9, 1\n" \
  "s_mov_b64 exec, 1\n" \
  "ds_add_u32 v8, v9 offset:8\n" \
  "s_mov_b64 exec, -1\n" \
  "s_waitcnt lgkmcnt(0)\n" \
  "v_mov_b32 v8, 131584\n" \
  "s_mov_b32 s92, 2000000\n" \
  ".Lp_c3_l:\n" \
  "s_sleep 1\n" \
  "ds_read_b32 v9, v8 offset:12\n" \
  "s_waitcnt lgkmcnt(0)\n" \
  "v_cmp_gt_u32 vcc, 4, v9\n" \
  "s_cbranch_vccz .Lp_c3_x\n" \
  "s_sub_u32 s92, s92, 1\n" \
  "s_cbranch_scc0 .Lp_c3_l\n" \
  ".Lp_c3_x:\n" \
  "s_sleep 12\n" \
  "s_add_u32 s94, s64, 0\n" \
  "buffer_load_dwordx4 v[16:19], v248, s[84:87], s94 offen offset:0 sc1\n" \
  "buffer_load_dwordx4 v[20:23], v248, s[84:87], s94 offen offset:1024 sc1\n" \
  "buffer_load_dwordx4 v[24:27], v248, s[84:87], s94 offen offset:2048 sc1\n" \
  "buffer_load_dwordx4 v[28:31], v248, s[84:87], s94 offen offset:3072 sc1\n" \
  "s_add_u32 s94, s64, 4096\n" \
  "buffer_load_dwordx4 v[32:35], v248, s[84:87], s94 offen offset:0 sc1\n" \
  "buffer_load_dwordx4 v[36:39], v248, s[84:87], s94 offen offset:1024 sc1\n" \
  "buffer_load_dwordx4 v[40:43], v248, s[84:87], s94 offen offset:2048 sc1\n" \
  "buffer_load_dwordx4 v[44:47], v248, s[84:87], s94 offen offset:3072 sc1\n" \
  "s_add_u32 s94, s64, 8192\n" \
  "buffer_load_dwordx4 v[48:51], v248, s[84:87], s94 offen offset:0 sc1\n" \
  "buffer_load_dwordx4 v[52:55], v248, s[84:87], s94 offen offset:1024 sc1\n" \
  "buffer_load_dwordx4 v[56:59], v248, s[84:87], s94 offen offset:2048 sc1\n" \
  "buffer_load_dwordx4 v[60:63], v248, s[84:87], s94 offen offset:3072 sc1\n" \
  "s_add_u32 s94, s64, 12288\n" \
  "buffer_load_dwordx4 v[64:67], v248, s[84:87], s94 offen offset:0 sc1\n" \
  "buffer_load_dwordx4 v[68:71], v248, s[84:87], s94 offen offset:1024 sc1\n" \
  "buffer_load_dwordx4 v[72:75], v248, s[84:87], s94 offen offset:2048 sc1\n" \
  "buffer_load_dwordx4 v[76:79], v248, s[84:87], s94 offen offset:3072 sc1\n" \
  "s_min_u32 s62, s41, 2\n" \
  "s_lshl_b32 s62, s62, 14\n" \
  "s_add_u32 s63, s46, s62\n" \
  "s_cmp_eq_u32 s41, 2\n" \
  "s_cbranch_scc1 .Lp_e2\n" \
  "s_add_u32 s94, s63, 65536\n" \
  "buffer_load_dwordx4 v[84:87], v248, s[84:87], s94 offen offset:0 sc1\n" \
  "buffer_load_dwordx4 v[88:91], v248, s[84:87], s94 offen offset:1024 sc1\n" \
  "buffer_load_dwordx4 v[92:95], v248, s[84:87], s94 offen offset:2048 sc1\n" \
  "buffer_load_dwordx4 v[96:99], v248, s[84:87], s94 offen offset:3072 sc1\n" \
  "s_add_u32 s94, s63, 69632\n" \
  "buffer_load_dwordx4 v[100:103], v248, s[84:87], s94 offen offset:0 sc1\n" \
  "buffer_load_dwordx4 v[104:107], v248, s[84:87], s94 offen offset:1024 sc1\n" \
  "buffer_load_dwordx4 v[108:111], v248, s[84:87], s94 offen offset:2048 sc1\n" \
  "buffer_load_dwordx4 v[112:115], v248, s[84:87], s94 offen offset:3072 sc1\n" \
  "s_add_u32 s94, s63, 73728\n" \
  "buffer_load_dwordx4 v[116:119], v248, s[84:87], s94 offen offset:0 sc1\n" \
  "buffer_load_dwordx4 v[120:123], v248, s[84:87], s94 offen offset:1024 sc1\n" \
  "buffer_load_dwordx4 v[124:127], v248, s[84:87], s94 offen offset:2048 sc1\n" \
  "buffer_load_dwordx4 v[128:131], v248, s[84:87], s94 offen offset:3072 sc1\n" \
  "s_add_u32 s94, s63, 77824\n" \
  "buffer_load_dwordx4 v[228:231], v248, s[84:87], s94 offen offset:0 sc1\n" \
  "buffer_load_dwordx4 v[232:235], v248, s[84:87], s94 offen offset:1024 sc1\n" \
  "buffer_load_dwordx4 v[236:239], v248, s[84:87], s94 offen offset:2048 sc1\n" \
  "buffer_load_dwordx4 v[240:243], v248, s[84:87], s94 offen offset:3072 sc1\n" \
  ".Lp_e2:\n" \
  "s_add_u32 s94, s47, 163840\n" \
  "buffer_load_dwordx4 v[212:215], v248, s[84:87], s94 offen offset:0 sc1\n" \
  "buffer_load_dwordx4 v[216:219], v248, s[84:87], s94 offen offset:1024 sc1\n" \
  "buffer_load_dwordx4 v[220:223], v248, s[84:87], s94 offen offset:2048 sc1\n" \
  "buffer_load_dwordx4 v[224:227], v248, s[84:87], s94 offen offset:3072 sc1\n" \
  "v_add_u32 v251, s61, v249\n" \
  "s_waitcnt vmcnt(16)\n" \
  "v_mfma_f32_16x16x32_bf16 v[4:7], v[0:3], v[16:19], 0\n" \
  "ds_write_b128 v251, v[16:19] offset:0\n" \
  "v_mfma_f32_16x16x32_bf16 v[4:7], v[0:3], v[20:23], v[4:7]\n" \
  "ds_write_b128 v251, v[20:23] offset:1024\n" \
  "v_mfma_f32_16x16x32_bf16 v[4:7], v[0:3], v[24:27], v[4:7]\n" \
  "ds_write_b128 v251, v[24:27] offset:2048\n" \
  "v_mfma_f32_16x16x32_bf16 v[4:7], v[0:3], v[28:31], v[4:7]\n" \
  "ds_write_b128 v251, v[28:31] offset:3072\n" \
  "s_waitcnt vmcnt(12)\n" \
  "v_mfma_f32_16x16x32_bf16 v[4:7], v[0:3], v[32:35], v[4:7]\n" \
  "ds_write_b128 v251, v[32:35] offset:4096\n" \
  "v_mfma_f32_16x16x32_bf16 v[4:7], v[0:3], v[36:39], v[4:7]\n" \
  "ds_write_b128 v251, v[36:39] offset:5120\n" \
  "v_mfma_f32_16x16x32_bf16 v[4:7], v[0:3], v[40:43], v[4:7]\n" \
  "ds_write_b128 v251, v[40:43] offset:6144\n" \
  "v_mfma_f32_16x16x32_bf16 v[4:7], v[0:3], v[44:47], v[4:7]\n" \
  "ds_write_b128 v251, v[44:47] offset:7168\n" \
  "s_waitcnt vmcnt(8)\n" \
  "v_mfma_f32_16x16x32_bf16 v[4:7], v[0:3], v[48:51], v[4:7]\n" \
  "ds_write_b128 v251, v[48:51] offset:8192\n" \
  "v_mfma_f32_16x16x32_bf16 v[4:7], v[0:3], v[52:55], v[4:7]\n" \
  "ds_write_b128 v251, v[52:55] offset:9216\n" \
  "v_mfma_f32_16x16x32_bf16 v[4:7], v[0:3], v[56:59], v[4:7]\n" \
  "ds_write_b128 v251, v[56:59] offset:10240\n" \
  "v_mfma_f32_16x16x32_bf16 v[4:7], v[0:3], v[60:63], v[4:7]\n" \
  "ds_write_b128 v251, v[60:63] offset:11264\n" \
  "s_waitcnt vmcnt(4)\n" \
  "v_mfma_f32_16x16x32_bf16 v[4:7], v[0:3], v[64:67], v[4:7]\n" \
  "ds_write_b128 v251, v[64:67] offset:12288\n" \
  "v_mfma_f32_16x16x32_bf16 v[4:7], v[0:3], v[68:71], v[4:7]\n" \
  "ds_write_b128 v251, v[68:71] offset:13312\n" \
  "v_mfma_f32_16x16x32_bf16 v[4:7], v[0:3], v[72:75], v[4:7]\n" \
  "ds_write_b128 v251, v[72:75] offset:14336\n" \
  "v_mfma_f32_16x16x32_bf16 v[4:7], v[0:3], v[76:79], v[4:7]\n" \
  "ds_write_b128 v251, v[76:79] offset:15360\n" \
  "s_add_u32 s62, s61, 0x20000\n" \
  "s_cmp_eq_u32 s41, 3\n" \
  "s_cselect_b32 s62, 0, s62\n" \
  "v_add_u32 v251, s62, v248\n" \
  "s_barrier\n" \
  "s_lshl_b32 s65, s48, 12\n" \
  "s_add_u32 s65, s65, 27262976\n" \
  "s_lshr_b32 s61, s57, 4\n" \
  "s_add_u32 s63, s61, 128\n" \
  "s_and_b32 s63, s63, 0x380\n" \
  "s_lshr_b32 s64, s48, 2\n" \
  "s_lshr_b32 s62, s42, 16\n" \
  "s_cmp_eq_u32 s64, s62\n" \
  "s_cselect_b32 s64, s63, s61\n" \
  "s_cmp_eq_u32 s41, 2\n" \
  "s_cbranch_scc1 .Lp_e3\n" \
  "s_waitcnt vmcnt(4)\n" \
  "v_mfma_f32_16x16x32_bf16 v[8:11], v[0:3], v[84:87], 0\n" \
  "ds_write_b128 v251, v[84:87] offset:0\n" \
  "v_mfma_f32_16x16x32_bf16 v[8:11], v[0:3], v[88:91], v[8:11]\n" \
  "ds_write_b128 v251, v[88:91] offset:1024\n" \
  "v_mfma_f32_16x16x32_bf16 v[8:11], v[0:3], v[92:95], v[8:11]\n" \
  "ds_write_b128 v251, v[92:95] offset:2048\n" \
  "v_mfma_f32_16x16x32_bf16 v[8:11], v[0:3], v[96:99], v[8:11]\n" \
  "ds_write_b128 v251, v[96:99] offset:3072\n" \
  "v_mfma_f32_16x16x32_bf16 v[8:11], v[0:3], v[100:103], v[8:11]\n" \
  "ds_write_b128 v251, v[100:103] offset:4096\n" \
  "v_mfma_f32_16x16x32_bf16 v[8:11], v[0:3], v[104:107], v[8:11]\n" \
  "ds_write_b128 v251, v[104:107] offset:5120\n" \
  "v_mfma_f32_16x16x32_bf16 v[8:11], v[0:3], v[108:111], v[8:11]\n" \
  "ds_write_b128 v251, v[108:111] offset:6144\n" \
  "v_mfma_f32_16x16x32_bf16 v[8:11], v[0:3], v[112:115], v[8:11]\n" \
  "ds_write_b128 v251, v[112:115] offset:7168\n" \
  "v_mfma_f32_16x16x32_bf16 v[8:11], v[0:3], v[116:119], v[8:11]\n" \
  "ds_write_b128 v251, v[116:119] offset:8192\n" \
  "v_mfma_f32_16x16x32_bf16 v[8:11], v[0:3], v[120:123], v[8:11]\n" \
  "ds_write_b128 v251, v[120:123] offset:9216\n" \
  "v_mfma_f32_16x16x32_bf16 v[8:11], v[0:3], v[124:127], v[8:11]\n" \
  "ds_write_b128 v251, v[124:127] offset:10240\n" \
  "v_mfma_f32_16x16x32_bf16 v[8:11], v[0:3], v[128:131], v[8:11]\n" \
  "ds_write_b128 v251, v[128:131] offset:11264\n" \
  "v_mfma_f32_16x16x32_bf16 v[8:11], v[0:3], v[228:231], v[8:11]\n" \
  "ds_write_b128 v251, v[228:231] offset:12288\n" \
  "v_mfma_f32_16x16x32_bf16 v[8:11], v[0:3], v[232:235], v[8:11]\n" \
  "ds_write_b128 v251, v[232:235] offset:13312\n" \
  "v_mfma_f32_16x16x32_bf16 v[8:11], v[0:3], v[236:239], v[8:11]\n" \
  "ds_write_b128 v251, v[236:239] offset:14336\n" \
  "v_mfma_f32_16x16x32_bf16 v[8:11], v[0:3], v[240:243], v[8:11]\n" \
  "ds_write_b128 v251, v[240:243] offset:15360\n" \
  ".Lp_e3:\n" \
  "s_lshl_b32 s60, s56, 12\n" \
  "s_add_u32 m0, s60, 16384\n" \
  "s_add_u32 s94, s47, 196608\n" \
  "buffer_load_dwordx4 v248, s[84:87], s94 offen offset:0 sc1 lds\n" \
  "buffer_load_dwordx4 v248, s[84:87], s94 offen offset:1024 sc1 lds\n" \
  "buffer_load_dwordx4 v248, s[84:87], s94 offen offset:2048 sc1 lds\n" \
  "buffer_load_dwordx4 v248, s[84:87], s94 offen offset:3072 sc1 lds\n" \
  "s_cmp_eq_u32 s41, 3\n" \
  "s_cselect_b32 s62, 0, 64\n" \
  "v_add_u32 v251, s62, v252\n" \
  "s_mov_b64 exec, 0xffff\n" \
  "s_nop 3\n" \
  "ds_write_b32 v252, v4 offset:64512\n" \
  "s_cmp_eq_u32 s41, 2\n" \
  "s_cbranch_scc1 .Lp_e4\n" \
  "ds_write_b32 v251, v8 offset:64704\n" \
  ".Lp_e4:\n" \
  "v_mov_b32 v12, 65024\n" \
  "v_mov_b32 v13, 0\n" \
  "ds_write_b32 v12, v13\n" \
  "s_mov_b64 exec, -1\n" \
  "s_waitcnt lgkmcnt(0)\n" \
  "v_and_b32 v33, 0x70, v248\n" \
  "v_lshlrev_b32 v33, 3, v33\n" \
  "s_mov_b32 s92, 2000\n" \
  ".Lp_q4_l:\n" \
  "buffer_load_dword v44, v33, s[84:87], s65 offen offset:3072 sc1 ; RF\n" \
  "s_waitcnt vmcnt(0) ; RF\n" \
  "v_cmp_ne_u32 vcc, s68, v44 ; RF\n" \
  "s_cbranch_vccz .Lp_q4_g\n" \
  "s_sleep 4\n" \
  "s_sub_u32 s92, s92, 1\n" \
  "s_cbranch_scc0 .Lp_q4_l\n" \
  ".Lp_q4_g:\n" \
  ".Lp_G:\n" \
  "s_lshl_b32 s94, s48, 14\n" \
  "s_add_u32 s94, s94, 16777216\n" \
  "v_add_u32 v255, s94, v248\n" \
  "s_mov_b32 s88, s58\n" \
  "s_lshl_b32 s89, s48, 10\n" \
  "s_add_u32 s89, s89, 26345472\n" \
  "s_mov_b32 s94, s88\n" \
  "buffer_load_dwordx4 v[16:19], v255, s[84:87], s94 offen offset:0 sc1\n" \
  "buffer_load_dwordx4 v[20:23], v255, s[84:87], s94 offen offset:1024 sc1\n" \
  "s_add_u32 s94, s94, 2048\n" \
  "s_and_b32 s94, s94, 0x3fff\n" \
  "buffer_load_dwordx4 v[24:27], v255, s[84:87], s94 offen offset:0 sc1\n" \
  "buffer_load_dwordx4 v[28:31], v255, s[84:87], s94 offen offset:1024 sc1\n" \
  "s_add_u32 s94, s94, 2048\n" \
  "s_and_b32 s94, s94, 0x3fff\n" \
  "buffer_load_dwordx4 v[32:35], v255, s[84:87], s94 offen offset:0 sc1\n" \
  "buffer_load_dwordx4 v[36:39], v255, s[84:87], s94 offen offset:1024 sc1\n" \
  "s_add_u32 s94, s94, 2048\n" \
  "s_and_b32 s94, s94, 0x3fff\n" \
  "buffer_load_dwordx4 v[40:43], v255, s[84:87], s94 offen offset:0 sc1\n" \
  "buffer_load_dwordx4 v[44:47], v255, s[84:87], s94 offen offset:1024 sc1\n" \
  "v_add_u32 v250, 0x20000, v248\n" \
  "s_waitcnt lgkmcnt(0)\n" \
  "s_barrier\n" \
  "v_lshrrev_b32 v253, 4, v248\n" \
  "v_and_b32 v253, 0x30, v253\n" \
  "v_add_u32 v253, 64512, v253\n" \
  "ds_read_b128 v[84:87], v249 offset:0\n" \
  "ds_read_b128 v[88:91], v249 offset:16384\n" \
  "ds_read_b128 v[92:95], v249 offset:32768\n" \
  "ds_read_b128 v[96:99], v249 offset:49152\n" \
  "ds_read_b128 v[100:103], v249 offset:1024\n" \
  "ds_read_b128 v[104:107], v249 offset:17408\n" \
  "ds_read_b128 v[108:111], v249 offset:33792\n" \
  "ds_read_b128 v[112:115], v249 offset:50176\n" \
  "ds_read_b128 v[116:119], v249 offset:2048\n" \
  "ds_read_b128 v[120:123], v249 offset:18432\n" \
  "ds_read_b128 v[124:127], v249 offset:34816\n" \
  "ds_read_b128 v[128:131], v249 offset:51200\n" \
  "v_lshrrev_b32 v252, 1, v248\n" \
  "v_and_b32 v252, 0x100, v252\n" \
  "v_and_b32 v251, 0xf0, v248\n" \
  "v_or_b32 v252, v252, v251\n" \
  "v_lshrrev_b32 v251, 5, v248\n" \
  "v_and_b32 v251, 8, v251\n" \
  "v_or_b32 v252, v252, v251\n" \
  "s_waitcnt vmcnt(7) lgkmcnt(8)\n" \
  "v_max_u32 v251, v16, v18\n" \
  "v_cmp_eq_u32 vcc, -1, v251\n" \
  "s_cbranch_vccnz .Lp_s47\n" \
  ".Lp_b47:\n" \
  "v_mfma_f32_16x16x32_bf16 v[0:3], v[84:87], v[16:19], 0\n" \
  "ds_read_b128 v[84:87], v249 offset:3072\n" \
  "s_add_u32 s94, s94, 2048\n" \
  "s_and_b32 s94, s94, 0x3fff\n" \
  "buffer_load_dwordx4 v[48:51], v255, s[84:87], s94 offen offset:0 sc1\n" \
  "v_mfma_f32_16x16x32_bf16 v[4:7], v[88:91], v[16:19], 0\n" \
  "ds_read_b128 v[88:91], v249 offset:19456\n" \
  "s_waitcnt vmcnt(7) lgkmcnt(6)\n" \
  "v_max_u32 v251, v20, v22\n" \
  "v_mfma_f32_16x16x32_bf16 v[8:11], v[92:95], v[16:19], 0\n" \
  "ds_read_b128 v[92:95], v249 offset:35840\n" \
  "v_cmp_eq_u32 vcc, -1, v251\n" \
  "v_mfma_f32_16x16x32_bf16 v[12:15], v[96:99], v[16:19], 0\n" \
  "ds_read_b128 v[96:99], v249 offset:52224\n" \
  "s_cbranch_vccnz .Lp_s48\n" \
  ".Lp_b48:\n" \
  "v_mfma_f32_16x16x32_bf16 v[0:3], v[100:103], v[20:23], v[0:3]\n" \
  "ds_read_b128 v[100:103], v249 offset:4096\n" \
  "buffer_load_dwordx4 v[52:55], v255, s[84:87], s94 offen offset:1024 sc1\n" \
  "v_mfma_f32_16x16x32_bf16 v[4:7], v[104:107], v[20:23], v[4:7]\n" \
  "ds_read_b128 v[104:107], v249 offset:20480\n" \
  "s_waitcnt vmcnt(7) lgkmcnt(6)\n" \
  "v_max_u32 v251, v24, v26\n" \
  "v_mfma_f32_16x16x32_bf16 v[8:11], v[108:111], v[20:23], v[8:11]\n" \
  "ds_read_b128 v[108:111], v249 offset:36864\n" \
  "v_cmp_eq_u32 vcc, -1, v251\n" \
  "v_mfma_f32_16x16x32_bf16 v[12:15], v[112:115], v[20:23], v[12:15]\n" \
  "ds_read_b128 v[112:115], v249 offset:53248\n" \
  "s_cbranch_vccnz .Lp_s49\n" \
  ".Lp_b49:\n" \
  "v_mfma_f32_16x16x32_bf16 v[0:3], v[116:119], v[24:27], v[0:3]\n" \
  "ds_read_b128 v[116:119], v249 offset:5120\n" \
  "s_add_u32 s94, s94, 2048\n" \
  "s_and_b32 s94, s94, 0x3fff\n" \
  "buffer_load_dwordx4 v[56:59], v255, s[84:87], s94 offen offset:0 sc1\n" \
  "v_mfma_f32_16x16x32_bf16 v[4:7], v[120:123], v[24:27], v[4:7]\n" \
  "ds_read_b128 v[120:123], v249 offset:21504\n" \
  "s_waitcnt vmcnt(7) lgkmcnt(6)\n" \
  "v_max_u32 v251, v28, v30\n" \
  "v_mfma_f32_16x16x32_bf16 v[8:11], v[124:127], v[24:27], v[8:11]\n" \
  "ds_read_b128 v[124:127], v249 offset:37888\n" \
  "v_cmp_eq_u32 vcc, -1, v251\n" \
  "v_mfma_f32_16x16x32_bf16 v[12:15], v[128:131], v[24:27], v[12:15]\n" \
  "ds_read_b128 v[128:131], v249 offset:54272\n" \
  "s_cbranch_vccnz .Lp_s50\n" \
  ".Lp_b50:\n" \
  "v_mfma_f32_16x16x32_bf16 v[0:3], v[84:87], v[28:31], v[0:3]\n" \
  "ds_read_b128 v[84:87], v249 offset:6144\n" \
  "buffer_load_dwordx4 v[60:63], v255, s[84:87], s94 offen offset:1024 sc1\n" \
  "v_mfma_f32_16x16x32_bf16 v[4:7], v[88:91], v[28:31], v[4:7]\n" \
  "ds_read_b128 v[88:91], v249 offset:22528\n" \
  "s_waitcnt vmcnt(7) lgkmcnt(6)\n" \
  "v_max_u32 v251, v32, v34\n" \
  "v_mfma_f32_16x16x32_bf16 v[8:11], v[92:95], v[28:31], v[8:11]\n" \
  "ds_read_b128 v[92:95], v249 offset:38912\n" \
  "v_cmp_eq_u32 vcc, -1, v251\n" \
  "v_mfma_f32_16x16x32_bf16 v[12:15], v[96:99], v[28:31], v[12:15]\n" \
  "ds_read_b128 v[96:99], v249 offset:55296\n" \
  "s_cbranch_vccnz .Lp_s51\n" \
  ".Lp_b51:\n" \
  "v_mfma_f32_16x16x32_bf16 v[0:3], v[100:103], v[32:35], v[0:3]\n" \
  "ds_read_b128 v[100:103], v249 offset:7168\n" \
  "s_add_u32 s94, s94, 2048\n" \
  "s_and_b32 s94, s94, 0x3fff\n" \
  "buffer_load_dwordx4 v[64:67], v255, s[84:87], s94 offen offset:0 sc1\n" \
  "v_mfma_f32_16x16x32_bf16 v[4:7], v[104:107], v[32:35], v[4:7]\n" \
  "ds_read_b128 v[104:107], v249 offset:23552\n" \
  "s_waitcnt vmcnt(7) lgkmcnt(6)\n" \
  "v_max_u32 v251, v36, v38\n" \
  "v_mfma_f32_16x16x32_bf16 v[8:11], v[108:111], v[32:35], v[8:11]\n" \
  "ds_read_b128 v[108:111], v249 offset:39936\n" \
  "v_cmp_eq_u32 vcc, -1, v251\n" \
  "v_mfma_f32_16x16x32_bf16 v[12:15], v[112:115], v[32:35], v[12:15]\n" \
  "ds_read_b128 v[112:115], v249 offset:56320\n" \
  "s_cbranch_vccnz .Lp_s52\n" \
  ".Lp_b52:\n" \
  "v_mfma_f32_16x16x32_bf16 v[0:3], v[116:119], v[36:39], v[0:3]\n" \
  "ds_read_b128 v[116:119], v249 offset:8192\n" \
  "buffer_load_dwordx4 v[68:71], v255, s[84:87], s94 offen offset:1024 sc1\n" \
  "v_mfma_f32_16x16x32_bf16 v[4:7], v[120:123], v[36:39], v[4:7]\n" \
  "ds_read_b128 v[120:123], v249 offset:24576\n" \
  "s_waitcnt vmcnt(7) lgkmcnt(6)\n" \
  "v_max_u32 v251, v40, v42\n" \
  "v_mfma_f32_16x16x32_bf16 v[8:11], v[124:127], v[36:39], v[8:11]\n" \
  "ds_read_b128 v[124:127], v249 offset:40960\n" \
  "v_cmp_eq_u32 vcc, -1, v251\n" \
  "v_mfma_f32_16x16x32_bf16 v[12:15], v[128:131], v[36:39], v[12:15]\n" \
  "ds_read_b128 v[128:131], v249 offset:57344\n" \
  "s_cbranch_vccnz .Lp_s53\n" \
  ".Lp_b53:\n" \
  "v_mfma_f32_16x16x32_bf16 v[0:3], v[84:87], v[40:43], v[0:3]\n" \
  "ds_read_b128 v[84:87], v249 offset:9216\n" \
  "s_add_u32 s94, s94, 2048\n" \
  "s_and_b32 s94, s94, 0x3fff\n" \
  "buffer_load_dwordx4 v[72:75], v255, s[84:87], s94 offen offset:0 sc1\n" \
  "v_mfma_f32_16x16x32_bf16 v[4:7], v[88:91], v[40:43], v[4:7]\n" \
  "ds_read_b128 v[88:91], v249 offset:25600\n" \
  "s_waitcnt vmcnt(7) lgkmcnt(6)\n" \
  "v_max_u32 v251, v44, v46\n" \
  "v_mfma_f32_16x16x32_bf16 v[8:11], v[92:95], v[40:43], v[8:11]\n" \
  "ds_read_b128 v[92:95], v249 offset:41984\n" \
  "v_cmp_eq_u32 vcc, -1, v251\n" \
  "v_mfma_f32_16x16x32_bf16 v[12:15], v[96:99], v[40:43], v[12:15]\n" \
  "ds_read_b128 v[96:99], v249 offset:58368\n" \
  "s_cbranch_vccnz .Lp_s54\n" \
  ".Lp_b54:\n" \
  "v_mfma_f32_16x16x32_bf16 v[0:3], v[100:103], v[44:47], v[0:3]\n" \
  "ds_read_b128 v[100:103], v249 offset:10240\n" \
  "buffer_load_dwordx4 v[76:79], v255, s[84:87], s94 offen offset:1024 sc1\n" \
  "buffer_load_dwordx4 v[80:83], v248, s[84:87], s89 offen offset:0 sc1\n" \
  "v_mfma_f32_16x16x32_bf16 v[4:7], v[104:107], v[44:47], v[4:7]\n" \
  "ds_read_b128 v[104:107], v249 offset:26624\n" \
  "s_waitcnt vmcnt(8) lgkmcnt(6)\n" \
  "v_max_u32 v251, v48, v50\n" \
  "v_mfma_f32_16x16x32_bf16 v[8:11], v[108:111], v[44:47], v[8:11]\n" \
  "ds_read_b128 v[108:111], v249 offset:43008\n" \
  "v_cmp_eq_u32 vcc, -1, v251\n" \
  "v_mfma_f32_16x16x32_bf16 v[12:15], v[112:115], v[44:47], v[12:15]\n" \
  "ds_read_b128 v[112:115], v249 offset:59392\n" \
  "s_cbranch_vccnz .Lp_s55\n" \
  ".Lp_b55:\n" \
  "v_mfma_f32_16x16x32_bf16 v[0:3], v[116:119], v[48:51], v[0:3]\n" \
  "ds_read_b128 v[116:119], v249 offset:11264\n" \
  "v_mfma_f32_16x16x32_bf16 v[4:7], v[120:123], v[48:51], v[4:7]\n" \
  "ds_read_b128 v[120:123], v249 offset:27648\n" \
  "s_waitcnt vmcnt(7) lgkmcnt(6)\n" \
  "v_max_u32 v251, v52, v54\n" \
  "v_mfma_f32_16x16x32_bf16 v[8:11], v[124:127], v[48:51], v[8:11]\n" \
  "ds_read_b128 v[124:127], v249 offset:44032\n" \
  "v_cmp_eq_u32 vcc, -1, v251\n" \
  "v_mfma_f32_16x16x32_bf16 v[12:15], v[128:131], v[48:51], v[12:15]\n" \
  "ds_read_b128 v[128:131], v249 offset:60416\n" \
  "s_cbranch_vccnz .Lp_s56\n" \
  ".Lp_b56:\n" \
  "v_mfma_f32_16x16x32_bf16 v[0:3], v[84:87], v[52:55], v[0:3]\n" \
  "ds_read_b128 v[84:87], v249 offset:12288\n" \
  "v_mfma_f32_16x16x32_bf16 v[4:7], v[88:91], v[52:55], v[4:7]\n" \
  "ds_read_b128 v[88:91], v249 offset:28672\n" \
  "s_waitcnt vmcnt(6) lgkmcnt(6)\n" \
  "v_max_u32 v251, v56, v58\n" \
  "v_mfma_f32_16x16x32_bf16 v[8:11], v[92:95], v[52:55], v[8:11]\n" \
  "ds_read_b128 v[92:95], v249 offset:45056\n" \
  "v_cmp_eq_u32 vcc, -1, v251\n" \
  "v_mfma_f32_16x16x32_bf16 v[12:15], v[96:99], v[52:55], v[12:15]\n" \
  "ds_read_b128 v[96:99], v249 offset:61440\n" \
  "s_cbranch_vccnz .Lp_s57\n" \
  ".Lp_b57:\n" \
  "v_mfma_f32_16x16x32_bf16 v[0:3], v[100:103], v[56:59], v[0:3]\n" \
  "ds_read_b128 v[100:103], v249 offset:13312\n" \
  "v_mfma_f32_16x16x32_bf16 v[4:7], v[104:107], v[56:59], v[4:7]\n" \
  "ds_read_b128 v[104:107], v249 offset:29696\n" \
  "s_waitcnt vmcnt(5) lgkmcnt(6)\n" \
  "v_max_u32 v251, v60, v62\n" \
  "v_mfma_f32_16x16x32_bf16 v[8:11], v[108:111], v[56:59], v[8:11]\n" \
  "ds_read_b128 v[108:111], v249 offset:46080\n" \
  "v_cmp_eq_u32 vcc, -1, v251\n" \
  "v_mfma_f32_16x16x32_bf16 v[12:15], v[112:115], v[56:59], v[12:15]\n" \
  "ds_read_b128 v[112:115], v249 offset:62464\n" \
  "s_cbranch_vccnz .Lp_s58\n" \
  ".Lp_b58:\n" \
  "v_mfma_f32_16x16x32_bf16 v[0:3], v[116:119], v[60:63], v[0:3]\n" \
  "ds_read_b128 v[116:119], v249 offset:14336\n" \
  "v_mfma_f32_16x16x32_bf16 v[4:7], v[120:123], v[60:63], v[4:7]\n" \
  "ds_read_b128 v[120:123], v249 offset:30720\n" \
  "s_waitcnt vmcnt(4) lgkmcnt(6)\n" \
  "v_max_u32 v251, v64, v66\n" \
  "v_mfma_f32_16x16x32_bf16 v[8:11], v[124:127], v[60:63], v[8:11]\n" \
  "ds_read_b128 v[124:127], v249 offset:47104\n" \
  "v_cmp_eq_u32 vcc, -1, v251\n" \
  "v_mfma_f32_16x16x32_bf16 v[12:15], v[128:131], v[60:63], v[12:15]\n" \
  "ds_read_b128 v[128:131], v249 offset:63488\n" \
  "s_cbranch_vccnz .Lp_s59\n" \
  ".Lp_b59:\n" \
  "v_mfma_f32_16x16x32_bf16 v[0:3], v[84:87], v[64:67], v[0:3]\n" \
  "ds_read_b128 v[84:87], v249 offset:15360\n" \
  "v_mfma_f32_16x16x32_bf16 v[4:7], v[88:91], v[64:67], v[4:7]\n" \
  "ds_read_b128 v[88:91], v249 offset:31744\n" \
  "s_waitcnt vmcnt(3) lgkmcnt(6)\n" \
  "v_max_u32 v251, v68, v70\n" \
  "v_mfma_f32_16x16x32_bf16 v[8:11], v[92:95], v[64:67], v[8:11]\n" \
  "ds_read_b128 v[92:95], v249 offset:48128\n" \
  "v_cmp_eq_u32 vcc, -1, v251\n" \
  "v_mfma_f32_16x16x32_bf16 v[12:15], v[96:99], v[64:67], v[12:15]\n" \
  "ds_read_b128 v[96:99], v249 offset:64512\n" \
  "s_cbranch_vccnz .Lp_s60\n" \
  ".Lp_b60:\n" \
  "v_mfma_f32_16x16x32_bf16 v[0:3], v[100:103], v[68:71], v[0:3]\n" \
  "ds_read_b128 v[100:103], v250 offset:0\n" \
  "v_mfma_f32_16x16x32_bf16 v[4:7], v[104:107], v[68:71], v[4:7]\n" \
  "ds_read_b128 v[104:107], v250 offset:16384\n" \
  "s_waitcnt vmcnt(2) lgkmcnt(6)\n" \
  "v_max_u32 v251, v72, v74\n" \
  "v_mfma_f32_16x16x32_bf16 v[8:11], v[108:111], v[68:71], v[8:11]\n" \
  "ds_read_b128 v[108:111], v250 offset:1024\n" \
  "v_cmp_eq_u32 vcc, -1, v251\n" \
  "v_mfma_f32_16x16x32_bf16 v[12:15], v[112:115], v[68:71], v[12:15]\n" \
  "ds_read_b128 v[112:115], v250 offset:17408\n" \
  "s_cbranch_vccnz .Lp_s61\n" \
  ".Lp_b61:\n" \
  "v_mfma_f32_16x16x32_bf16 v[0:3], v[116:119], v[72:75], v[0:3]\n" \
  "ds_read_b128 v[116:119], v250 offset:2048\n" \
  "v_mfma_f32_16x16x32_bf16 v[4:7], v[120:123], v[72:75], v[4:7]\n" \
  "ds_read_b128 v[120:123], v253 offset:0\n" \
  "s_waitcnt vmcnt(1) lgkmcnt(6)\n" \
  "v_max_u32 v251, v76, v78\n" \
  "v_mfma_f32_16x16x32_bf16 v[8:11], v[124:127], v[72:75], v[8:11]\n" \
  "ds_read_b128 v[124:127], v250 offset:18432\n" \
  "v_cmp_eq_u32 vcc, -1, v251\n" \
  "v_mfma_f32_16x16x32_bf16 v[12:15], v[128:131], v[72:75], v[12:15]\n" \
  "ds_read_b128 v[128:131], v250 offset:3072\n" \
  "s_cbranch_vccnz .Lp_s62\n" \
  ".Lp_b62:\n" \
  "v_mfma_f32_16x16x32_bf16 v[0:3], v[84:87], v[76:79], v[0:3]\n" \
  "ds_read_b128 v[84:87], v250 offset:19456\n" \
  "v_mfma_f32_16x16x32_bf16 v[4:7], v[88:91], v[76:79], v[4:7]\n" \
  "ds_read_b128 v[88:91], v250 offset:4096\n" \
  "s_waitcnt vmcnt(0) lgkmcnt(6)\n" \
  "v_max_u32 v251, v80, v82\n" \
  "v_mfma_f32_16x16x32_bf16 v[8:11], v[92:95], v[76:79], v[8:11]\n" \
  "ds_read_b128 v[92:95], v250 offset:20480\n" \
  "v_cmp_eq_u32 vcc, -1, v251\n" \
  "v_mfma_f32_16x16x32_bf16 v[12:15], v[96:99], v[76:79], v[12:15]\n" \
  "ds_read_b128 v[96:99], v250 offset:5120\n" \
  "s_cbranch_vccnz .Lp_s63\n" \
  ".Lp_b63:\n" \
  "v_mfma_f32_16x16x32_bf16 v[228:231], v[100:103], v[16:19], 0\n" \
  "ds_read_b128 v[100:103], v250 offset:21504\n" \
  "v_add_f32 v80, v80, v82\n" \
  "v_add_f32 v81, v81, v83\n" \
  "s_nop 1\n" \
  "v_permlane16_swap_b32 v80, v81\n" \
  "v_mfma_f32_16x16x32_bf16 v[232:235], v[104:107], v[16:19], 0\n" \
  "ds_read_b128 v[104:107], v253 offset:64\n" \
  "v_add_f32 v80, v80, v81\n" \
  "v_mov_b32 v81, v80\n" \
  "s_nop 1\n" \
  "v_permlane32_swap_b32 v80, v81\n" \
  "v_add_f32 v80, v80, v81\n" \
  "v_mov_b32 v81, v80\n" \
  "v_mfma_f32_16x16x32_bf16 v[228:231], v[108:111], v[20:23], v[228:231]\n" \
  "ds_read_b128 v[108:111], v250 offset:6144\n" \
  "v_permlane16_swap_b32 v80, v81\n" \
  "v_mul_f32 v247, 0x3b000000, v80\n" \
  "v_mul_f32 v81, 0x3b000000, v81\n" \
  "v_fma_f32 v81, -v247, v247, v81\n" \
  "v_max_f32 v81, 0, v81\n" \
  "v_mfma_f32_16x16x32_bf16 v[232:235], v[112:115], v[20:23], v[232:235]\n" \
  "ds_read_b128 v[112:115], v250 offset:22528\n" \
  "v_add_f32 v82, 0x3727c5ac, v81\n" \
  "v_rsq_f32 v83, v82\n" \
  "s_nop 0\n" \
  "v_mul_f32 v244, 0xc0135761, v83\n" \
  "v_mul_f32 v246, v82, v83\n" \
  "v_mul_f32 v81, v83, v83\n" \
  "s_waitcnt lgkmcnt(8)\n" \
  "v_mfma_f32_16x16x32_bf16 v[228:231], v[116:119], v[24:27], v[228:231]\n" \
  "ds_read_b128 v[116:119], v250 offset:7168\n" \
  "v_mul_f32 v81, 0x3d372713, v81\n" \
  "v_mul_f32 v245, v244, v81\n" \
  "v_fma_f32 v0, -v247, v120, v0\n" \
  "v_fma_f32 v1, -v247, v121, v1\n" \
  "v_fma_f32 v2, -v247, v122, v2\n" \
  "v_fma_f32 v3, -v247, v123, v3\n" \
  "ds_read_b128 v[120:123], v250 offset:23552\n" \
  "v_mul_f32 v80, v0, v0\n" \
  "v_mul_f32 v81, v1, v1\n" \
  "v_mfma_f32_16x16x32_bf16 v[232:235], v[124:127], v[24:27], v[232:235]\n" \
  "ds_read_b128 v[124:127], v250 offset:8192\n" \
  "v_mul_f32 v82, v2, v2\n" \
  "v_mul_f32 v83, v3, v3\n" \
  "v_fma_f32 v80, v80, v245, v244\n" \
  "v_fma_f32 v81, v81, v245, v244\n" \
  "v_fma_f32 v82, v82, v245, v244\n" \
  "v_mfma_f32_16x16x32_bf16 v[228:231], v[128:131], v[28:31], v[228:231]\n" \
  "ds_read_b128 v[128:131], v250 offset:24576\n" \
  "v_fma_f32 v83, v83, v245, v244\n" \
  "v_mul_f32 v80, v80, v0\n" \
  "v_mul_f32 v81, v81, v1\n" \
  "v_mul_f32 v82, v82, v2\n" \
  "v_mul_f32 v83, v83, v3\n" \
  "s_waitcnt lgkmcnt(8)\n" \
  "v_mfma_f32_16x16x32_bf16 v[232:235], v[84:87], v[28:31], v[232:235]\n" \
  "ds_read_b128 v[84:87], v250 offset:9216\n" \
  "v_exp_f32 v80, v80\n" \
  "v_exp_f32 v81, v81\n" \
  "v_exp_f32 v82, v82\n" \
  "v_exp_f32 v83, v83\n" \
  "v_fma_f32 v80, v80, v246, v246\n" \
  "v_mfma_f32_16x16x32_bf16 v[228:231], v[88:91], v[32:35], v[228:231]\n" \
  "ds_read_b128 v[88:91], v253 offset:128\n" \
  "v_fma_f32 v81, v81, v246, v246\n" \
  "v_fma_f32 v82, v82, v246, v246\n" \
  "v_fma_f32 v83, v83, v246, v246\n" \
  "v_rcp_f32 v80, v80\n" \
  "v_rcp_f32 v81, v81\n" \
  "v_mfma_f32_16x16x32_bf16 v[232:235], v[92:95], v[32:35], v[232:235]\n" \
  "ds_read_b128 v[92:95], v250 offset:25600\n" \
  "v_rcp_f32 v82, v82\n" \
  "v_rcp_f32 v83, v83\n" \
  "v_mul_f32 v80, v80, v0\n" \
  "v_mul_f32 v81, v81, v1\n" \
  "v_mul_f32 v82, v82, v2\n" \
  "v_mfma_f32_16x16x32_bf16 v[228:231], v[96:99], v[36:39], v[228:231]\n" \
  "ds_read_b128 v[96:99], v250 offset:10240\n" \
  "v_mul_f32 v83, v83, v3\n" \
  "v_cvt_pk_bf16_f32 v0, v80, v81\n" \
  "v_cvt_pk_bf16_f32 v1, v82, v83\n" \
  "v_min_u32 v0, -2, v0\n" \
  "v_min_u32 v1, -2, v1\n" \
  "s_waitcnt lgkmcnt(8)\n" \
  "v_mfma_f32_16x16x32_bf16 v[232:235], v[100:103], v[36:39], v[232:235]\n" \
  "ds_read_b128 v[100:103], v250 offset:26624\n" \
  "s_cmp_lg_u32 s40, 0\n" \
  "s_cbranch_scc1 .Lp_w3\n" \
  "s_add_u32 s94, s47, 98304\n" \
  "buffer_load_dwordx4 v[180:183], v248, s[84:87], s94 offen offset:0 sc1\n" \
  "buffer_load_dwordx4 v[184:187], v248, s[84:87], s94 offen offset:1024 sc1\n" \
  "buffer_load_dwordx4 v[188:191], v248, s[84:87], s94 offen offset:2048 sc1\n" \
  "buffer_load_dwordx4 v[192:195], v248, s[84:87], s94 offen offset:3072 sc1\n" \
  ".Lp_w3:\n" \
  "v_fma_f32 v4, -v247, v104, v4\n" \
  "v_fma_f32 v5, -v247, v105, v5\n" \
  "v_fma_f32 v6, -v247, v106, v6\n" \
  "v_fma_f32 v7, -v247, v107, v7\n" \
  "ds_read_b128 v[104:107], v250 offset:11264\n" \
  "v_mul_f32 v80, v4, v4\n" \
  "v_mul_f32 v81, v5, v5\n" \
  "v_mul_f32 v82, v6, v6\n" \
  "v_mfma_f32_16x16x32_bf16 v[228:231], v[108:111], v[40:43], v[228:231]\n" \
  "ds_read_b128 v[108:111], v250 offset:27648\n" \
  "v_mul_f32 v83, v7, v7\n" \
  "v_fma_f32 v80, v80, v245, v244\n" \
  "v_fma_f32 v81, v81, v245, v244\n" \
  "v_fma_f32 v82, v82, v245, v244\n" \
  "v_fma_f32 v83, v83, v245, v244\n" \
  "v_mfma_f32_16x16x32_bf16 v[232:235], v[112:115], v[40:43], v[232:235]\n" \
  "ds_read_b128 v[112:115], v250 offset:12288\n" \
  "v_mul_f32 v80, v80, v4\n" \
  "v_mul_f32 v81, v81, v5\n" \
  "v_mul_f32 v82, v82, v6\n" \
  "v_mul_f32 v83, v83, v7\n" \
  "v_exp_f32 v80, v80\n" \
  "s_waitcnt lgkmcnt(8)\n" \
  "v_mfma_f32_16x16x32_bf16 v[228:231], v[116:119], v[44:47], v[228:231]\n" \
  "ds_read_b128 v[116:119], v250 offset:28672\n" \
  "v_exp_f32 v81, v81\n" \
  "v_exp_f32 v82, v82\n" \
  "v_exp_f32 v83, v83\n" \
  "v_fma_f32 v80, v80, v246, v246\n" \
  "v_fma_f32 v81, v81, v246, v246\n" \
  "v_mfma_f32_16x16x32_bf16 v[232:235], v[120:123], v[44:47], v[232:235]\n" \
  "ds_read_b128 v[120:123], v253 offset:192\n" \
  "v_fma_f32 v82, v82, v246, v246\n" \
  "v_fma_f32 v83, v83, v246, v246\n" \
  "v_rcp_f32 v80, v80\n" \
  "v_rcp_f32 v81, v81\n" \
  "v_rcp_f32 v82, v82\n" \
  "v_mfma_f32_16x16x32_bf16 v[228:231], v[124:127], v[48:51], v[228:231]\n" \
  "ds_read_b128 v[124:127], v250 offset:13312\n" \
  "v_rcp_f32 v83, v83\n" \
  "v_mul_f32 v80, v80, v4\n" \
  "v_mul_f32 v81, v81, v5\n" \
  "v_mul_f32 v82, v82, v6\n" \
  "v_mfma_f32_16x16x32_bf16 v[232:235], v[128:131], v[48:51], v[232:235]\n" \
  "ds_read_b128 v[128:131], v250 offset:29696\n" \
  "v_mul_f32 v83, v83, v7\n" \
  "v_cvt_pk_bf16_f32 v2, v80, v81\n" \
  "v_cvt_pk_bf16_f32 v3, v82, v83\n" \
  "v_min_u32 v2, -2, v2\n" \
  "v_min_u32 v3, -2, v3\n" \
  "s_waitcnt lgkmcnt(8)\n" \
  "v_mfma_f32_16x16x32_bf16 v[228:231], v[84:87], v[52:55], v[228:231]\n" \
  "ds_read_b128 v[84:87], v250 offset:14336\n" \
  "s_add_u32 s94, s49, 0\n" \
  "buffer_store_dwordx4 v[0:3], v248, s[84:87], s94 offen sc1\n" \
  "s_cmp_lg_u32 s40, 0\n" \
  "s_cbranch_scc1 .Lp_w4\n" \
  "s_add_u32 s94, s47, 131072\n" \
  "buffer_load_dwordx4 v[196:199], v248, s[84:87], s94 offen offset:0 sc1\n" \
  "buffer_load_dwordx4 v[200:203], v248, s[84:87], s94 offen offset:1024 sc1\n" \
  "buffer_load_dwordx4 v[204:207], v248, s[84:87], s94 offen offset:2048 sc1\n" \
  "buffer_load_dwordx4 v[208:211], v248, s[84:87], s94 offen offset:3072 sc1\n" \
  ".Lp_w4:\n" \
  "v_fma_f32 v8, -v247, v88, v8\n" \
  "v_fma_f32 v9, -v247, v89, v9\n" \
  "v_fma_f32 v10, -v247, v90, v10\n" \
  "v_fma_f32 v11, -v247, v91, v11\n" \
  "ds_read_b128 v[88:91], v250 offset:30720\n" \
  "v_mul_f32 v80, v8, v8\n" \
  "v_mul_f32 v81, v9, v9\n" \
  "v_mfma_f32_16x16x32_bf16 v[232:235], v[92:95], v[52:55], v[232:235]\n" \
  "ds_read_b128 v[92:95], v250 offset:15360\n" \
  "v_mul_f32 v82, v10, v10\n" \
  "v_mul_f32 v83, v11, v11\n" \
  "v_fma_f32 v80, v80, v245, v244\n" \
  "v_fma_f32 v81, v81, v245, v244\n" \
  "v_fma_f32 v82, v82, v245, v244\n" \
  "v_mfma_f32_16x16x32_bf16 v[228:231], v[96:99], v[56:59], v[228:231]\n" \
  "ds_read_b128 v[96:99], v250 offset:31744\n" \
  "v_fma_f32 v83, v83, v245, v244\n" \
  "v_mul_f32 v80, v80, v8\n" \
  "v_mul_f32 v81, v81, v9\n" \
  "v_mul_f32 v82, v82, v10\n" \
  "v_mul_f32 v83, v83, v11\n" \
  "s_waitcnt lgkmcnt(8)\n" \
  "v_mfma_f32_16x16x32_bf16 v[232:235], v[100:103], v[56:59], v[232:235]\n" \
  "ds_read_b128 v[100:103], v248 offset:0\n" \
  "v_exp_f32 v80, v80\n" \
  "v_exp_f32 v81, v81\n" \
  "v_exp_f32 v82, v82\n" \
  "v_exp_f32 v83, v83\n" \
  "v_fma_f32 v80, v80, v246, v246\n" \
  "v_mfma_f32_16x16x32_bf16 v[228:231], v[104:107], v[60:63], v[228:231]\n" \
  "ds_read_b128 v[104:107], v253 offset:256\n" \
  "v_fma_f32 v81, v81, v246, v246\n" \
  "v_fma_f32 v82, v82, v246, v246\n" \
  "v_fma_f32 v83, v83, v246, v246\n" \
  "v_rcp_f32 v80, v80\n" \
  "v_rcp_f32 v81, v81\n" \
  "v_mfma_f32_16x16x32_bf16 v[232:235], v[108:111], v[60:63], v[232:235]\n" \
  "ds_read_b128 v[108:111], v248 offset:1024\n" \
  "v_rcp_f32 v82, v82\n" \
  "v_rcp_f32 v83, v83\n" \
  "v_mul_f32 v80, v80, v8\n" \
  "v_mul_f32 v81, v81, v9\n" \
  "v_mul_f32 v82, v82, v10\n" \
  "v_mfma_f32_16x16x32_bf16 v[228:231], v[112:115], v[64:67], v[228:231]\n" \
  "ds_read_b128 v[112:115], v248 offset:2048\n" \
  "v_mul_f32 v83, v83, v11\n" \
  "v_cvt_pk_bf16_f32 v8, v80, v81\n" \
  "v_cvt_pk_bf16_f32 v9, v82, v83\n" \
  "v_min_u32 v8, -2, v8\n" \
  "v_min_u32 v9, -2, v9\n" \
  "s_waitcnt lgkmcnt(8)\n" \
  "v_mfma_f32_16x16x32_bf16 v[232:235], v[116:119], v[64:67], v[232:235]\n" \
  "ds_read_b128 v[116:119], v248 offset:3072\n" \
  "s_cmp_lg_u32 s40, 0\n" \
  "s_cbranch_scc1 .Lp_w5\n" \
  "s_add_u32 s94, s47, 163840\n" \
  "buffer_load_dwordx4 v[212:215], v248, s[84:87], s94 offen offset:0 sc1\n" \
  "buffer_load_dwordx4 v[216:219], v248, s[84:87], s94 offen offset:1024 sc1\n" \
  "buffer_load_dwordx4 v[220:223], v248, s[84:87], s94 offen offset:2048 sc1\n" \
  "buffer_load_dwordx4 v[224:227], v248, s[84:87], s94 offen offset:3072 sc1\n" \
  ".Lp_w5:\n" \
  "v_fma_f32 v12, -v247, v120, v12\n" \
  "v_fma_f32 v13, -v247, v121, v13\n" \
  "v_fma_f32 v14, -v247, v122, v14\n" \
  "v_fma_f32 v15, -v247, v123, v15\n" \
  "ds_read_b128 v[120:123], v248 offset:4096\n" \
  "v_mul_f32 v80, v12, v12\n" \
  "v_mul_f32 v81, v13, v13\n" \
  "v_mul_f32 v82, v14, v14\n" \
  "v_mfma_f32_16x16x32_bf16 v[228:231], v[124:127], v[68:71], v[228:231]\n" \
  "ds_read_b128 v[124:127], v248 offset:5120\n" \
  "v_mul_f32 v83, v15, v15\n" \
  "v_fma_f32 v80, v80, v245, v244\n" \
  "v_fma_f32 v81, v81, v245, v244\n" \
  "v_fma_f32 v82, v82, v245, v244\n" \
  "v_fma_f32 v83, v83, v245, v244\n" \
  "v_mfma_f32_16x16x32_bf16 v[232:235], v[128:131], v[68:71], v[232:235]\n" \
  "ds_read_b128 v[128:131], v248 offset:6144\n" \
  "v_mul_f32 v80, v80, v12\n" \
  "v_mul_f32 v81, v81, v13\n" \
  "v_mul_f32 v82, v82, v14\n" \
  "v_mul_f32 v83, v83, v15\n" \
  "v_exp_f32 v80, v80\n" \
  "s_waitcnt lgkmcnt(8)\n" \
  "v_mfma_f32_16x16x32_bf16 v[228:231], v[84:87], v[72:75], v[228:231]\n" \
  "ds_read_b128 v[84:87], v248 offset:7168\n" \
  "v_exp_f32 v81, v81\n" \
  "v_exp_f32 v82, v82\n" \
  "v_exp_f32 v83, v83\n" \
  "v_fma_f32 v80, v80, v246, v246\n" \
  "v_fma_f32 v81, v81, v246, v246\n" \
  "v_mfma_f32_16x16x32_bf16 v[232:235], v[88:91], v[72:75], v[232:235]\n" \
  "ds_read_b128 v[88:91], v248 offset:8192\n" \
  "v_fma_f32 v82, v82, v246, v246\n" \
  "v_fma_f32 v83, v83, v246, v246\n" \
  "v_rcp_f32 v80, v80\n" \
  "v_rcp_f32 v81, v81\n" \
  "v_rcp_f32 v82, v82\n" \
  "v_mfma_f32_16x16x32_bf16 v[228:231], v[92:95], v[76:79], v[228:231]\n" \
  "ds_read_b128 v[92:95], v253 offset:320\n" \
  "v_rcp_f32 v83, v83\n" \
  "v_mul_f32 v80, v80, v12\n" \
  "v_mul_f32 v81, v81, v13\n" \
  "v_mul_f32 v82, v82, v14\n" \
  "v_mul_f32 v83, v83, v15\n" \
  "v_mfma_f32_16x16x32_bf16 v[232:235], v[96:99], v[76:79], v[232:235]\n" \
  "ds_read_b128 v[96:99], v248 offset:9216\n" \
  "v_cvt_pk_bf16_f32 v10, v80, v81\n" \
  "v_cvt_pk_bf16_f32 v11, v82, v83\n" \
  "v_min_u32 v10, -2, v10\n" \
  "v_min_u32 v11, -2, v11\n" \
  "s_add_u32 s94, s49, 16384\n" \
  "buffer_store_dwordx4 v[8:11], v248, s[84:87], s94 offen sc1\n" \
  "s_waitcnt lgkmcnt(8)\n" \
  "v_mfma_f32_16x16x32_bf16 v[236:239], v[100:103], v[16:19], 0\n" \
  "ds_read_b128 v[100:103], v248 offset:10240\n" \
  "v_fma_f32 v228, -v247, v104, v228\n" \
  "v_fma_f32 v229, -v247, v105, v229\n" \
  "v_fma_f32 v230, -v247, v106, v230\n" \
  "v_fma_f32 v231, -v247, v107, v231\n" \
  "ds_read_b128 v[104:107], v248 offset:11264\n" \
  "v_mul_f32 v80, v228, v228\n" \
  "v_mul_f32 v81, v229, v229\n" \
  "v_mul_f32 v82, v230, v230\n" \
  "v_mfma_f32_16x16x32_bf16 v[236:239], v[108:111], v[20:23], v[236:239]\n" \
  "ds_read_b128 v[108:111], v248 offset:12288\n" \
  "v_mul_f32 v83, v231, v231\n" \
  "v_fma_f32 v80, v80, v245, v244\n" \
  "v_fma_f32 v81, v81, v245, v244\n" \
  "v_fma_f32 v82, v82, v245, v244\n" \
  "v_mfma_f32_16x16x32_bf16 v[236:239], v[112:115], v[24:27], v[236:239]\n" \
  "ds_read_b128 v[112:115], v248 offset:13312\n" \
  "v_fma_f32 v83, v83, v245, v244\n" \
  "v_mul_f32 v80, v80, v228\n" \
  "v_mul_f32 v81, v81, v229\n" \
  "v_mul_f32 v82, v82, v230\n" \
  "s_waitcnt lgkmcnt(8)\n" \
  "v_mfma_f32_16x16x32_bf16 v[236:239], v[116:119], v[28:31], v[236:239]\n" \
  "ds_read_b128 v[116:119], v248 offset:14336\n" \
  "v_mul_f32 v83, v83, v231\n" \
  "v_exp_f32 v80, v80\n" \
  "v_exp_f32 v81, v81\n" \
  "v_exp_f32 v82, v82\n" \
  "v_mfma_f32_16x16x32_bf16 v[236:239], v[120:123], v[32:35], v[236:239]\n" \
  "ds_read_b128 v[120:123], v248 offset:15360\n" \
  "v_exp_f32 v83, v83\n" \
  "v_fma_f32 v80, v80, v246, v246\n" \
  "v_fma_f32 v81, v81, v246, v246\n" \
  "v_fma_f32 v82, v82, v246, v246\n" \
  "v_mfma_f32_16x16x32_bf16 v[236:239], v[124:127], v[36:39], v[236:239]\n" \
  "ds_read_b128 v[124:127], v253 offset:384\n" \
  "v_fma_f32 v83, v83, v246, v246\n" \
  "v_rcp_f32 v80, v80\n" \
  "v_rcp_f32 v81, v81\n" \
  "v_rcp_f32 v82, v82\n" \
  "v_rcp_f32 v83, v83\n" \
  "v_mfma_f32_16x16x32_bf16 v[236:239], v[128:131], v[40:43], v[236:239]\n" \
  "v_mul_f32 v80, v80, v228\n" \
  "v_mul_f32 v81, v81, v229\n" \
  "v_mul_f32 v82, v82, v230\n" \
  "v_mul_f32 v83, v83, v231\n" \
  "s_waitcnt lgkmcnt(7)\n" \
  "v_mfma_f32_16x16x32_bf16 v[236:239], v[84:87], v[44:47], v[236:239]\n" \
  "v_cvt_pk_bf16_f32 v228, v80, v81\n" \
  "v_cvt_pk_bf16_f32 v229, v82, v83\n" \
  "v_min_u32 v228, -2, v228\n" \
  "v_min_u32 v229, -2, v229\n" \
  "v_mfma_f32_16x16x32_bf16 v[236:239], v[88:91], v[48:51], v[236:239]\n" \
  "v_fma_f32 v232, -v247, v92, v232\n" \
  "v_fma_f32 v233, -v247, v93, v233\n" \
  "v_fma_f32 v234, -v247, v94, v234\n" \
  "v_fma_f32 v235, -v247, v95, v235\n" \
  "v_mul_f32 v80, v232, v232\n" \
  "v_mul_f32 v81, v233, v233\n" \
  "v_mul_f32 v82, v234, v234\n" \
  "v_mfma_f32_16x16x32_bf16 v[236:239], v[96:99], v[52:55], v[236:239]\n" \
  "v_mul_f32 v83, v235, v235\n" \
  "v_fma_f32 v80, v80, v245, v244\n" \
  "v_fma_f32 v81, v81, v245, v244\n" \
  "v_fma_f32 v82, v82, v245, v244\n" \
  "s_waitcnt lgkmcnt(3)\n" \
  "v_mfma_f32_16x16x32_bf16 v[236:239], v[100:103], v[56:59], v[236:239]\n" \
  "v_fma_f32 v83, v83, v245, v244\n" \
  "v_mul_f32 v80, v80, v232\n" \
  "v_mul_f32 v81, v81, v233\n" \
  "v_mul_f32 v82, v82, v234\n" \
  "v_mul_f32 v83, v83, v235\n" \
  "v_mfma_f32_16x16x32_bf16 v[236:239], v[104:107], v[60:63], v[236:239]\n" \
  "v_exp_f32 v80, v80\n" \
  "v_exp_f32 v81, v81\n" \
  "v_exp_f32 v82, v82\n" \
  "v_exp_f32 v83, v83\n" \
  "v_mfma_f32_16x16x32_bf16 v[236:239], v[108:111], v[64:67], v[236:239]\n" \
  "s_cmp_eq_u32 s40, 0\n" \
  "s_cbranch_scc1 .Lp_ne\n" \
  "s_add_u32 s94, s51, 0\n" \
  "buffer_load_dwordx4 v[16:19], v248, s[84:87], s94 offen offset:0 sc1\n" \
  "s_add_u32 s94, s51, 57344\n" \
  "buffer_load_dwordx4 v[20:23], v248, s[84:87], s94 offen offset:0 sc1\n" \
  "s_add_u32 s94, s51, 8192\n" \
  "buffer_load_dwordx4 v[24:27], v248, s[84:87], s94 offen offset:0 sc1\n" \
  "s_add_u32 s94, s51, 65536\n" \
  "buffer_load_dwordx4 v[28:31], v248, s[84:87], s94 offen offset:0 sc1\n" \
  ".Lp_ne:\n" \
  "v_fma_f32 v80, v80, v246, v246\n" \
  "v_fma_f32 v81, v81, v246, v246\n" \
  "v_fma_f32 v82, v82, v246, v246\n" \
  "v_fma_f32 v83, v83, v246, v246\n" \
  "v_mfma_f32_16x16x32_bf16 v[236:239], v[112:115], v[68:71], v[236:239]\n" \
  "v_rcp_f32 v80, v80\n" \
  "v_rcp_f32 v81, v81\n" \
  "v_rcp_f32 v82, v82\n" \
  "v_rcp_f32 v83, v83\n" \
  "s_waitcnt lgkmcnt(0)\n" \
  "v_mfma_f32_16x16x32_bf16 v[236:239], v[116:119], v[72:75], v[236:239]\n" \
  "v_mul_f32 v80, v80, v232\n" \
  "v_mul_f32 v81, v81, v233\n" \
  "v_mul_f32 v82, v82, v234\n" \
  "v_mul_f32 v83, v83, v235\n" \
  "v_mfma_f32_16x16x32_bf16 v[236:239], v[120:123], v[76:79], v[236:239]\n" \
  "v_cvt_pk_bf16_f32 v230, v80, v81\n" \
  "v_cvt_pk_bf16_f32 v231, v82, v83\n" \
  "v_min_u32 v230, -2, v230\n" \
  "v_min_u32 v231, -2, v231\n" \
  "s_add_u32 s94, s49, 32768\n" \
  "buffer_store_dwordx4 v[228:231], v248, s[84:87], s94 offen sc1\n" \
  "v_mov_b32 v232, 65000\n" \
  "v_mov_b32 v233, 1\n" \
  "s_mov_b64 exec, 1\n" \
  "ds_add_u32 v232, v233 offset:24\n" \
  "s_mov_b64 exec, -1\n" \
  "v_fma_f32 v236, -v247, v124, v236\n" \
  "v_fma_f32 v237, -v247, v125, v237\n" \
  "v_fma_f32 v238, -v247, v126, v238\n" \
  "v_fma_f32 v239, -v247, v127, v239\n" \
  "v_mul_f32 v80, v236, v236\n" \
  "v_mul_f32 v81, v237, v237\n" \
  "v_mul_f32 v82, v238, v238\n" \
  "v_mul_f32 v83, v239, v239\n" \
  "v_fma_f32 v80, v80, v245, v244\n" \
  "v_fma_f32 v81, v81, v245, v244\n" \
  "v_fma_f32 v82, v82, v245, v244\n" \
  "v_fma_f32 v83, v83, v245, v244\n" \
  "v_mul_f32 v80, v80, v236\n" \
  "v_mul_f32 v81, v81, v237\n" \
  "v_mul_f32 v82, v82, v238\n" \
  "v_mul_f32 v83, v83, v239\n" \
  "v_exp_f32 v80, v80\n" \
  "v_exp_f32 v81, v81\n" \
  "v_exp_f32 v82, v82\n" \
  "v_exp_f32 v83, v83\n" \
  "v_fma_f32 v80, v80, v246, v246\n" \
  "v_fma_f32 v81, v81, v246, v246\n" \
  "v_fma_f32 v82, v82, v246, v246\n" \
  "v_fma_f32 v83, v83, v246, v246\n" \
  "v_rcp_f32 v80, v80\n" \
  "v_rcp_f32 v81, v81\n" \
  "v_rcp_f32 v82, v82\n" \
  "v_rcp_f32 v83, v83\n" \
  "v_mul_f32 v80, v80, v236\n" \
  "v_mul_f32 v81, v81, v237\n" \
  "v_mul_f32 v82, v82, v238\n" \
  "v_mul_f32 v83, v83, v239\n" \
  "v_cvt_pk_bf16_f32 v236, v80, v81\n" \
  "v_cvt_pk_bf16_f32 v237, v82, v83\n" \
  "v_min_u32 v236, -2, v236\n" \
  "v_min_u32 v237, -2, v237\n" \
  "buffer_store_dwordx2 v[236:237], v252, s[84:87], s50 offen sc1\n" \
  "v_add_u32 v251, s60, v248\n" \
  "s_lshl_b32 s61, s56, 10\n" \
  "ds_read_b128 v[84:87], v251 offset:16384\n" \
  "ds_read_b128 v[88:91], v251 offset:17408\n" \
  "ds_read_b128 v[92:95], v251 offset:18432\n" \
  "ds_read_b128 v[96:99], v251 offset:19456\n" \
  "v_add_u32 v253, s61, v248\n" \
  "v_add_u32 v249, s61, v249\n" \
  "v_add_u32 v250, s61, v250\n" \
  "s_cmp_lg_u32 s40, 0\n" \
  "s_cbranch_scc1 .Lp_e5s\n" \
  "s_sleep 6\n" \
  "s_add_u32 s94, s51, 0\n" \
  "buffer_load_dwordx4 v[16:19], v248, s[84:87], s94 offen offset:0 sc1\n" \
  "s_add_u32 s94, s51, 57344\n" \
  "buffer_load_dwordx4 v[20:23], v248, s[84:87], s94 offen offset:0 sc1\n" \
  "s_add_u32 s94, s51, 8192\n" \
  "buffer_load_dwordx4 v[24:27], v248, s[84:87], s94 offen offset:0 sc1\n" \
  "s_add_u32 s94, s51, 65536\n" \
  "buffer_load_dwordx4 v[28:31], v248, s[84:87], s94 offen offset:0 sc1\n" \
  "s_add_u32 s94, s51, 16384\n" \
  "buffer_load_dwordx4 v[32:35], v248, s[84:87], s94 offen offset:0 sc1\n" \
  "s_add_u32 s94, s51, 73728\n" \
  "buffer_load_dwordx4 v[52:55], v248, s[84:87], s94 offen offset:0 sc1\n" \
  "s_add_u32 s94, s51, 24576\n" \
  "buffer_load_dwordx4 v[36:39], v248, s[84:87], s94 offen offset:0 sc1\n" \
  "s_add_u32 s94, s51, 81920\n" \
  "buffer_load_dwordx4 v[56:59], v248, s[84:87], s94 offen offset:0 sc1\n" \
  "s_sleep 2\n" \
  "s_add_u32 s94, s51, 32768\n" \
  "buffer_load_dwordx4 v[40:43], v248, s[84:87], s94 offen offset:0 sc1\n" \
  "s_add_u32 s94, s51, 90112\n" \
  "buffer_load_dwordx4 v[60:63], v248, s[84:87], s94 offen offset:0 sc1\n" \
  "s_add_u32 s94, s51, 40960\n" \
  "buffer_load_dwordx4 v[44:47], v248, s[84:87], s94 offen offset:0 sc1\n" \
  "s_add_u32 s94, s51, 98304\n" \
  "buffer_load_dwordx4 v[64:67], v248, s[84:87], s94 offen offset:0 sc1\n" \
  "s_add_u32 s94, s51, 49152\n" \
  "buffer_load_dwordx4 v[48:51], v248, s[84:87], s94 offen offset:0 sc1\n" \
  "s_add_u32 s94, s51, 106496\n" \
  "buffer_load_dwordx4 v[68:71], v248, s[84:87], s94 offen offset:0 sc1\n" \
  "s_waitcnt lgkmcnt(0)\n" \
  "s_waitcnt vmcnt(13)\n" \
  "v_max_u32 v251, v16, v18\n" \
  "v_cmp_eq_u32 vcc, -1, v251\n" \
  "s_cbranch_vccnz .Lp_s64\n" \
  ".Lp_b64:\n" \
  "v_mfma_f32_16x16x32_bf16 v[100:103], v[132:135], v[16:19], 0\n" \
  "v_mfma_f32_16x16x32_bf16 v[104:107], v[136:139], v[16:19], 0\n" \
  "s_waitcnt vmcnt(12)\n" \
  "v_max_u32 v251, v20, v22\n" \
  "v_mfma_f32_16x16x32_bf16 v[108:111], v[140:143], v[16:19], 0\n" \
  "v_cmp_eq_u32 vcc, -1, v251\n" \
  "v_mfma_f32_16x16x32_bf16 v[112:115], v[144:147], v[16:19], 0\n" \
  "s_cbranch_vccnz .Lp_s65\n" \
  ".Lp_b65:\n" \
  "v_mfma_f32_16x16x32_bf16 v[116:119], v[132:135], v[20:23], 0\n" \
  "v_mfma_f32_16x16x32_bf16 v[120:123], v[136:139], v[20:23], 0\n" \
  "s_waitcnt vmcnt(11)\n" \
  "v_max_u32 v251, v24, v26\n" \
  "v_mfma_f32_16x16x32_bf16 v[124:127], v[140:143], v[20:23], 0\n" \
  "v_cmp_eq_u32 vcc, -1, v251\n" \
  "v_mfma_f32_16x16x32_bf16 v[128:131], v[144:147], v[20:23], 0\n" \
  "s_cbranch_vccnz .Lp_s66\n" \
  ".Lp_b66:\n" \
  "v_mfma_f32_16x16x32_bf16 v[100:103], v[148:151], v[24:27], v[100:103]\n" \
  "v_mfma_f32_16x16x32_bf16 v[104:107], v[152:155], v[24:27], v[104:107]\n" \
  "s_waitcnt vmcnt(10)\n" \
  "v_max_u32 v251, v28, v30\n" \
  "v_mfma_f32_16x16x32_bf16 v[108:111], v[156:159], v[24:27], v[108:111]\n" \
  "v_cmp_eq_u32 vcc, -1, v251\n" \
  "v_mfma_f32_16x16x32_bf16 v[112:115], v[160:163], v[24:27], v[112:115]\n" \
  "s_cbranch_vccnz .Lp_s67\n" \
  ".Lp_b67:\n" \
  "v_mfma_f32_16x16x32_bf16 v[116:119], v[148:151], v[28:31], v[116:119]\n" \
  "v_mfma_f32_16x16x32_bf16 v[120:123], v[152:155], v[28:31], v[120:123]\n" \
  "s_waitcnt vmcnt(9)\n" \
  "v_max_u32 v251, v32, v34\n" \
  "v_mfma_f32_16x16x32_bf16 v[124:127], v[156:159], v[28:31], v[124:127]\n" \
  "v_cmp_eq_u32 vcc, -1, v251\n" \
  "v_mfma_f32_16x16x32_bf16 v[128:131], v[160:163], v[28:31], v[128:131]\n" \
  "s_cbranch_vccnz .Lp_s68\n" \
  ".Lp_b68:\n" \
  "v_mfma_f32_16x16x32_bf16 v[100:103], v[164:167], v[32:35], v[100:103]\n" \
  "v_mfma_f32_16x16x32_bf16 v[104:107], v[168:171], v[32:35], v[104:107]\n" \
  "s_waitcnt vmcnt(8)\n" \
  "v_max_u32 v251, v52, v54\n" \
  "v_mfma_f32_16x16x32_bf16 v[108:111], v[172:175], v[32:35], v[108:111]\n" \
  "v_cmp_eq_u32 vcc, -1, v251\n" \
  "v_mfma_f32_16x16x32_bf16 v[112:115], v[176:179], v[32:35], v[112:115]\n" \
  "s_cbranch_vccnz .Lp_s69\n" \
  ".Lp_b69:\n" \
  "v_mfma_f32_16x16x32_bf16 v[116:119], v[164:167], v[52:55], v[116:119]\n" \
  "v_mfma_f32_16x16x32_bf16 v[120:123], v[168:171], v[52:55], v[120:123]\n" \
  "s_waitcnt vmcnt(7)\n" \
  "v_max_u32 v251, v36, v38\n" \
  "v_mfma_f32_16x16x32_bf16 v[124:127], v[172:175], v[52:55], v[124:127]\n" \
  "v_cmp_eq_u32 vcc, -1, v251\n" \
  "v_mfma_f32_16x16x32_bf16 v[128:131], v[176:179], v[52:55], v[128:131]\n" \
  "s_cbranch_vccnz .Lp_s70\n" \
  ".Lp_b70:\n" \
  "v_mfma_f32_16x16x32_bf16 v[100:103], v[180:183], v[36:39], v[100:103]\n" \
  "v_mfma_f32_16x16x32_bf16 v[104:107], v[184:187], v[36:39], v[104:107]\n" \
  "s_waitcnt vmcnt(6)\n" \
  "v_max_u32 v251, v56, v58\n" \
  "v_mfma_f32_16x16x32_bf16 v[108:111], v[188:191], v[36:39], v[108:111]\n" \
  "v_cmp_eq_u32 vcc, -1, v251\n" \
  "v_mfma_f32_16x16x32_bf16 v[112:115], v[192:195], v[36:39], v[112:115]\n" \
  "s_cbranch_vccnz .Lp_s71\n" \
  ".Lp_b71:\n" \
  "v_mfma_f32_16x16x32_bf16 v[116:119], v[180:183], v[56:59], v[116:119]\n" \
  "v_mfma_f32_16x16x32_bf16 v[120:123], v[184:187], v[56:59], v[120:123]\n" \
  "s_waitcnt vmcnt(5)\n" \
  "v_max_u32 v251, v40, v42\n" \
  "v_mfma_f32_16x16x32_bf16 v[124:127], v[188:191], v[56:59], v[124:127]\n" \
  "v_cmp_eq_u32 vcc, -1, v251\n" \
  "v_mfma_f32_16x16x32_bf16 v[128:131], v[192:195], v[56:59], v[128:131]\n" \
  "s_cbranch_vccnz .Lp_s72\n" \
  ".Lp_b72:\n" \
  "v_mfma_f32_16x16x32_bf16 v[100:103], v[196:199], v[40:43], v[100:103]\n" \
  "v_mfma_f32_16x16x32_bf16 v[104:107], v[200:203], v[40:43], v[104:107]\n" \
  "s_waitcnt vmcnt(4)\n" \
  "v_max_u32 v251, v60, v62\n" \
  "v_mfma_f32_16x16x32_bf16 v[108:111], v[204:207], v[40:43], v[108:111]\n" \
  "v_cmp_eq_u32 vcc, -1, v251\n" \
  "v_mfma_f32_16x16x32_bf16 v[112:115], v[208:211], v[40:43], v[112:115]\n" \
  "s_cbranch_vccnz .Lp_s73\n" \
  ".Lp_b73:\n" \
  "v_mfma_f32_16x16x32_bf16 v[116:119], v[196:199], v[60:63], v[116:119]\n" \
  "v_mfma_f32_16x16x32_bf16 v[120:123], v[200:203], v[60:63], v[120:123]\n" \
  "s_waitcnt vmcnt(3)\n" \
  "v_max_u32 v251, v44, v46\n" \
  "v_mfma_f32_16x16x32_bf16 v[124:127], v[204:207], v[60:63], v[124:127]\n" \
  "v_cmp_eq_u32 vcc, -1, v251\n" \
  "v_mfma_f32_16x16x32_bf16 v[128:131], v[208:211], v[60:63], v[128:131]\n" \
  "s_cbranch_vccnz .Lp_s74\n" \
  ".Lp_b74:\n" \
  "v_mfma_f32_16x16x32_bf16 v[100:103], v[212:215], v[44:47], v[100:103]\n" \
  "v_mfma_f32_16x16x32_bf16 v[104:107], v[216:219], v[44:47], v[104:107]\n" \
  "s_waitcnt vmcnt(2)\n" \
  "v_max_u32 v251, v64, v66\n" \
  "v_mfma_f32_16x16x32_bf16 v[108:111], v[220:223], v[44:47], v[108:111]\n" \
  "v_cmp_eq_u32 vcc, -1, v251\n" \
  "v_mfma_f32_16x16x32_bf16 v[112:115], v[224:227], v[44:47], v[112:115]\n" \
  "s_cbranch_vccnz .Lp_s75\n" \
  ".Lp_b75:\n" \
  "v_mfma_f32_16x16x32_bf16 v[116:119], v[212:215], v[64:67], v[116:119]\n" \
  "v_mfma_f32_16x16x32_bf16 v[120:123], v[216:219], v[64:67], v[120:123]\n" \
  "s_waitcnt vmcnt(1)\n" \
  "v_max_u32 v251, v48, v50\n" \
  "v_mfma_f32_16x16x32_bf16 v[124:127], v[220:223], v[64:67], v[124:127]\n" \
  "v_cmp_eq_u32 vcc, -1, v251\n" \
  "v_mfma_f32_16x16x32_bf16 v[128:131], v[224:227], v[64:67], v[128:131]\n" \
  "s_cbranch_vccnz .Lp_s76\n" \
  ".Lp_b76:\n" \
  "v_mfma_f32_16x16x32_bf16 v[100:103], v[84:87], v[48:51], v[100:103]\n" \
  "v_mov_b32 v244, 65000\n" \
  "ds_read_b32 v245, v244 offset:24\n" \
  "v_mfma_f32_16x16x32_bf16 v[104:107], v[88:91], v[48:51], v[104:107]\n" \
  "s_waitcnt vmcnt(0)\n" \
  "v_max_u32 v251, v68, v70\n" \
  "v_mfma_f32_16x16x32_bf16 v[108:111], v[92:95], v[48:51], v[108:111]\n" \
  "v_cmp_eq_u32 vcc, -1, v251\n" \
  "v_mfma_f32_16x16x32_bf16 v[112:115], v[96:99], v[48:51], v[112:115]\n" \
  "s_cbranch_vccnz .Lp_s77\n" \
  ".Lp_b77:\n" \
  "v_mfma_f32_16x16x32_bf16 v[116:119], v[84:87], v[68:71], v[116:119]\n" \
  "v_mfma_f32_16x16x32_bf16 v[120:123], v[88:91], v[68:71], v[120:123]\n" \
  "v_mfma_f32_16x16x32_bf16 v[124:127], v[92:95], v[68:71], v[124:127]\n" \
  "v_mfma_f32_16x16x32_bf16 v[128:131], v[96:99], v[68:71], v[128:131]\n" \
  "s_waitcnt lgkmcnt(0)\n" \
  "v_cmp_gt_u32 vcc, 8, v245\n" \
  "s_cbranch_vccz .Lp_c6_x\n" \
  "s_mov_b32 s92, 2000000\n" \
  ".Lp_c6_l:\n" \
  "s_sleep 1\n" \
  "ds_read_b32 v245, v244 offset:24\n" \
  "s_waitcnt lgkmcnt(0)\n" \
  "v_cmp_gt_u32 vcc, 8, v245\n" \
  "s_cbranch_vccz .Lp_c6_x\n" \
  "s_sub_u32 s92, s92, 1\n" \
  "s_cbranch_scc0 .Lp_c6_l\n" \
  ".Lp_c6_x:\n" \
  "s_add_u32 s94, s51, 114688\n" \
  "buffer_load_dwordx4 v[72:75], v248, s[84:87], s94 offen offset:0 sc1\n" \
  "s_add_u32 s94, s51, 172032\n" \
  "buffer_load_dwordx4 v[232:235], v248, s[84:87], s94 offen offset:0 sc1\n" \
  "s_add_u32 s94, s51, 122880\n" \
  "buffer_load_dwordx4 v[76:79], v248, s[84:87], s94 offen offset:0 sc1\n" \
  "s_add_u32 s94, s51, 180224\n" \
  "buffer_load_dwordx4 v[236:239], v248, s[84:87], s94 offen offset:0 sc1\n" \
  "s_add_u32 s94, s51, 131072\n" \
  "buffer_load_dwordx4 v[0:3], v248, s[84:87], s94 offen offset:0 sc1\n" \
  "s_add_u32 s94, s51, 188416\n" \
  "buffer_load_dwordx4 v[80:83], v248, s[84:87], s94 offen offset:0 sc1\n" \
  "s_add_u32 s94, s51, 139264\n" \
  "buffer_load_dwordx4 v[4:7], v248, s[84:87], s94 offen offset:0 sc1\n" \
  "s_add_u32 s94, s51, 196608\n" \
  "buffer_load_dwordx4 v[16:19], v248, s[84:87], s94 offen offset:0 sc1\n" \
  "s_lshl_b32 s61, s41, 14\n" \
  "s_lshl_b32 s62, s41, 11\n" \
  "s_add_u32 s61, s61, 0x10000\n" \
  "v_add_u32 v244, s61, v248\n" \
  "v_add_u32 v245, s62, v248\n" \
  "v_and_b32 v246, 0xf0, v248\n" \
  "v_lshrrev_b32 v247, 8, v248\n" \
  "v_lshlrev_b32 v246, 6, v246\n" \
  "v_lshl_or_b32 v246, v247, 5, v246\n" \
  "s_lshr_b32 s62, s41, 1\n" \
  "s_lshl_b32 s62, s62, 14\n" \
  "s_and_b32 s63, s41, 1\n" \
  "s_lshl_b32 s63, s63, 4\n" \
  "s_add_u32 s62, s62, s63\n" \
  "v_add_u32 v246, s62, v246\n" \
  "s_waitcnt vmcnt(7)\n" \
  "v_max_u32 v251, v72, v74\n" \
  "v_cmp_eq_u32 vcc, -1, v251\n" \
  "s_cbranch_vccnz .Lp_s78\n" \
  ".Lp_b78:\n" \
  "v_mfma_f32_16x16x32_bf16 v[40:43], v[132:135], v[72:75], 0\n" \
  "v_mfma_f32_16x16x32_bf16 v[44:47], v[136:139], v[72:75], 0\n" \
  "s_waitcnt vmcnt(6)\n" \
  "v_max_u32 v251, v232, v234\n" \
  "v_mfma_f32_16x16x32_bf16 v[48:51], v[140:143], v[72:75], 0\n" \
  "v_cmp_eq_u32 vcc, -1, v251\n" \
  "v_mfma_f32_16x16x32_bf16 v[52:55], v[144:147], v[72:75], 0\n" \
  "s_cbranch_vccnz .Lp_s79\n" \
  ".Lp_b79:\n" \
  "v_mfma_f32_16x16x32_bf16 v[56:59], v[132:135], v[232:235], 0\n" \
  "v_mfma_f32_16x16x32_bf16 v[60:63], v[136:139], v[232:235], 0\n" \
  "s_waitcnt vmcnt(5)\n" \
  "v_max_u32 v251, v76, v78\n" \
  "v_mfma_f32_16x16x32_bf16 v[64:67], v[140:143], v[232:235], 0\n" \
  "v_cmp_eq_u32 vcc, -1, v251\n" \
  "v_mfma_f32_16x16x32_bf16 v[68:71], v[144:147], v[232:235], 0\n" \
  "s_cbranch_vccnz .Lp_s80\n" \
  ".Lp_b80:\n" \
  "v_mfma_f32_16x16x32_bf16 v[40:43], v[148:151], v[76:79], v[40:43]\n" \
  "v_mfma_f32_16x16x32_bf16 v[44:47], v[152:155], v[76:79], v[44:47]\n" \
  "s_waitcnt vmcnt(4)\n" \
  "v_max_u32 v251, v236, v238\n" \
  "v_mfma_f32_16x16x32_bf16 v[48:51], v[156:159], v[76:79], v[48:51]\n" \
  "v_cmp_eq_u32 vcc, -1, v251\n" \
  "v_mfma_f32_16x16x32_bf16 v[52:55], v[160:163], v[76:79], v[52:55]\n" \
  "s_cbranch_vccnz .Lp_s81\n" \
  ".Lp_b81:\n" \
  "v_mfma_f32_16x16x32_bf16 v[56:59], v[148:151], v[236:239], v[56:59]\n" \
  "v_mfma_f32_16x16x32_bf16 v[60:63], v[152:155], v[236:239], v[60:63]\n" \
  "s_waitcnt vmcnt(3)\n" \
  "v_max_u32 v251, v0, v2\n" \
  "v_mfma_f32_16x16x32_bf16 v[64:67], v[156:159], v[236:239], v[64:67]\n" \
  "v_cmp_eq_u32 vcc, -1, v251\n" \
  "v_mfma_f32_16x16x32_bf16 v[68:71], v[160:163], v[236:239], v[68:71]\n" \
  "s_cbranch_vccnz .Lp_s82\n" \
  ".Lp_b82:\n" \
  "v_mfma_f32_16x16x32_bf16 v[40:43], v[164:167], v[0:3], v[40:43]\n" \
  "v_mfma_f32_16x16x32_bf16 v[44:47], v[168:171], v[0:3], v[44:47]\n" \
  "s_waitcnt vmcnt(2)\n" \
  "v_max_u32 v251, v80, v82\n" \
  "v_mfma_f32_16x16x32_bf16 v[48:51], v[172:175], v[0:3], v[48:51]\n" \
  "v_cmp_eq_u32 vcc, -1, v251\n" \
  "v_mfma_f32_16x16x32_bf16 v[52:55], v[176:179], v[0:3], v[52:55]\n" \
  "s_cbranch_vccnz .Lp_s83\n" \
  ".Lp_b83:\n" \
  "v_mfma_f32_16x16x32_bf16 v[56:59], v[164:167], v[80:83], v[56:59]\n" \
  "v_mfma_f32_16x16x32_bf16 v[60:63], v[168:171], v[80:83], v[60:63]\n" \
  "s_waitcnt vmcnt(1)\n" \
  "v_max_u32 v251, v4, v6\n" \
  "v_mfma_f32_16x16x32_bf16 v[64:67], v[172:175], v[80:83], v[64:67]\n" \
  "v_cmp_eq_u32 vcc, -1, v251\n" \
  "v_mfma_f32_16x16x32_bf16 v[68:71], v[176:179], v[80:83], v[68:71]\n" \
  "s_cbranch_vccnz .Lp_s84\n" \
  ".Lp_b84:\n" \
  "v_mfma_f32_16x16x32_bf16 v[40:43], v[180:183], v[4:7], v[40:43]\n" \
  "v_mfma_f32_16x16x32_bf16 v[44:47], v[184:187], v[4:7], v[44:47]\n" \
  "s_waitcnt vmcnt(0)\n" \
  "v_max_u32 v251, v16, v18\n" \
  "v_mfma_f32_16x16x32_bf16 v[48:51], v[188:191], v[4:7], v[48:51]\n" \
  "v_cmp_eq_u32 vcc, -1, v251\n" \
  "v_mfma_f32_16x16x32_bf16 v[52:55], v[192:195], v[4:7], v[52:55]\n" \
  "s_cbranch_vccnz .Lp_s85\n" \
  ".Lp_b85:\n" \
  "v_mfma_f32_16x16x32_bf16 v[56:59], v[180:183], v[16:19], v[56:59]\n" \
  "v_mfma_f32_16x16x32_bf16 v[60:63], v[184:187], v[16:19], v[60:63]\n" \
  "v_mfma_f32_16x16x32_bf16 v[64:67], v[188:191], v[16:19], v[64:67]\n" \
  "v_mfma_f32_16x16x32_bf16 v[68:71], v[192:195], v[16:19], v[68:71]\n" \
  "v_add_u32 v251, s60, v248\n" \
  "s_barrier\n" \
  "s_add_u32 s94, s51, 147456\n" \
  "buffer_load_dwordx4 v[8:11], v248, s[84:87], s94 offen offset:0 sc1\n" \
  "s_add_u32 s94, s51, 204800\n" \
  "buffer_load_dwordx4 v[24:27], v248, s[84:87], s94 offen offset:0 sc1\n" \
  "s_add_u32 s94, s51, 155648\n" \
  "buffer_load_dwordx4 v[12:15], v248, s[84:87], s94 offen offset:0 sc1\n" \
  "s_add_u32 s94, s51, 212992\n" \
  "buffer_load_dwordx4 v[32:35], v248, s[84:87], s94 offen offset:0 sc1\n" \
  "s_add_u32 s94, s51, 163840\n" \
  "buffer_load_dwordx4 v[228:231], v248, s[84:87], s94 offen offset:0 sc1\n" \
  "s_add_u32 s94, s51, 221184\n" \
  "buffer_load_dwordx4 v[36:39], v248, s[84:87], s94 offen offset:0 sc1\n" \
  "ds_write_b128 v251, v[240:243] offset:52224\n" \
  "ds_write_b128 v249, v[100:103] offset:0\n" \
  "ds_write_b128 v249, v[104:107] offset:8192\n" \
  "ds_write_b128 v249, v[108:111] offset:16384\n" \
  "ds_write_b128 v249, v[112:115] offset:24576\n" \
  "ds_write_b128 v249, v[116:119] offset:32768\n" \
  "ds_write_b128 v249, v[120:123] offset:40960\n" \
  "ds_write_b128 v249, v[124:127] offset:49152\n" \
  "ds_write_b128 v249, v[128:131] offset:57344\n" \
  "s_waitcnt vmcnt(5)\n" \
  "v_max_u32 v251, v8, v10\n" \
  "v_cmp_eq_u32 vcc, -1, v251\n" \
  "s_cbranch_vccnz .Lp_s86\n" \
  ".Lp_b86:\n" \
  "v_mfma_f32_16x16x32_bf16 v[40:43], v[196:199], v[8:11], v[40:43]\n" \
  "v_mfma_f32_16x16x32_bf16 v[44:47], v[200:203], v[8:11], v[44:47]\n" \
  "s_waitcnt vmcnt(4)\n" \
  "v_max_u32 v251, v24, v26\n" \
  "v_mfma_f32_16x16x32_bf16 v[48:51], v[204:207], v[8:11], v[48:51]\n" \
  "v_cmp_eq_u32 vcc, -1, v251\n" \
  "v_mfma_f32_16x16x32_bf16 v[52:55], v[208:211], v[8:11], v[52:55]\n" \
  "s_cbranch_vccnz .Lp_s87\n" \
  ".Lp_b87:\n" \
  "v_mfma_f32_16x16x32_bf16 v[56:59], v[196:199], v[24:27], v[56:59]\n" \
  "v_mfma_f32_16x16x32_bf16 v[60:63], v[200:203], v[24:27], v[60:63]\n" \
  "s_waitcnt vmcnt(3)\n" \
  "v_max_u32 v251, v12, v14\n" \
  "v_mfma_f32_16x16x32_bf16 v[64:67], v[204:207], v[24:27], v[64:67]\n" \
  "v_cmp_eq_u32 vcc, -1, v251\n" \
  "v_mfma_f32_16x16x32_bf16 v[68:71], v[208:211], v[24:27], v[68:71]\n" \
  "s_cbranch_vccnz .Lp_s88\n" \
  ".Lp_b88:\n" \
  "v_mfma_f32_16x16x32_bf16 v[40:43], v[212:215], v[12:15], v[40:43]\n" \
  "v_mfma_f32_16x16x32_bf16 v[44:47], v[216:219], v[12:15], v[44:47]\n" \
  "s_waitcnt vmcnt(2)\n" \
  "v_max_u32 v251, v32, v34\n" \
  "v_mfma_f32_16x16x32_bf16 v[48:51], v[220:223], v[12:15], v[48:51]\n" \
  "v_cmp_eq_u32 vcc, -1, v251\n" \
  "v_mfma_f32_16x16x32_bf16 v[52:55], v[224:227], v[12:15], v[52:55]\n" \
  "s_cbranch_vccnz .Lp_s89\n" \
  ".Lp_b89:\n" \
  "v_mfma_f32_16x16x32_bf16 v[56:59], v[212:215], v[32:35], v[56:59]\n" \
  "v_mfma_f32_16x16x32_bf16 v[60:63], v[216:219], v[32:35], v[60:63]\n" \
  "s_waitcnt vmcnt(1)\n" \
  "v_max_u32 v251, v228, v230\n" \
  "v_mfma_f32_16x16x32_bf16 v[64:67], v[220:223], v[32:35], v[64:67]\n" \
  "v_cmp_eq_u32 vcc, -1, v251\n" \
  "v_mfma_f32_16x16x32_bf16 v[68:71], v[224:227], v[32:35], v[68:71]\n" \
  "s_cbranch_vccnz .Lp_s90\n" \
  ".Lp_b90:\n" \
  "v_mfma_f32_16x16x32_bf16 v[40:43], v[84:87], v[228:231], v[40:43]\n" \
  "v_mfma_f32_16x16x32_bf16 v[44:47], v[88:91], v[228:231], v[44:47]\n" \
  "s_waitcnt vmcnt(0)\n" \
  "v_max_u32 v251, v36, v38\n" \
  "v_mfma_f32_16x16x32_bf16 v[48:51], v[92:95], v[228:231], v[48:51]\n" \
  "v_cmp_eq_u32 vcc, -1, v251\n" \
  "v_mfma_f32_16x16x32_bf16 v[52:55], v[96:99], v[228:231], v[52:55]\n" \
  "s_cbranch_vccnz .Lp_s91\n" \
  ".Lp_b91:\n" \
  "v_mfma_f32_16x16x32_bf16 v[56:59], v[84:87], v[36:39], v[56:59]\n" \
  "ds_write_b128 v253, v[40:43] offset:0\n" \
  "v_mfma_f32_16x16x32_bf16 v[60:63], v[88:91], v[36:39], v[60:63]\n" \
  "ds_write_b128 v253, v[44:47] offset:8192\n" \
  "v_mfma_f32_16x16x32_bf16 v[64:67], v[92:95], v[36:39], v[64:67]\n" \
  "ds_write_b128 v253, v[48:51] offset:16384\n" \
  "v_mfma_f32_16x16x32_bf16 v[68:71], v[96:99], v[36:39], v[68:71]\n" \
  "ds_write_b128 v253, v[52:55] offset:24576\n" \
  "s_nop 0\n" \
  "ds_write_b128 v253, v[56:59] offset:32768\n" \
  "s_nop 0\n" \
  "ds_write_b128 v253, v[60:63] offset:40960\n" \
  "s_nop 0\n" \
  "ds_write_b128 v250, v[64:67] offset:0\n" \
  "s_nop 0\n" \
  "ds_write_b128 v250, v[68:71] offset:8192\n" \
  "s_waitcnt lgkmcnt(0)\n" \
  "s_barrier\n" \
  "ds_read_b128 v[80:83], v245 offset:49152\n" \
  "ds_read_b128 v[84:87], v245 offset:50176\n" \
  "ds_read_b128 v[16:19], v244 offset:0\n" \
  "ds_read_b128 v[48:51], v244 offset:8192\n" \
  "ds_read_b128 v[20:23], v244 offset:1024\n" \
  "ds_read_b128 v[52:55], v244 offset:9216\n" \
  "ds_read_b128 v[24:27], v244 offset:2048\n" \
  "ds_read_b128 v[56:59], v244 offset:10240\n" \
  "ds_read_b128 v[28:31], v244 offset:3072\n" \
  "ds_read_b128 v[60:63], v244 offset:11264\n" \
  "ds_read_b128 v[32:35], v244 offset:4096\n" \
  "ds_read_b128 v[64:67], v244 offset:12288\n" \
  "ds_read_b128 v[36:39], v244 offset:5120\n" \
  "ds_read_b128 v[68:71], v244 offset:13312\n" \
  "ds_read_b128 v[40:43], v244 offset:6144\n" \
  "ds_read_b128 v[72:75], v244 offset:14336\n" \
  "ds_read_b128 v[44:47], v244 offset:7168\n" \
  "ds_read_b128 v[76:79], v244 offset:15360\n" \
  "s_waitcnt lgkmcnt(12)\n" \
  "v_pk_add_f32 v[80:81], v[80:81], v[16:17]\n" \
  "v_pk_add_f32 v[82:83], v[82:83], v[18:19]\n" \
  "v_pk_add_f32 v[84:85], v[84:85], v[48:49]\n" \
  "v_pk_add_f32 v[86:87], v[86:87], v[50:51]\n" \
  "v_pk_add_f32 v[80:81], v[80:81], v[20:21]\n" \
  "v_pk_add_f32 v[82:83], v[82:83], v[22:23]\n" \
  "v_pk_add_f32 v[84:85], v[84:85], v[52:53]\n" \
  "v_pk_add_f32 v[86:87], v[86:87], v[54:55]\n" \
  "s_waitcnt lgkmcnt(8)\n" \
  "v_pk_add_f32 v[80:81], v[80:81], v[24:25]\n" \
  "v_pk_add_f32 v[82:83], v[82:83], v[26:27]\n" \
  "v_pk_add_f32 v[84:85], v[84:85], v[56:57]\n" \
  "v_pk_add_f32 v[86:87], v[86:87], v[58:59]\n" \
  "v_pk_add_f32 v[80:81], v[80:81], v[28:29]\n" \
  "v_pk_add_f32 v[82:83], v[82:83], v[30:31]\n" \
  "v_pk_add_f32 v[84:85], v[84:85], v[60:61]\n" \
  "v_pk_add_f32 v[86:87], v[86:87], v[62:63]\n" \
  "s_waitcnt lgkmcnt(4)\n" \
  "v_pk_add_f32 v[80:81], v[80:81], v[32:33]\n" \
  "v_pk_add_f32 v[82:83], v[82:83], v[34:35]\n" \
  "v_pk_add_f32 v[84:85], v[84:85], v[64:65]\n" \
  "v_pk_add_f32 v[86:87], v[86:87], v[66:67]\n" \
  "v_pk_add_f32 v[80:81], v[80:81], v[36:37]\n" \
  "v_pk_add_f32 v[82:83], v[82:83], v[38:39]\n" \
  "v_pk_add_f32 v[84:85], v[84:85], v[68:69]\n" \
  "v_pk_add_f32 v[86:87], v[86:87], v[70:71]\n" \
  "s_waitcnt lgkmcnt(0)\n" \
  "v_pk_add_f32 v[80:81], v[80:81], v[40:41]\n" \
  "v_pk_add_f32 v[82:83], v[82:83], v[42:43]\n" \
  "v_pk_add_f32 v[84:85], v[84:85], v[72:73]\n" \
  "v_pk_add_f32 v[86:87], v[86:87], v[74:75]\n" \
  "v_pk_add_f32 v[80:81], v[80:81], v[44:45]\n" \
  "v_pk_add_f32 v[82:83], v[82:83], v[46:47]\n" \
  "v_pk_add_f32 v[84:85], v[84:85], v[76:77]\n" \
  "v_pk_add_f32 v[86:87], v[86:87], v[78:79]\n" \
  "v_cvt_pk_bf16_f32 v88, v80, v81\n" \
  "v_cvt_pk_bf16_f32 v89, v82, v83\n" \
  "v_cvt_pk_bf16_f32 v90, v84, v85\n" \
  "v_cvt_pk_bf16_f32 v91, v86, v87\n" \
  "global_store_dwordx4 v246, v[88:91], s[54:55]\n" \
  "s_branch .Lp_end\n" \
  ".Lp_e5s:\n" \
  "s_add_u32 s94, s51, 16384\n" \
  "buffer_load_dwordx4 v[32:35], v248, s[84:87], s94 offen offset:0 sc1\n" \
  "s_add_u32 s94, s51, 73728\n" \
  "buffer_load_dwordx4 v[52:55], v248, s[84:87], s94 offen offset:0 sc1\n" \
  "s_waitcnt vmcnt(7)\n" \
  "v_max_u32 v251, v16, v18\n" \
  "v_cmp_eq_u32 vcc, -1, v251\n" \
  "s_cbranch_vccnz .Lp_s92\n" \
  ".Lp_b92:\n" \
  "v_mfma_f32_16x16x32_bf16 v[100:103], v[132:135], v[16:19], 0\n" \
  "s_add_u32 s94, s51, 24576\n" \
  "buffer_load_dwordx4 v[36:39], v248, s[84:87], s94 offen offset:0 sc1\n" \
  "v_mfma_f32_16x16x32_bf16 v[104:107], v[136:139], v[16:19], 0\n" \
  "s_waitcnt vmcnt(7)\n" \
  "v_max_u32 v251, v20, v22\n" \
  "s_add_u32 s94, s51, 81920\n" \
  "buffer_load_dwordx4 v[56:59], v248, s[84:87], s94 offen offset:0 sc1\n" \
  "v_mfma_f32_16x16x32_bf16 v[108:111], v[140:143], v[16:19], 0\n" \
  "v_cmp_eq_u32 vcc, -1, v251\n" \
  "s_add_u32 s94, s51, 32768\n" \
  "buffer_load_dwordx4 v[40:43], v248, s[84:87], s94 offen offset:0 sc1\n" \
  "v_mfma_f32_16x16x32_bf16 v[112:115], v[144:147], v[16:19], 0\n" \
  "s_cbranch_vccnz .Lp_s93\n" \
  ".Lp_b93:\n" \
  "s_add_u32 s94, s51, 90112\n" \
  "buffer_load_dwordx4 v[60:63], v248, s[84:87], s94 offen offset:0 sc1\n" \
  "v_mfma_f32_16x16x32_bf16 v[116:119], v[132:135], v[20:23], 0\n" \
  "s_add_u32 s94, s51, 40960\n" \
  "buffer_load_dwordx4 v[44:47], v248, s[84:87], s94 offen offset:0 sc1\n" \
  "v_mfma_f32_16x16x32_bf16 v[120:123], v[136:139], v[20:23], 0\n" \
  "s_waitcnt vmcnt(10)\n" \
  "v_max_u32 v251, v24, v26\n" \
  "s_add_u32 s94, s51, 98304\n" \
  "buffer_load_dwordx4 v[64:67], v248, s[84:87], s94 offen offset:0 sc1\n" \
  "v_mfma_f32_16x16x32_bf16 v[124:127], v[140:143], v[20:23], 0\n" \
  "v_cmp_eq_u32 vcc, -1, v251\n" \
  "s_add_u32 s94, s51, 49152\n" \
  "buffer_load_dwordx4 v[48:51], v248, s[84:87], s94 offen offset:0 sc1\n" \
  "v_mfma_f32_16x16x32_bf16 v[128:131], v[144:147], v[20:23], 0\n" \
  "s_cbranch_vccnz .Lp_s94\n" \
  ".Lp_b94:\n" \
  "s_add_u32 s94, s51, 106496\n" \
  "buffer_load_dwordx4 v[68:71], v248, s[84:87], s94 offen offset:0 sc1\n" \
  "v_mfma_f32_16x16x32_bf16 v[100:103], v[148:151], v[24:27], v[100:103]\n" \
  "s_add_u32 s94, s51, 114688\n" \
  "buffer_load_dwordx4 v[72:75], v248, s[84:87], s94 offen offset:0 sc1\n" \
  "v_mfma_f32_16x16x32_bf16 v[104:107], v[152:155], v[24:27], v[104:107]\n" \
  "s_waitcnt vmcnt(13)\n" \
  "v_max_u32 v251, v28, v30\n" \
  "s_add_u32 s94, s51, 172032\n" \
  "buffer_load_dwordx4 v[232:235], v248, s[84:87], s94 offen offset:0 sc1\n" \
  "v_mfma_f32_16x16x32_bf16 v[108:111], v[156:159], v[24:27], v[108:111]\n" \
  "v_cmp_eq_u32 vcc, -1, v251\n" \
  "s_add_u32 s94, s51, 122880\n" \
  "buffer_load_dwordx4 v[76:79], v248, s[84:87], s94 offen offset:0 sc1\n" \
  "v_mfma_f32_16x16x32_bf16 v[112:115], v[160:163], v[24:27], v[112:115]\n" \
  "s_cbranch_vccnz .Lp_s95\n" \
  ".Lp_b95:\n" \
  "s_add_u32 s94, s51, 180224\n" \
  "buffer_load_dwordx4 v[236:239], v248, s[84:87], s94 offen offset:0 sc1\n" \
  "v_mfma_f32_16x16x32_bf16 v[116:119], v[148:151], v[28:31], v[116:119]\n" \
  "s_add_u32 s94, s51, 131072\n" \
  "buffer_load_dwordx4 v[0:3], v248, s[84:87], s94 offen offset:0 sc1\n" \
  "v_mfma_f32_16x16x32_bf16 v[120:123], v[152:155], v[28:31], v[120:123]\n" \
  "s_waitcnt vmcnt(14)\n" \
  "v_max_u32 v251, v32, v34\n" \
  "s_add_u32 s94, s51, 188416\n" \
  "buffer_load_dwordx4 v[80:83], v248, s[84:87], s94 offen offset:0 sc1\n" \
  "v_mfma_f32_16x16x32_bf16 v[124:127], v[156:159], v[28:31], v[124:127]\n" \
  "v_cmp_eq_u32 vcc, -1, v251\n" \
  "s_add_u32 s94, s51, 139264\n" \
  "buffer_load_dwordx4 v[4:7], v248, s[84:87], s94 offen offset:0 sc1\n" \
  "v_mfma_f32_16x16x32_bf16 v[128:131], v[160:163], v[28:31], v[128:131]\n" \
  "s_cbranch_vccnz .Lp_s96\n" \
  ".Lp_b96:\n" \
  "s_add_u32 s94, s51, 196608\n" \
  "buffer_load_dwordx4 v[16:19], v248, s[84:87], s94 offen offset:0 sc1\n" \
  "v_mfma_f32_16x16x32_bf16 v[100:103], v[164:167], v[32:35], v[100:103]\n" \
  "v_mfma_f32_16x16x32_bf16 v[104:107], v[168:171], v[32:35], v[104:107]\n" \
  "s_waitcnt vmcnt(16)\n" \
  "v_max_u32 v251, v52, v54\n" \
  "v_mfma_f32_16x16x32_bf16 v[108:111], v[172:175], v[32:35], v[108:111]\n" \
  "v_cmp_eq_u32 vcc, -1, v251\n" \
  "v_mfma_f32_16x16x32_bf16 v[112:115], v[176:179], v[32:35], v[112:115]\n" \
  "s_cbranch_vccnz .Lp_s97\n" \
  ".Lp_b97:\n" \
  "s_waitcnt lgkmcnt(0)\n" \
  "s_barrier\n" \
  "s_add_u32 s94, s51, 147456\n" \
  "buffer_load_dwordx4 v[8:11], v248, s[84:87], s94 offen offset:0 sc1\n" \
  "v_mfma_f32_16x16x32_bf16 v[116:119], v[164:167], v[52:55], v[116:119]\n" \
  "v_mfma_f32_16x16x32_bf16 v[120:123], v[168:171], v[52:55], v[120:123]\n" \
  "s_waitcnt vmcnt(16)\n" \
  "v_max_u32 v251, v36, v38\n" \
  "s_add_u32 s94, s51, 204800\n" \
  "buffer_load_dwordx4 v[24:27], v248, s[84:87], s94 offen offset:0 sc1\n" \
  "v_mfma_f32_16x16x32_bf16 v[124:127], v[172:175], v[52:55], v[124:127]\n" \
  "v_cmp_eq_u32 vcc, -1, v251\n" \
  "v_mfma_f32_16x16x32_bf16 v[128:131], v[176:179], v[52:55], v[128:131]\n" \
  "s_cbranch_vccnz .Lp_s98\n" \
  ".Lp_b98:\n" \
  "s_add_u32 s94, s51, 155648\n" \
  "buffer_load_dwordx4 v[12:15], v248, s[84:87], s94 offen offset:0 sc1\n" \
  "v_mfma_f32_16x16x32_bf16 v[100:103], v[180:183], v[36:39], v[100:103]\n" \
  "v_mfma_f32_16x16x32_bf16 v[104:107], v[184:187], v[36:39], v[104:107]\n" \
  "s_waitcnt vmcnt(17)\n" \
  "v_max_u32 v251, v56, v58\n" \
  "s_add_u32 s94, s51, 212992\n" \
  "buffer_load_dwordx4 v[32:35], v248, s[84:87], s94 offen offset:0 sc1\n" \
  "v_mfma_f32_16x16x32_bf16 v[108:111], v[188:191], v[36:39], v[108:111]\n" \
  "v_cmp_eq_u32 vcc, -1, v251\n" \
  "v_mfma_f32_16x16x32_bf16 v[112:115], v[192:195], v[36:39], v[112:115]\n" \
  "s_cbranch_vccnz .Lp_s99\n" \
  ".Lp_b99:\n" \
  "s_add_u32 s94, s51, 163840\n" \
  "buffer_load_dwordx4 v[228:231], v248, s[84:87], s94 offen offset:0 sc1\n" \
  "v_mfma_f32_16x16x32_bf16 v[116:119], v[180:183], v[56:59], v[116:119]\n" \
  "v_mfma_f32_16x16x32_bf16 v[120:123], v[184:187], v[56:59], v[120:123]\n" \
  "s_waitcnt vmcnt(18)\n" \
  "v_max_u32 v251, v40, v42\n" \
  "s_add_u32 s94, s51, 221184\n" \
  "buffer_load_dwordx4 v[36:39], v248, s[84:87], s94 offen offset:0 sc1\n" \
  "v_mfma_f32_16x16x32_bf16 v[124:127], v[188:191], v[56:59], v[124:127]\n" \
  "v_cmp_eq_u32 vcc, -1, v251\n" \
  "v_mfma_f32_16x16x32_bf16 v[128:131], v[192:195], v[56:59], v[128:131]\n" \
  "s_cbranch_vccnz .Lp_s100\n" \
  ".Lp_b100:\n" \
  "v_mfma_f32_16x16x32_bf16 v[100:103], v[196:199], v[40:43], v[100:103]\n" \
  "s_lshl_b32 s61, s41, 14\n" \
  "s_lshl_b32 s62, s41, 11\n" \
  "s_cmp_eq_u32 s41, 3\n" \
  "s_cselect_b32 s61, 0x20000, s61\n" \
  "v_add_u32 v244, s61, v248\n" \
  "v_add_u32 v245, s62, v248\n" \
  "v_and_b32 v246, 0xf0, v248\n" \
  "v_lshrrev_b32 v247, 8, v248\n" \
  "v_lshlrev_b32 v246, 6, v246\n" \
  "v_lshl_or_b32 v246, v247, 5, v246\n" \
  "s_lshr_b32 s62, s41, 1\n" \
  "s_lshl_b32 s62, s62, 14\n" \
  "s_and_b32 s63, s41, 1\n" \
  "s_lshl_b32 s63, s63, 4\n" \
  "s_add_u32 s62, s62, s63\n" \
  "s_add_u32 s62, s62, 0x8000\n" \
  "v_add_u32 v246, s62, v246\n" \
  "v_mfma_f32_16x16x32_bf16 v[104:107], v[200:203], v[40:43], v[104:107]\n" \
  "s_waitcnt vmcnt(18)\n" \
  "v_max_u32 v251, v60, v62\n" \
  "v_mfma_f32_16x16x32_bf16 v[108:111], v[204:207], v[40:43], v[108:111]\n" \
  "v_cmp_eq_u32 vcc, -1, v251\n" \
  "v_mfma_f32_16x16x32_bf16 v[112:115], v[208:211], v[40:43], v[112:115]\n" \
  "s_cbranch_vccnz .Lp_s101\n" \
  ".Lp_b101:\n" \
  "v_mfma_f32_16x16x32_bf16 v[116:119], v[196:199], v[60:63], v[116:119]\n" \
  "v_mfma_f32_16x16x32_bf16 v[120:123], v[200:203], v[60:63], v[120:123]\n" \
  "s_waitcnt vmcnt(17)\n" \
  "v_max_u32 v251, v44, v46\n" \
  "v_mfma_f32_16x16x32_bf16 v[124:127], v[204:207], v[60:63], v[124:127]\n" \
  "v_cmp_eq_u32 vcc, -1, v251\n" \
  "v_mfma_f32_16x16x32_bf16 v[128:131], v[208:211], v[60:63], v[128:131]\n" \
  "s_cbranch_vccnz .Lp_s102\n" \
  ".Lp_b102:\n" \
  "v_mfma_f32_16x16x32_bf16 v[100:103], v[212:215], v[44:47], v[100:103]\n" \
  "v_mfma_f32_16x16x32_bf16 v[104:107], v[216:219], v[44:47], v[104:107]\n" \
  "s_waitcnt vmcnt(16)\n" \
  "v_max_u32 v251, v64, v66\n" \
  "v_mfma_f32_16x16x32_bf16 v[108:111], v[220:223], v[44:47], v[108:111]\n" \
  "v_cmp_eq_u32 vcc, -1, v251\n" \
  "v_mfma_f32_16x16x32_bf16 v[112:115], v[224:227], v[44:47], v[112:115]\n" \
  "s_cbranch_vccnz .Lp_s103\n" \
  ".Lp_b103:\n" \
  "v_mfma_f32_16x16x32_bf16 v[116:119], v[212:215], v[64:67], v[116:119]\n" \
  "v_mfma_f32_16x16x32_bf16 v[120:123], v[216:219], v[64:67], v[120:123]\n" \
  "s_waitcnt vmcnt(15)\n" \
  "v_max_u32 v251, v48, v50\n" \
  "v_mfma_f32_16x16x32_bf16 v[124:127], v[220:223], v[64:67], v[124:127]\n" \
  "v_cmp_eq_u32 vcc, -1, v251\n" \
  "v_mfma_f32_16x16x32_bf16 v[128:131], v[224:227], v[64:67], v[128:131]\n" \
  "s_cbranch_vccnz .Lp_s104\n" \
  ".Lp_b104:\n" \
  "v_mfma_f32_16x16x32_bf16 v[100:103], v[84:87], v[48:51], v[100:103]\n" \
  "v_mfma_f32_16x16x32_bf16 v[104:107], v[88:91], v[48:51], v[104:107]\n" \
  "s_waitcnt vmcnt(14)\n" \
  "v_max_u32 v251, v68, v70\n" \
  "v_mfma_f32_16x16x32_bf16 v[108:111], v[92:95], v[48:51], v[108:111]\n" \
  "v_cmp_eq_u32 vcc, -1, v251\n" \
  "v_mfma_f32_16x16x32_bf16 v[112:115], v[96:99], v[48:51], v[112:115]\n" \
  "s_cbranch_vccnz .Lp_s105\n" \
  ".Lp_b105:\n" \
  "v_mfma_f32_16x16x32_bf16 v[116:119], v[84:87], v[68:71], v[116:119]\n" \
  "v_mfma_f32_16x16x32_bf16 v[120:123], v[88:91], v[68:71], v[120:123]\n" \
  "v_mfma_f32_16x16x32_bf16 v[124:127], v[92:95], v[68:71], v[124:127]\n" \
  "v_mfma_f32_16x16x32_bf16 v[128:131], v[96:99], v[68:71], v[128:131]\n" \
  "s_waitcnt vmcnt(13)\n" \
  "v_max_u32 v251, v72, v74\n" \
  "v_cmp_eq_u32 vcc, -1, v251\n" \
  "s_cbranch_vccnz .Lp_s106\n" \
  ".Lp_b106:\n" \
  "v_mfma_f32_16x16x32_bf16 v[40:43], v[132:135], v[72:75], 0\n" \
  "v_mfma_f32_16x16x32_bf16 v[44:47], v[136:139], v[72:75], 0\n" \
  "s_waitcnt vmcnt(12)\n" \
  "v_max_u32 v251, v232, v234\n" \
  "v_mfma_f32_16x16x32_bf16 v[48:51], v[140:143], v[72:75], 0\n" \
  "v_cmp_eq_u32 vcc, -1, v251\n" \
  "v_mfma_f32_16x16x32_bf16 v[52:55], v[144:147], v[72:75], 0\n" \
  "s_cbranch_vccnz .Lp_s107\n" \
  ".Lp_b107:\n" \
  "v_mfma_f32_16x16x32_bf16 v[56:59], v[132:135], v[232:235], 0\n" \
  "ds_write_b128 v249, v[100:103] offset:0\n" \
  "v_mfma_f32_16x16x32_bf16 v[60:63], v[136:139], v[232:235], 0\n" \
  "s_waitcnt vmcnt(11)\n" \
  "v_max_u32 v251, v76, v78\n" \
  "v_mfma_f32_16x16x32_bf16 v[64:67], v[140:143], v[232:235], 0\n" \
  "v_cmp_eq_u32 vcc, -1, v251\n" \
  "ds_write_b128 v249, v[104:107] offset:8192\n" \
  "v_mfma_f32_16x16x32_bf16 v[68:71], v[144:147], v[232:235], 0\n" \
  "s_cbranch_vccnz .Lp_s108\n" \
  ".Lp_b108:\n" \
  "v_mfma_f32_16x16x32_bf16 v[40:43], v[148:151], v[76:79], v[40:43]\n" \
  "ds_write_b128 v249, v[108:111] offset:16384\n" \
  "v_mfma_f32_16x16x32_bf16 v[44:47], v[152:155], v[76:79], v[44:47]\n" \
  "s_waitcnt vmcnt(10)\n" \
  "v_max_u32 v251, v236, v238\n" \
  "v_mfma_f32_16x16x32_bf16 v[48:51], v[156:159], v[76:79], v[48:51]\n" \
  "v_cmp_eq_u32 vcc, -1, v251\n" \
  "ds_write_b128 v249, v[112:115] offset:24576\n" \
  "v_mfma_f32_16x16x32_bf16 v[52:55], v[160:163], v[76:79], v[52:55]\n" \
  "s_cbranch_vccnz .Lp_s109\n" \
  ".Lp_b109:\n" \
  "v_mfma_f32_16x16x32_bf16 v[56:59], v[148:151], v[236:239], v[56:59]\n" \
  "ds_write_b128 v249, v[116:119] offset:32768\n" \
  "v_mfma_f32_16x16x32_bf16 v[60:63], v[152:155], v[236:239], v[60:63]\n" \
  "s_waitcnt vmcnt(9)\n" \
  "v_max_u32 v251, v0, v2\n" \
  "v_mfma_f32_16x16x32_bf16 v[64:67], v[156:159], v[236:239], v[64:67]\n" \
  "v_cmp_eq_u32 vcc, -1, v251\n" \
  "ds_write_b128 v249, v[120:123] offset:40960\n" \
  "v_mfma_f32_16x16x32_bf16 v[68:71], v[160:163], v[236:239], v[68:71]\n" \
  "s_cbranch_vccnz .Lp_s110\n" \
  ".Lp_b110:\n" \
  "v_mfma_f32_16x16x32_bf16 v[40:43], v[164:167], v[0:3], v[40:43]\n" \
  "ds_write_b128 v249, v[124:127] offset:49152\n" \
  "v_mfma_f32_16x16x32_bf16 v[44:47], v[168:171], v[0:3], v[44:47]\n" \
  "s_waitcnt vmcnt(8)\n" \
  "v_max_u32 v251, v80, v82\n" \
  "v_mfma_f32_16x16x32_bf16 v[48:51], v[172:175], v[0:3], v[48:51]\n" \
  "v_cmp_eq_u32 vcc, -1, v251\n" \
  "ds_write_b128 v249, v[128:131] offset:57344\n" \
  "v_mfma_f32_16x16x32_bf16 v[52:55], v[176:179], v[0:3], v[52:55]\n" \
  "s_cbranch_vccnz .Lp_s111\n" \
  ".Lp_b111:\n" \
  "v_mfma_f32_16x16x32_bf16 v[56:59], v[164:167], v[80:83], v[56:59]\n" \
  "v_mfma_f32_16x16x32_bf16 v[60:63], v[168:171], v[80:83], v[60:63]\n" \
  "s_waitcnt vmcnt(7)\n" \
  "v_max_u32 v251, v4, v6\n" \
  "v_mfma_f32_16x16x32_bf16 v[64:67], v[172:175], v[80:83], v[64:67]\n" \
  "v_cmp_eq_u32 vcc, -1, v251\n" \
  "v_mfma_f32_16x16x32_bf16 v[68:71], v[176:179], v[80:83], v[68:71]\n" \
  "s_cbranch_vccnz .Lp_s112\n" \
  ".Lp_b112:\n" \
  "v_mfma_f32_16x16x32_bf16 v[40:43], v[180:183], v[4:7], v[40:43]\n" \
  "v_mfma_f32_16x16x32_bf16 v[44:47], v[184:187], v[4:7], v[44:47]\n" \
  "s_waitcnt vmcnt(6)\n" \
  "v_max_u32 v251, v16, v18\n" \
  "v_mfma_f32_16x16x32_bf16 v[48:51], v[188:191], v[4:7], v[48:51]\n" \
  "v_cmp_eq_u32 vcc, -1, v251\n" \
  "v_mfma_f32_16x16x32_bf16 v[52:55], v[192:195], v[4:7], v[52:55]\n" \
  "s_cbranch_vccnz .Lp_s113\n" \
  ".Lp_b113:\n" \
  "v_mfma_f32_16x16x32_bf16 v[56:59], v[180:183], v[16:19], v[56:59]\n" \
  "v_mfma_f32_16x16x32_bf16 v[60:63], v[184:187], v[16:19], v[60:63]\n" \
  "s_waitcnt vmcnt(5)\n" \
  "v_max_u32 v251, v8, v10\n" \
  "v_mfma_f32_16x16x32_bf16 v[64:67], v[188:191], v[16:19], v[64:67]\n" \
  "v_cmp_eq_u32 vcc, -1, v251\n" \
  "v_mfma_f32_16x16x32_bf16 v[68:71], v[192:195], v[16:19], v[68:71]\n" \
  "s_cbranch_vccnz .Lp_s114\n" \
  ".Lp_b114:\n" \
  "v_mfma_f32_16x16x32_bf16 v[40:43], v[196:199], v[8:11], v[40:43]\n" \
  "v_mfma_f32_16x16x32_bf16 v[44:47], v[200:203], v[8:11], v[44:47]\n" \
  "s_waitcnt vmcnt(4)\n" \
  "v_max_u32 v251, v24, v26\n" \
  "v_mfma_f32_16x16x32_bf16 v[48:51], v[204:207], v[8:11], v[48:51]\n" \
  "v_cmp_eq_u32 vcc, -1, v251\n" \
  "v_mfma_f32_16x16x32_bf16 v[52:55], v[208:211], v[8:11], v[52:55]\n" \
  "s_cbranch_vccnz .Lp_s115\n" \
  ".Lp_b115:\n" \
  "v_mfma_f32_16x16x32_bf16 v[56:59], v[196:199], v[24:27], v[56:59]\n" \
  "v_mfma_f32_16x16x32_bf16 v[60:63], v[200:203], v[24:27], v[60:63]\n" \
  "s_waitcnt vmcnt(3)\n" \
  "v_max_u32 v251, v12, v14\n" \
  "v_mfma_f32_16x16x32_bf16 v[64:67], v[204:207], v[24:27], v[64:67]\n" \
  "v_cmp_eq_u32 vcc, -1, v251\n" \
  "v_mfma_f32_16x16x32_bf16 v[68:71], v[208:211], v[24:27], v[68:71]\n" \
  "s_cbranch_vccnz .Lp_s116\n" \
  ".Lp_b116:\n" \
  "v_mfma_f32_16x16x32_bf16 v[40:43], v[212:215], v[12:15], v[40:43]\n" \
  "v_mfma_f32_16x16x32_bf16 v[44:47], v[216:219], v[12:15], v[44:47]\n" \
  "s_waitcnt vmcnt(2)\n" \
  "v_max_u32 v251, v32, v34\n" \
  "v_mfma_f32_16x16x32_bf16 v[48:51], v[220:223], v[12:15], v[48:51]\n" \
  "v_cmp_eq_u32 vcc, -1, v251\n" \
  "v_mfma_f32_16x16x32_bf16 v[52:55], v[224:227], v[12:15], v[52:55]\n" \
  "s_cbranch_vccnz .Lp_s117\n" \
  ".Lp_b117:\n" \
  "v_mfma_f32_16x16x32_bf16 v[56:59], v[212:215], v[32:35], v[56:59]\n" \
  "v_mfma_f32_16x16x32_bf16 v[60:63], v[216:219], v[32:35], v[60:63]\n" \
  "s_waitcnt vmcnt(1)\n" \
  "v_max_u32 v251, v228, v230\n" \
  "v_mfma_f32_16x16x32_bf16 v[64:67], v[220:223], v[32:35], v[64:67]\n" \
  "v_cmp_eq_u32 vcc, -1, v251\n" \
  "v_mfma_f32_16x16x32_bf16 v[68:71], v[224:227], v[32:35], v[68:71]\n" \
  "s_cbranch_vccnz .Lp_s118\n" \
  ".Lp_b118:\n" \
  "v_mfma_f32_16x16x32_bf16 v[40:43], v[84:87], v[228:231], v[40:43]\n" \
  "v_mfma_f32_16x16x32_bf16 v[44:47], v[88:91], v[228:231], v[44:47]\n" \
  "s_waitcnt vmcnt(0)\n" \
  "v_max_u32 v251, v36, v38\n" \
  "v_mfma_f32_16x16x32_bf16 v[48:51], v[92:95], v[228:231], v[48:51]\n" \
  "v_cmp_eq_u32 vcc, -1, v251\n" \
  "v_mfma_f32_16x16x32_bf16 v[52:55], v[96:99], v[228:231], v[52:55]\n" \
  "s_cbranch_vccnz .Lp_s119\n" \
  ".Lp_b119:\n" \
  "v_mfma_f32_16x16x32_bf16 v[56:59], v[84:87], v[36:39], v[56:59]\n" \
  "ds_write_b128 v253, v[40:43] offset:0\n" \
  "v_mfma_f32_16x16x32_bf16 v[60:63], v[88:91], v[36:39], v[60:63]\n" \
  "ds_write_b128 v253, v[44:47] offset:8192\n" \
  "v_mfma_f32_16x16x32_bf16 v[64:67], v[92:95], v[36:39], v[64:67]\n" \
  "ds_write_b128 v253, v[48:51] offset:16384\n" \
  "v_mfma_f32_16x16x32_bf16 v[68:71], v[96:99], v[36:39], v[68:71]\n" \
  "ds_write_b128 v253, v[52:55] offset:24576\n" \
  "s_nop 0\n" \
  "ds_write_b128 v253, v[56:59] offset:32768\n" \
  "s_nop 0\n" \
  "ds_write_b128 v253, v[60:63] offset:40960\n" \
  "s_nop 0\n" \
  "ds_write_b128 v250, v[64:67] offset:0\n" \
  "s_nop 0\n" \
  "ds_write_b128 v250, v[68:71] offset:8192\n" \
  "s_waitcnt lgkmcnt(0)\n" \
  "s_barrier\n" \
  "ds_read_b128 v[80:83], v245 offset:57344\n" \
  "ds_read_b128 v[84:87], v245 offset:58368\n" \
  "ds_read_b128 v[16:19], v244 offset:0\n" \
  "ds_read_b128 v[48:51], v244 offset:8192\n" \
  "ds_read_b128 v[20:23], v244 offset:1024\n" \
  "ds_read_b128 v[52:55], v244 offset:9216\n" \
  "ds_read_b128 v[24:27], v244 offset:2048\n" \
  "ds_read_b128 v[56:59], v244 offset:10240\n" \
  "ds_read_b128 v[28:31], v244 offset:3072\n" \
  "ds_read_b128 v[60:63], v244 offset:11264\n" \
  "ds_read_b128 v[32:35], v244 offset:4096\n" \
  "ds_read_b128 v[64:67], v244 offset:12288\n" \
  "ds_read_b128 v[36:39], v244 offset:5120\n" \
  "ds_read_b128 v[68:71], v244 offset:13312\n" \
  "ds_read_b128 v[40:43], v244 offset:6144\n" \
  "ds_read_b128 v[72:75], v244 offset:14336\n" \
  "ds_read_b128 v[44:47], v244 offset:7168\n" \
  "ds_read_b128 v[76:79], v244 offset:15360\n" \
  "s_waitcnt lgkmcnt(12)\n" \
  "v_pk_add_f32 v[80:81], v[80:81], v[16:17]\n" \
  "v_pk_add_f32 v[82:83], v[82:83], v[18:19]\n" \
  "v_pk_add_f32 v[84:85], v[84:85], v[48:49]\n" \
  "v_pk_add_f32 v[86:87], v[86:87], v[50:51]\n" \
  "v_pk_add_f32 v[80:81], v[80:81], v[20:21]\n" \
  "v_pk_add_f32 v[82:83], v[82:83], v[22:23]\n" \
  "v_pk_add_f32 v[84:85], v[84:85], v[52:53]\n" \
  "v_pk_add_f32 v[86:87], v[86:87], v[54:55]\n" \
  "s_waitcnt lgkmcnt(8)\n" \
  "v_pk_add_f32 v[80:81], v[80:81], v[24:25]\n" \
  "v_pk_add_f32 v[82:83], v[82:83], v[26:27]\n" \
  "v_pk_add_f32 v[84:85], v[84:85], v[56:57]\n" \
  "v_pk_add_f32 v[86:87], v[86:87], v[58:59]\n" \
  "v_pk_add_f32 v[80:81], v[80:81], v[28:29]\n" \
  "v_pk_add_f32 v[82:83], v[82:83], v[30:31]\n" \
  "v_pk_add_f32 v[84:85], v[84:85], v[60:61]\n" \
  "v_pk_add_f32 v[86:87], v[86:87], v[62:63]\n" \
  "s_waitcnt lgkmcnt(4)\n" \
  "v_pk_add_f32 v[80:81], v[80:81], v[32:33]\n" \
  "v_pk_add_f32 v[82:83], v[82:83], v[34:35]\n" \
  "v_pk_add_f32 v[84:85], v[84:85], v[64:65]\n" \
  "v_pk_add_f32 v[86:87], v[86:87], v[66:67]\n" \
  "v_pk_add_f32 v[80:81], v[80:81], v[36:37]\n" \
  "v_pk_add_f32 v[82:83], v[82:83], v[38:39]\n" \
  "v_pk_add_f32 v[84:85], v[84:85], v[68:69]\n" \
  "v_pk_add_f32 v[86:87], v[86:87], v[70:71]\n" \
  "s_waitcnt lgkmcnt(0)\n" \
  "v_pk_add_f32 v[80:81], v[80:81], v[40:41]\n" \
  "v_pk_add_f32 v[82:83], v[82:83], v[42:43]\n" \
  "v_pk_add_f32 v[84:85], v[84:85], v[72:73]\n" \
  "v_pk_add_f32 v[86:87], v[86:87], v[74:75]\n" \
  "v_pk_add_f32 v[80:81], v[80:81], v[44:45]\n" \
  "v_pk_add_f32 v[82:83], v[82:83], v[46:47]\n" \
  "v_pk_add_f32 v[84:85], v[84:85], v[76:77]\n" \
  "v_pk_add_f32 v[86:87], v[86:87], v[78:79]\n" \
  "v_cvt_pk_bf16_f32 v88, v80, v81\n" \
  "v_cvt_pk_bf16_f32 v89, v82, v83\n" \
  "v_cvt_pk_bf16_f32 v90, v84, v85\n" \
  "v_cvt_pk_bf16_f32 v91, v86, v87\n" \
  "global_store_dwordx4 v246, v[88:91], s[54:55]\n" \
  "s_branch .Lp_end\n" \
  "; STUBS\n" \
  ".Lp_s0:\n" \
  "s_call_b64 s[90:91], .Lp_rfa_p0\n" \
  "s_branch .Lp_b0\n" \
  ".Lp_s1:\n" \
  "s_call_b64 s[90:91], .Lp_rfa_p1\n" \
  "s_branch .Lp_b1\n" \
  ".Lp_s2:\n" \
  "s_call_b64 s[90:91], .Lp_rfa_p2\n" \
  "s_branch .Lp_b2\n" \
  ".Lp_s3:\n" \
  "s_call_b64 s[90:91], .Lp_rfa_p3\n" \
  "s_branch .Lp_b3\n" \
  ".Lp_s4:\n" \
  "s_call_b64 s[90:91], .Lp_rfa_p4\n" \
  "s_branch .Lp_b4\n" \
  ".Lp_s5:\n" \
  "s_call_b64 s[90:91], .Lp_rfa_p5\n" \
  "s_branch .Lp_b5\n" \
  ".Lp_s6:\n" \
  "s_call_b64 s[90:91], .Lp_rfa_p6\n" \
  "s_branch .Lp_b6\n" \
  ".Lp_s7:\n" \
  "s_call_b64 s[90:91], .Lp_rfa_p7\n" \
  "s_branch .Lp_b7\n" \
  ".Lp_s8:\n" \
  "s_call_b64 s[90:91], .Lp_rfa_p8\n" \
  "s_branch .Lp_b8\n" \
  ".Lp_s9:\n" \
  "s_call_b64 s[90:91], .Lp_rfa_p9\n" \
  "s_branch .Lp_b9\n" \
  ".Lp_s10:\n" \
  "s_call_b64 s[90:91], .Lp_rfa_p10\n" \
  "s_branch .Lp_b10\n" \
  ".Lp_s11:\n" \
  "s_call_b64 s[90:91], .Lp_rfa_p11\n" \
  "s_branch .Lp_b11\n" \
  ".Lp_s12:\n" \
  "s_call_b64 s[90:91], .Lp_rfa_p12\n" \
  "s_branch .Lp_b12\n" \
  ".Lp_s13:\n" \
  "s_call_b64 s[90:91], .Lp_rfa_p13\n" \
  "s_branch .Lp_b13\n" \
  ".Lp_s14:\n" \
  "s_call_b64 s[90:91], .Lp_rfa_p14\n" \
  "s_branch .Lp_b14\n" \
  ".Lp_s15:\n" \
  "s_call_b64 s[90:91], .Lp_rfa_p15\n" \
  "s_branch .Lp_b15\n" \
  ".Lp_s16:\n" \
  "s_call_b64 s[90:91], .Lp_rfa_p2\n" \
  "s_branch .Lp_b16\n" \
  ".Lp_s17:\n" \
  "s_call_b64 s[90:91], .Lp_rfa_p3\n" \
  "s_branch .Lp_b17\n" \
  ".Lp_s18:\n" \
  "s_call_b64 s[90:91], .Lp_rfa_p4\n" \
  "s_branch .Lp_b18\n" \
  ".Lp_s19:\n" \
  "s_call_b64 s[90:91], .Lp_rfa_p5\n" \
  "s_branch .Lp_b19\n" \
  ".Lp_s20:\n" \
  "s_call_b64 s[90:91], .Lp_rfa_p6\n" \
  "s_branch .Lp_b20\n" \
  ".Lp_s21:\n" \
  "s_call_b64 s[90:91], .Lp_rfa_p7\n" \
  "s_branch .Lp_b21\n" \
  ".Lp_s22:\n" \
  "s_call_b64 s[90:91], .Lp_rfa_p8\n" \
  "s_branch .Lp_b22\n" \
  ".Lp_s23:\n" \
  "s_call_b64 s[90:91], .Lp_rfa_p16\n" \
  "s_branch .Lp_b23\n" \
  ".Lp_s24:\n" \
  "s_call_b64 s[90:91], .Lp_rfa_p9\n" \
  "s_branch .Lp_b24\n" \
  ".Lp_s25:\n" \
  "s_call_b64 s[90:91], .Lp_rfa_p10\n" \
  "s_branch .Lp_b25\n" \
  ".Lp_s26:\n" \
  "s_call_b64 s[90:91], .Lp_rfa_p11\n" \
  "s_branch .Lp_b26\n" \
  ".Lp_s27:\n" \
  "s_call_b64 s[90:91], .Lp_rfa_p12\n" \
  "s_branch .Lp_b27\n" \
  ".Lp_s28:\n" \
  "s_call_b64 s[90:91], .Lp_rfa_p13\n" \
  "s_branch .Lp_b28\n" \
  ".Lp_s29:\n" \
  "s_call_b64 s[90:91], .Lp_rfa_p14\n" \
  "s_branch .Lp_b29\n" \
  ".Lp_s30:\n" \
  "s_call_b64 s[90:91], .Lp_rfa_p15\n" \
  "s_branch .Lp_b30\n" \
  ".Lp_s31:\n" \
  "s_call_b64 s[90:91], .Lp_rfa_p0\n" \
  "s_branch .Lp_b31\n" \
  ".Lp_s32:\n" \
  "s_call_b64 s[90:91], .Lp_rfa_p1\n" \
  "s_branch .Lp_b32\n" \
  ".Lp_s33:\n" \
  "s_call_b64 s[90:91], .Lp_rfa_p2\n" \
  "s_branch .Lp_b33\n" \
  ".Lp_s34:\n" \
  "s_call_b64 s[90:91], .Lp_rfa_p3\n" \
  "s_branch .Lp_b34\n" \
  ".Lp_s35:\n" \
  "s_call_b64 s[90:91], .Lp_rfa_p4\n" \
  "s_branch .Lp_b35\n" \
  ".Lp_s36:\n" \
  "s_call_b64 s[90:91], .Lp_rfa_p5\n" \
  "s_branch .Lp_b36\n" \
  ".Lp_s37:\n" \
  "s_call_b64 s[90:91], .Lp_rfa_p6\n" \
  "s_branch .Lp_b37\n" \
  ".Lp_s38:\n" \
  "s_call_b64 s[90:91], .Lp_rfa_p7\n" \
  "s_branch .Lp_b38\n" \
  ".Lp_s39:\n" \
  "s_call_b64 s[90:91], .Lp_rfa_p8\n" \
  "s_branch .Lp_b39\n" \
  ".Lp_s40:\n" \
  "s_call_b64 s[90:91], .Lp_rfa_p9\n" \
  "s_branch .Lp_b40\n" \
  ".Lp_s41:\n" \
  "s_call_b64 s[90:91], .Lp_rfa_p10\n" \
  "s_branch .Lp_b41\n" \
  ".Lp_s42:\n" \
  "s_call_b64 s[90:91], .Lp_rfa_p11\n" \
  "s_branch .Lp_b42\n" \
  ".Lp_s43:\n" \
  "s_call_b64 s[90:91], .Lp_rfa_p12\n" \
  "s_branch .Lp_b43\n" \
  ".Lp_s44:\n" \
  "s_call_b64 s[90:91], .Lp_rfa_p13\n" \
  "s_branch .Lp_b44\n" \
  ".Lp_s45:\n" \
  "s_call_b64 s[90:91], .Lp_rfa_p14\n" \
  "s_branch .Lp_b45\n" \
  ".Lp_s46:\n" \
  "s_call_b64 s[90:91], .Lp_rfa_p15\n" \
  "s_branch .Lp_b46\n" \
  ".Lp_s47:\n" \
  "s_call_b64 s[90:91], .Lp_rfa_p0\n" \
  "s_branch .Lp_b47\n" \
  ".Lp_s48:\n" \
  "s_call_b64 s[90:91], .Lp_rfa_p1\n" \
  "s_branch .Lp_b48\n" \
  ".Lp_s49:\n" \
  "s_call_b64 s[90:91], .Lp_rfa_p2\n" \
  "s_branch .Lp_b49\n" \
  ".Lp_s50:\n" \
  "s_call_b64 s[90:91], .Lp_rfa_p3\n" \
  "s_branch .Lp_b50\n" \
  ".Lp_s51:\n" \
  "s_call_b64 s[90:91], .Lp_rfa_p4\n" \
  "s_branch .Lp_b51\n" \
  ".Lp_s52:\n" \
  "s_call_b64 s[90:91], .Lp_rfa_p5\n" \
  "s_branch .Lp_b52\n" \
  ".Lp_s53:\n" \
  "s_call_b64 s[90:91], .Lp_rfa_p6\n" \
  "s_branch .Lp_b53\n" \
  ".Lp_s54:\n" \
  "s_call_b64 s[90:91], .Lp_rfa_p7\n" \
  "s_branch .Lp_b54\n" \
  ".Lp_s55:\n" \
  "s_call_b64 s[90:91], .Lp_rfa_p8\n" \
  "s_branch .Lp_b55\n" \
  ".Lp_s56:\n" \
  "s_call_b64 s[90:91], .Lp_rfa_p9\n" \
  "s_branch .Lp_b56\n" \
  ".Lp_s57:\n" \
  "s_call_b64 s[90:91], .Lp_rfa_p10\n" \
  "s_branch .Lp_b57\n" \
  ".Lp_s58:\n" \
  "s_call_b64 s[90:91], .Lp_rfa_p11\n" \
  "s_branch .Lp_b58\n" \
  ".Lp_s59:\n" \
  "s_call_b64 s[90:91], .Lp_rfa_p12\n" \
  "s_branch .Lp_b59\n" \
  ".Lp_s60:\n" \
  "s_call_b64 s[90:91], .Lp_rfa_p13\n" \
  "s_branch .Lp_b60\n" \
  ".Lp_s61:\n" \
  "s_call_b64 s[90:91], .Lp_rfa_p14\n" \
  "s_branch .Lp_b61\n" \
  ".Lp_s62:\n" \
  "s_call_b64 s[90:91], .Lp_rfa_p15\n" \
  "s_branch .Lp_b62\n" \
  ".Lp_s63:\n" \
  "s_call_b64 s[90:91], .Lp_rfa_p16\n" \
  "s_branch .Lp_b63\n" \
  ".Lp_s64:\n" \
  "s_call_b64 s[90:91], .Lp_rf1_p0\n" \
  "s_branch .Lp_b64\n" \
  ".Lp_s65:\n" \
  "s_call_b64 s[90:91], .Lp_rf1_p1\n" \
  "s_branch .Lp_b65\n" \
  ".Lp_s66:\n" \
  "s_call_b64 s[90:91], .Lp_rf1_p2\n" \
  "s_branch .Lp_b66\n" \
  ".Lp_s67:\n" \
  "s_call_b64 s[90:91], .Lp_rf1_p3\n" \
  "s_branch .Lp_b67\n" \
  ".Lp_s68:\n" \
  "s_call_b64 s[90:91], .Lp_rf1_p4\n" \
  "s_branch .Lp_b68\n" \
  ".Lp_s69:\n" \
  "s_call_b64 s[90:91], .Lp_rf1_p5\n" \
  "s_branch .Lp_b69\n" \
  ".Lp_s70:\n" \
  "s_call_b64 s[90:91], .Lp_rf1_p6\n" \
  "s_branch .Lp_b70\n" \
  ".Lp_s71:\n" \
  "s_call_b64 s[90:91], .Lp_rf1_p7\n" \
  "s_branch .Lp_b71\n" \
  ".Lp_s72:\n" \
  "s_call_b64 s[90:91], .Lp_rf1_p8\n" \
  "s_branch .Lp_b72\n" \
  ".Lp_s73:\n" \
  "s_call_b64 s[90:91], .Lp_rf1_p9\n" \
  "s_branch .Lp_b73\n" \
  ".Lp_s74:\n" \
  "s_call_b64 s[90:91], .Lp_rf1_p10\n" \
  "s_branch .Lp_b74\n" \
  ".Lp_s75:\n" \
  "s_call_b64 s[90:91], .Lp_rf1_p11\n" \
  "s_branch .Lp_b75\n" \
  ".Lp_s76:\n" \
  "s_call_b64 s[90:91], .Lp_rf1_p12\n" \
  "s_branch .Lp_b76\n" \
  ".Lp_s77:\n" \
  "s_call_b64 s[90:91], .Lp_rf1_p13\n" \
  "s_branch .Lp_b77\n" \
  ".Lp_s78:\n" \
  "s_call_b64 s[90:91], .Lp_rf2_p0\n" \
  "s_branch .Lp_b78\n" \
  ".Lp_s79:\n" \
  "s_call_b64 s[90:91], .Lp_rf2_p1\n" \
  "s_branch .Lp_b79\n" \
  ".Lp_s80:\n" \
  "s_call_b64 s[90:91], .Lp_rf2_p2\n" \
  "s_branch .Lp_b80\n" \
  ".Lp_s81:\n" \
  "s_call_b64 s[90:91], .Lp_rf2_p3\n" \
  "s_branch .Lp_b81\n" \
  ".Lp_s82:\n" \
  "s_call_b64 s[90:91], .Lp_rf2_p4\n" \
  "s_branch .Lp_b82\n" \
  ".Lp_s83:\n" \
  "s_call_b64 s[90:91], .Lp_rf2_p5\n" \
  "s_branch .Lp_b83\n" \
  ".Lp_s84:\n" \
  "s_call_b64 s[90:91], .Lp_rf2_p6\n" \
  "s_branch .Lp_b84\n" \
  ".Lp_s85:\n" \
  "s_call_b64 s[90:91], .Lp_rf2_p7\n" \
  "s_branch .Lp_b85\n" \
  ".Lp_s86:\n" \
  "s_call_b64 s[90:91], .Lp_rf2_p8\n" \
  "s_branch .Lp_b86\n" \
  ".Lp_s87:\n" \
  "s_call_b64 s[90:91], .Lp_rf2_p9\n" \
  "s_branch .Lp_b87\n" \
  ".Lp_s88:\n" \
  "s_call_b64 s[90:91], .Lp_rf2_p10\n" \
  "s_branch .Lp_b88\n" \
  ".Lp_s89:\n" \
  "s_call_b64 s[90:91], .Lp_rf2_p11\n" \
  "s_branch .Lp_b89\n" \
  ".Lp_s90:\n" \
  "s_call_b64 s[90:91], .Lp_rf2_p12\n" \
  "s_branch .Lp_b90\n" \
  ".Lp_s91:\n" \
  "s_call_b64 s[90:91], .Lp_rf2_p13\n" \
  "s_branch .Lp_b91\n" \
  ".Lp_s92:\n" \
  "s_call_b64 s[90:91], .Lp_rf1_p0\n" \
  "s_branch .Lp_b92\n" \
  ".Lp_s93:\n" \
  "s_call_b64 s[90:91], .Lp_rf1_p1\n" \
  "s_branch .Lp_b93\n" \
  ".Lp_s94:\n" \
  "s_call_b64 s[90:91], .Lp_rf1_p2\n" \
  "s_branch .Lp_b94\n" \
  ".Lp_s95:\n" \
  "s_call_b64 s[90:91], .Lp_rf1_p3\n" \
  "s_branch .Lp_b95\n" \
  ".Lp_s96:\n" \
  "s_call_b64 s[90:91], .Lp_rf1_p4\n" \
  "s_branch .Lp_b96\n" \
  ".Lp_s97:\n" \
  "s_call_b64 s[90:91], .Lp_rf1_p5\n" \
  "s_branch .Lp_b97\n" \
  ".Lp_s98:\n" \
  "s_call_b64 s[90:91], .Lp_rf1_p6\n" \
  "s_branch .Lp_b98\n" \
  ".Lp_s99:\n" \
  "s_call_b64 s[90:91], .Lp_rf1_p7\n" \
  "s_branch .Lp_b99\n" \
  ".Lp_s100:\n" \
  "s_call_b64 s[90:91], .Lp_rf1_p8\n" \
  "s_branch .Lp_b100\n" \
  ".Lp_s101:\n" \
  "s_call_b64 s[90:91], .Lp_rf1_p9\n" \
  "s_branch .Lp_b101\n" \
  ".Lp_s102:\n" \
  "s_call_b64 s[90:91], .Lp_rf1_p10\n" \
  "s_branch .Lp_b102\n" \
  ".Lp_s103:\n" \
  "s_call_b64 s[90:91], .Lp_rf1_p11\n" \
  "s_branch .Lp_b103\n" \
  ".Lp_s104:\n" \
  "s_call_b64 s[90:91], .Lp_rf1_p12\n" \
  "s_branch .Lp_b104\n" \
  ".Lp_s105:\n" \
  "s_call_b64 s[90:91], .Lp_rf1_p13\n" \
  "s_branch .Lp_b105\n" \
  ".Lp_s106:\n" \
  "s_call_b64 s[90:91], .Lp_rf2_p0\n" \
  "s_branch .Lp_b106\n" \
  ".Lp_s107:\n" \
  "s_call_b64 s[90:91], .Lp_rf2_p1\n" \
  "s_branch .Lp_b107\n" \
  ".Lp_s108:\n" \
  "s_call_b64 s[90:91], .Lp_rf2_p2\n" \
  "s_branch .Lp_b108\n" \
  ".Lp_s109:\n" \
  "s_call_b64 s[90:91], .Lp_rf2_p3\n" \
  "s_branch .Lp_b109\n" \
  ".Lp_s110:\n" \
  "s_call_b64 s[90:91], .Lp_rf2_p4\n" \
  "s_branch .Lp_b110\n" \
  ".Lp_s111:\n" \
  "s_call_b64 s[90:91], .Lp_rf2_p5\n" \
  "s_branch .Lp_b111\n" \
  ".Lp_s112:\n" \
  "s_call_b64 s[90:91], .Lp_rf2_p6\n" \
  "s_branch .Lp_b112\n" \
  ".Lp_s113:\n" \
  "s_call_b64 s[90:91], .Lp_rf2_p7\n" \
  "s_branch .Lp_b113\n" \
  ".Lp_s114:\n" \
  "s_call_b64 s[90:91], .Lp_rf2_p8\n" \
  "s_branch .Lp_b114\n" \
  ".Lp_s115:\n" \
  "s_call_b64 s[90:91], .Lp_rf2_p9\n" \
  "s_branch .Lp_b115\n" \
  ".Lp_s116:\n" \
  "s_call_b64 s[90:91], .Lp_rf2_p10\n" \
  "s_branch .Lp_b116\n" \
  ".Lp_s117:\n" \
  "s_call_b64 s[90:91], .Lp_rf2_p11\n" \
  "s_branch .Lp_b117\n" \
  ".Lp_s118:\n" \
  "s_call_b64 s[90:91], .Lp_rf2_p12\n" \
  "s_branch .Lp_b118\n" \
  ".Lp_s119:\n" \
  "s_call_b64 s[90:91], .Lp_rf2_p13\n" \
  "s_branch .Lp_b119\n" \
  ".Lp_rfa_p0:\n" \
  "s_mov_b32 s96, s94\n" \
  "s_add_u32 s94, s88, 0\n" \
  "s_and_b32 s94, s94, 0x3fff\n" \
  "buffer_load_dwordx4 v[16:19], v255, s[84:87], s94 offen sc1 ; RF\n" \
  "s_add_u32 s94, s88, 1024\n" \
  "s_and_b32 s94, s94, 0x3fff\n" \
  "buffer_load_dwordx4 v[20:23], v255, s[84:87], s94 offen sc1 ; RF\n" \
  "s_mov_b32 s92, 200000\n" \
  "s_getpc_b64 s[98:99]\n" \
  "s_waitcnt vmcnt(0) ; RF\n" \
  "s_mov_b32 s93, 0\n" \
  "s_branch .Lp_rfa_c0\n" \
  ".Lp_rfa_p1:\n" \
  "s_mov_b32 s96, s94\n" \
  "s_add_u32 s94, s88, 1024\n" \
  "s_and_b32 s94, s94, 0x3fff\n" \
  "buffer_load_dwordx4 v[20:23], v255, s[84:87], s94 offen sc1 ; RF\n" \
  "s_mov_b32 s92, 200000\n" \
  "s_getpc_b64 s[98:99]\n" \
  "s_waitcnt vmcnt(0) ; RF\n" \
  "s_mov_b32 s93, 0\n" \
  "s_branch .Lp_rfa_c1\n" \
  ".Lp_rfa_p2:\n" \
  "s_mov_b32 s96, s94\n" \
  "s_add_u32 s94, s88, 2048\n" \
  "s_and_b32 s94, s94, 0x3fff\n" \
  "buffer_load_dwordx4 v[24:27], v255, s[84:87], s94 offen sc1 ; RF\n" \
  "s_add_u32 s94, s88, 3072\n" \
  "s_and_b32 s94, s94, 0x3fff\n" \
  "buffer_load_dwordx4 v[28:31], v255, s[84:87], s94 offen sc1 ; RF\n" \
  "s_mov_b32 s92, 200000\n" \
  "s_getpc_b64 s[98:99]\n" \
  "s_waitcnt vmcnt(0) ; RF\n" \
  "s_mov_b32 s93, 0\n" \
  "s_branch .Lp_rfa_c2\n" \
  ".Lp_rfa_p3:\n" \
  "s_mov_b32 s96, s94\n" \
  "s_add_u32 s94, s88, 3072\n" \
  "s_and_b32 s94, s94, 0x3fff\n" \
  "buffer_load_dwordx4 v[28:31], v255, s[84:87], s94 offen sc1 ; RF\n" \
  "s_mov_b32 s92, 200000\n" \
  "s_getpc_b64 s[98:99]\n" \
  "s_waitcnt vmcnt(0) ; RF\n" \
  "s_mov_b32 s93, 0\n" \
  "s_branch .Lp_rfa_c3\n" \
  ".Lp_rfa_p4:\n" \
  "s_mov_b32 s96, s94\n" \
  "s_add_u32 s94, s88, 4096\n" \
  "s_and_b32 s94, s94, 0x3fff\n" \
  "buffer_load_dwordx4 v[32:35], v255, s[84:87], s94 offen sc1 ; RF\n" \
  "s_add_u32 s94, s88, 5120\n" \
  "s_and_b32 s94, s94, 0x3fff\n" \
  "buffer_load_dwordx4 v[36:39], v255, s[84:87], s94 offen sc1 ; RF\n" \
  "s_mov_b32 s92, 200000\n" \
  "s_getpc_b64 s[98:99]\n" \
  "s_waitcnt vmcnt(0) ; RF\n" \
  "s_mov_b32 s93, 0\n" \
  "s_branch .Lp_rfa_c4\n" \
  ".Lp_rfa_p5:\n" \
  "s_mov_b32 s96, s94\n" \
  "s_add_u32 s94, s88, 5120\n" \
  "s_and_b32 s94, s94, 0x3fff\n" \
  "buffer_load_dwordx4 v[36:39], v255, s[84:87], s94 offen sc1 ; RF\n" \
  "s_mov_b32 s92, 200000\n" \
  "s_getpc_b64 s[98:99]\n" \
  "s_waitcnt vmcnt(0) ; RF\n" \
  "s_mov_b32 s93, 0\n" \
  "s_branch .Lp_rfa_c5\n" \
  ".Lp_rfa_p6:\n" \
  "s_mov_b32 s96, s94\n" \
  "s_add_u32 s94, s88, 6144\n" \
  "s_and_b32 s94, s94, 0x3fff\n" \
  "buffer_load_dwordx4 v[40:43], v255, s[84:87], s94 offen sc1 ; RF\n" \
  "s_add_u32 s94, s88, 7168\n" \
  "s_and_b32 s94, s94, 0x3fff\n" \
  "buffer_load_dwordx4 v[44:47], v255, s[84:87], s94 offen sc1 ; RF\n" \
  "s_mov_b32 s92, 200000\n" \
  "s_getpc_b64 s[98:99]\n" \
  "s_waitcnt vmcnt(0) ; RF\n" \
  "s_mov_b32 s93, 0\n" \
  "s_branch .Lp_rfa_c6\n" \
  ".Lp_rfa_p7:\n" \
  "s_mov_b32 s96, s94\n" \
  "s_add_u32 s94, s88, 7168\n" \
  "s_and_b32 s94, s94, 0x3fff\n" \
  "buffer_load_dwordx4 v[44:47], v255, s[84:87], s94 offen sc1 ; RF\n" \
  "s_mov_b32 s92, 200000\n" \
  "s_getpc_b64 s[98:99]\n" \
  "s_waitcnt vmcnt(0) ; RF\n" \
  "s_mov_b32 s93, 0\n" \
  "s_branch .Lp_rfa_c7\n" \
  ".Lp_rfa_p8:\n" \
  "s_mov_b32 s96, s94\n" \
  "s_add_u32 s94, s88, 8192\n" \
  "s_and_b32 s94, s94, 0x3fff\n" \
  "buffer_load_dwordx4 v[48:51], v255, s[84:87], s94 offen sc1 ; RF\n" \
  "s_add_u32 s94, s88, 9216\n" \
  "s_and_b32 s94, s94, 0x3fff\n" \
  "buffer_load_dwordx4 v[52:55], v255, s[84:87], s94 offen sc1 ; RF\n" \
  "s_mov_b32 s92, 200000\n" \
  "s_getpc_b64 s[98:99]\n" \
  "s_waitcnt vmcnt(0) ; RF\n" \
  "s_mov_b32 s93, 0\n" \
  "s_branch .Lp_rfa_c8\n" \
  ".Lp_rfa_p9:\n" \
  "s_mov_b32 s96, s94\n" \
  "s_add_u32 s94, s88, 9216\n" \
  "s_and_b32 s94, s94, 0x3fff\n" \
  "buffer_load_dwordx4 v[52:55], v255, s[84:87], s94 offen sc1 ; RF\n" \
  "s_mov_b32 s92, 200000\n" \
  "s_getpc_b64 s[98:99]\n" \
  "s_waitcnt vmcnt(0) ; RF\n" \
  "s_mov_b32 s93, 0\n" \
  "s_branch .Lp_rfa_c9\n" \
  ".Lp_rfa_p10:\n" \
  "s_mov_b32 s96, s94\n" \
  "s_add_u32 s94, s88, 10240\n" \
  "s_and_b32 s94, s94, 0x3fff\n" \
  "buffer_load_dwordx4 v[56:59], v255, s[84:87], s94 offen sc1 ; RF\n" \
  "s_add_u32 s94, s88, 11264\n" \
  "s_and_b32 s94, s94, 0x3fff\n" \
  "buffer_load_dwordx4 v[60:63], v255, s[84:87], s94 offen sc1 ; RF\n" \
  "s_mov_b32 s92, 200000\n" \
  "s_getpc_b64 s[98:99]\n" \
  "s_waitcnt vmcnt(0) ; RF\n" \
  "s_mov_b32 s93, 0\n" \
  "s_branch .Lp_rfa_c10\n" \
  ".Lp_rfa_p11:\n" \
  "s_mov_b32 s96, s94\n" \
  "s_add_u32 s94, s88, 11264\n" \
  "s_and_b32 s94, s94, 0x3fff\n" \
  "buffer_load_dwordx4 v[60:63], v255, s[84:87], s94 offen sc1 ; RF\n" \
  "s_mov_b32 s92, 200000\n" \
  "s_getpc_b64 s[98:99]\n" \
  "s_waitcnt vmcnt(0) ; RF\n" \
  "s_mov_b32 s93, 0\n" \
  "s_branch .Lp_rfa_c11\n" \
  ".Lp_rfa_p12:\n" \
  "s_mov_b32 s96, s94\n" \
  "s_add_u32 s94, s88, 12288\n" \
  "s_and_b32 s94, s94, 0x3fff\n" \
  "buffer_load_dwordx4 v[64:67], v255, s[84:87], s94 offen sc1 ; RF\n" \
  "s_add_u32 s94, s88, 13312\n" \
  "s_and_b32 s94, s94, 0x3fff\n" \
  "buffer_load_dwordx4 v[68:71], v255, s[84:87], s94 offen sc1 ; RF\n" \
  "s_mov_b32 s92, 200000\n" \
  "s_getpc_b64 s[98:99]\n" \
  "s_waitcnt vmcnt(0) ; RF\n" \
  "s_mov_b32 s93, 0\n" \
  "s_branch .Lp_rfa_c12\n" \
  ".Lp_rfa_p13:\n" \
  "s_mov_b32 s96, s94\n" \
  "s_add_u32 s94, s88, 13312\n" \
  "s_and_b32 s94, s94, 0x3fff\n" \
  "buffer_load_dwordx4 v[68:71], v255, s[84:87], s94 offen sc1 ; RF\n" \
  "s_mov_b32 s92, 200000\n" \
  "s_getpc_b64 s[98:99]\n" \
  "s_waitcnt vmcnt(0) ; RF\n" \
  "s_mov_b32 s93, 0\n" \
  "s_branch .Lp_rfa_c13\n" \
  ".Lp_rfa_p14:\n" \
  "s_mov_b32 s96, s94\n" \
  "s_add_u32 s94, s88, 14336\n" \
  "s_and_b32 s94, s94, 0x3fff\n" \
  "buffer_load_dwordx4 v[72:75], v255, s[84:87], s94 offen sc1 ; RF\n" \
  "s_add_u32 s94, s88, 15360\n" \
  "s_and_b32 s94, s94, 0x3fff\n" \
  "buffer_load_dwordx4 v[76:79], v255, s[84:87], s94 offen sc1 ; RF\n" \
  "s_mov_b32 s92, 200000\n" \
  "s_getpc_b64 s[98:99]\n" \
  "s_waitcnt vmcnt(0) ; RF\n" \
  "s_mov_b32 s93, 0\n" \
  "s_branch .Lp_rfa_c14\n" \
  ".Lp_rfa_p15:\n" \
  "s_mov_b32 s96, s94\n" \
  "s_add_u32 s94, s88, 15360\n" \
  "s_and_b32 s94, s94, 0x3fff\n" \
  "buffer_load_dwordx4 v[76:79], v255, s[84:87], s94 offen sc1 ; RF\n" \
  "s_mov_b32 s92, 200000\n" \
  "s_getpc_b64 s[98:99]\n" \
  "s_waitcnt vmcnt(0) ; RF\n" \
  "s_mov_b32 s93, 0\n" \
  "s_branch .Lp_rfa_c15\n" \
  ".Lp_rfa_p16:\n" \
  "s_mov_b32 s96, s94\n" \
  "s_add_u32 s94, s89, 0\n" \
  "buffer_load_dwordx4 v[80:83], v248, s[84:87], s94 offen sc1 ; RF\n" \
  "s_mov_b32 s92, 200000\n" \
  "s_getpc_b64 s[98:99]\n" \
  "s_waitcnt vmcnt(0) ; RF\n" \
  "s_mov_b32 s93, 0\n" \
  "s_branch .Lp_rfa_c16\n" \
  ".Lp_rfa_c0:\n" \
  "v_max_u32 v251, v16, v18\n" \
  "v_cmp_eq_u32 vcc, -1, v251\n" \
  "s_cbranch_vccz .Lp_rfa_c1\n" \
  "s_add_u32 s94, s88, 0\n" \
  "s_and_b32 s94, s94, 0x3fff\n" \
  "buffer_load_dwordx4 v[16:19], v255, s[84:87], s94 offen sc1 ; RF\n" \
  "s_mov_b32 s93, 1\n" \
  ".Lp_rfa_c1:\n" \
  "v_max_u32 v251, v20, v22\n" \
  "v_cmp_eq_u32 vcc, -1, v251\n" \
  "s_cbranch_vccz .Lp_rfa_c2\n" \
  "s_add_u32 s94, s88, 1024\n" \
  "s_and_b32 s94, s94, 0x3fff\n" \
  "buffer_load_dwordx4 v[20:23], v255, s[84:87], s94 offen sc1 ; RF\n" \
  "s_mov_b32 s93, 1\n" \
  ".Lp_rfa_c2:\n" \
  "v_max_u32 v251, v24, v26\n" \
  "v_cmp_eq_u32 vcc, -1, v251\n" \
  "s_cbranch_vccz .Lp_rfa_c3\n" \
  "s_add_u32 s94, s88, 2048\n" \
  "s_and_b32 s94, s94, 0x3fff\n" \
  "buffer_load_dwordx4 v[24:27], v255, s[84:87], s94 offen sc1 ; RF\n" \
  "s_mov_b32 s93, 1\n" \
  ".Lp_rfa_c3:\n" \
  "v_max_u32 v251, v28, v30\n" \
  "v_cmp_eq_u32 vcc, -1, v251\n" \
  "s_cbranch_vccz .Lp_rfa_c4\n" \
  "s_add_u32 s94, s88, 3072\n" \
  "s_and_b32 s94, s94, 0x3fff\n" \
  "buffer_load_dwordx4 v[28:31], v255, s[84:87], s94 offen sc1 ; RF\n" \
  "s_mov_b32 s93, 1\n" \
  ".Lp_rfa_c4:\n" \
  "v_max_u32 v251, v32, v34\n" \
  "v_cmp_eq_u32 vcc, -1, v251\n" \
  "s_cbranch_vccz .Lp_rfa_c5\n" \
  "s_add_u32 s94, s88, 4096\n" \
  "s_and_b32 s94, s94, 0x3fff\n" \
  "buffer_load_dwordx4 v[32:35], v255, s[84:87], s94 offen sc1 ; RF\n" \
  "s_mov_b32 s93, 1\n" \
  ".Lp_rfa_c5:\n" \
  "v_max_u32 v251, v36, v38\n" \
  "v_cmp_eq_u32 vcc, -1, v251\n" \
  "s_cbranch_vccz .Lp_rfa_c6\n" \
  "s_add_u32 s94, s88, 5120\n" \
  "s_and_b32 s94, s94, 0x3fff\n" \
  "buffer_load_dwordx4 v[36:39], v255, s[84:87], s94 offen sc1 ; RF\n" \
  "s_mov_b32 s93, 1\n" \
  ".Lp_rfa_c6:\n" \
  "v_max_u32 v251, v40, v42\n" \
  "v_cmp_eq_u32 vcc, -1, v251\n" \
  "s_cbranch_vccz .Lp_rfa_c7\n" \
  "s_add_u32 s94, s88, 6144\n" \
  "s_and_b32 s94, s94, 0x3fff\n" \
  "buffer_load_dwordx4 v[40:43], v255, s[84:87], s94 offen sc1 ; RF\n" \
  "s_mov_b32 s93, 1\n" \
  ".Lp_rfa_c7:\n" \
  "v_max_u32 v251, v44, v46\n" \
  "v_cmp_eq_u32 vcc, -1, v251\n" \
  "s_cbranch_vccz .Lp_rfa_c8\n" \
  "s_add_u32 s94, s88, 7168\n" \
  "s_and_b32 s94, s94, 0x3fff\n" \
  "buffer_load_dwordx4 v[44:47], v255, s[84:87], s94 offen sc1 ; RF\n" \
  "s_mov_b32 s93, 1\n" \
  ".Lp_rfa_c8:\n" \
  "v_max_u32 v251, v48, v50\n" \
  "v_cmp_eq_u32 vcc, -1, v251\n" \
  "s_cbranch_vccz .Lp_rfa_c9\n" \
  "s_add_u32 s94, s88, 8192\n" \
  "s_and_b32 s94, s94, 0x3fff\n" \
  "buffer_load_dwordx4 v[48:51], v255, s[84:87], s94 offen sc1 ; RF\n" \
  "s_mov_b32 s93, 1\n" \
  ".Lp_rfa_c9:\n" \
  "v_max_u32 v251, v52, v54\n" \
  "v_cmp_eq_u32 vcc, -1, v251\n" \
  "s_cbranch_vccz .Lp_rfa_c10\n" \
  "s_add_u32 s94, s88, 9216\n" \
  "s_and_b32 s94, s94, 0x3fff\n" \
  "buffer_load_dwordx4 v[52:55], v255, s[84:87], s94 offen sc1 ; RF\n" \
  "s_mov_b32 s93, 1\n" \
  ".Lp_rfa_c10:\n" \
  "v_max_u32 v251, v56, v58\n" \
  "v_cmp_eq_u32 vcc, -1, v251\n" \
  "s_cbranch_vccz .Lp_rfa_c11\n" \
  "s_add_u32 s94, s88, 10240\n" \
  "s_and_b32 s94, s94, 0x3fff\n" \
  "buffer_load_dwordx4 v[56:59], v255, s[84:87], s94 offen sc1 ; RF\n" \
  "s_mov_b32 s93, 1\n" \
  ".Lp_rfa_c11:\n" \
  "v_max_u32 v251, v60, v62\n" \
  "v_cmp_eq_u32 vcc, -1, v251\n" \
  "s_cbranch_vccz .Lp_rfa_c12\n" \
  "s_add_u32 s94, s88, 11264\n" \
  "s_and_b32 s94, s94, 0x3fff\n" \
  "buffer_load_dwordx4 v[60:63], v255, s[84:87], s94 offen sc1 ; RF\n" \
  "s_mov_b32 s93, 1\n" \
  ".Lp_rfa_c12:\n" \
  "v_max_u32 v251, v64, v66\n" \
  "v_cmp_eq_u32 vcc, -1, v251\n" \
  "s_cbranch_vccz .Lp_rfa_c13\n" \
  "s_add_u32 s94, s88, 12288\n" \
  "s_and_b32 s94, s94, 0x3fff\n" \
  "buffer_load_dwordx4 v[64:67], v255, s[84:87], s94 offen sc1 ; RF\n" \
  "s_mov_b32 s93, 1\n" \
  ".Lp_rfa_c13:\n" \
  "v_max_u32 v251, v68, v70\n" \
  "v_cmp_eq_u32 vcc, -1, v251\n" \
  "s_cbranch_vccz .Lp_rfa_c14\n" \
  "s_add_u32 s94, s88, 13312\n" \
  "s_and_b32 s94, s94, 0x3fff\n" \
  "buffer_load_dwordx4 v[68:71], v255, s[84:87], s94 offen sc1 ; RF\n" \
  "s_mov_b32 s93, 1\n" \
  ".Lp_rfa_c14:\n" \
  "v_max_u32 v251, v72, v74\n" \
  "v_cmp_eq_u32 vcc, -1, v251\n" \
  "s_cbranch_vccz .Lp_rfa_c15\n" \
  "s_add_u32 s94, s88, 14336\n" \
  "s_and_b32 s94, s94, 0x3fff\n" \
  "buffer_load_dwordx4 v[72:75], v255, s[84:87], s94 offen sc1 ; RF\n" \
  "s_mov_b32 s93, 1\n" \
  ".Lp_rfa_c15:\n" \
  "v_max_u32 v251, v76, v78\n" \
  "v_cmp_eq_u32 vcc, -1, v251\n" \
  "s_cbranch_vccz .Lp_rfa_c16\n" \
  "s_add_u32 s94, s88, 15360\n" \
  "s_and_b32 s94, s94, 0x3fff\n" \
  "buffer_load_dwordx4 v[76:79], v255, s[84:87], s94 offen sc1 ; RF\n" \
  "s_mov_b32 s93, 1\n" \
  ".Lp_rfa_c16:\n" \
  "v_max_u32 v251, v80, v82\n" \
  "v_cmp_eq_u32 vcc, -1, v251\n" \
  "s_cbranch_vccz .Lp_rfa_c17\n" \
  "s_add_u32 s94, s89, 0\n" \
  "buffer_load_dwordx4 v[80:83], v248, s[84:87], s94 offen sc1 ; RF\n" \
  "s_mov_b32 s93, 1\n" \
  ".Lp_rfa_c17:\n" \
  "s_cmp_eq_u32 s93, 0\n" \
  "s_cbranch_scc1 .Lp_rfa_x\n" \
  "s_sub_u32 s92, s92, 1\n" \
  "s_cbranch_scc1 .Lp_rfa_y\n" \
  "s_setpc_b64 s[98:99]\n" \
  ".Lp_rfa_y:\n" \
  "s_waitcnt vmcnt(0) ; RF\n" \
  ".Lp_rfa_x:\n" \
  "s_mov_b32 s94, s96\n" \
  "s_setpc_b64 s[90:91]\n" \
  ".Lp_rf1_p0:\n" \
  "s_mov_b32 s96, s94\n" \
  "s_add_u32 s94, s51, 0\n" \
  "buffer_load_dwordx4 v[16:19], v248, s[84:87], s94 offen sc1 ; RF\n" \
  "s_add_u32 s94, s51, 57344\n" \
  "buffer_load_dwordx4 v[20:23], v248, s[84:87], s94 offen sc1 ; RF\n" \
  "s_mov_b32 s92, 200000\n" \
  "s_getpc_b64 s[98:99]\n" \
  "s_waitcnt vmcnt(0) ; RF\n" \
  "s_mov_b32 s93, 0\n" \
  "s_branch .Lp_rf1_c0\n" \
  ".Lp_rf1_p1:\n" \
  "s_mov_b32 s96, s94\n" \
  "s_add_u32 s94, s51, 57344\n" \
  "buffer_load_dwordx4 v[20:23], v248, s[84:87], s94 offen sc1 ; RF\n" \
  "s_mov_b32 s92, 200000\n" \
  "s_getpc_b64 s[98:99]\n" \
  "s_waitcnt vmcnt(0) ; RF\n" \
  "s_mov_b32 s93, 0\n" \
  "s_branch .Lp_rf1_c1\n" \
  ".Lp_rf1_p2:\n" \
  "s_mov_b32 s96, s94\n" \
  "s_add_u32 s94, s51, 8192\n" \
  "buffer_load_dwordx4 v[24:27], v248, s[84:87], s94 offen sc1 ; RF\n" \
  "s_add_u32 s94, s51, 65536\n" \
  "buffer_load_dwordx4 v[28:31], v248, s[84:87], s94 offen sc1 ; RF\n" \
  "s_mov_b32 s92, 200000\n" \
  "s_getpc_b64 s[98:99]\n" \
  "s_waitcnt vmcnt(0) ; RF\n" \
  "s_mov_b32 s93, 0\n" \
  "s_branch .Lp_rf1_c2\n" \
  ".Lp_rf1_p3:\n" \
  "s_mov_b32 s96, s94\n" \
  "s_add_u32 s94, s51, 65536\n" \
  "buffer_load_dwordx4 v[28:31], v248, s[84:87], s94 offen sc1 ; RF\n" \
  "s_mov_b32 s92, 200000\n" \
  "s_getpc_b64 s[98:99]\n" \
  "s_waitcnt vmcnt(0) ; RF\n" \
  "s_mov_b32 s93, 0\n" \
  "s_branch .Lp_rf1_c3\n" \
  ".Lp_rf1_p4:\n" \
  "s_mov_b32 s96, s94\n" \
  "s_add_u32 s94, s51, 16384\n" \
  "buffer_load_dwordx4 v[32:35], v248, s[84:87], s94 offen sc1 ; RF\n" \
  "s_add_u32 s94, s51, 73728\n" \
  "buffer_load_dwordx4 v[52:55], v248, s[84:87], s94 offen sc1 ; RF\n" \
  "s_mov_b32 s92, 200000\n" \
  "s_getpc_b64 s[98:99]\n" \
  "s_waitcnt vmcnt(0) ; RF\n" \
  "s_mov_b32 s93, 0\n" \
  "s_branch .Lp_rf1_c4\n" \
  ".Lp_rf1_p5:\n" \
  "s_mov_b32 s96, s94\n" \
  "s_add_u32 s94, s51, 73728\n" \
  "buffer_load_dwordx4 v[52:55], v248, s[84:87], s94 offen sc1 ; RF\n" \
  "s_mov_b32 s92, 200000\n" \
  "s_getpc_b64 s[98:99]\n" \
  "s_waitcnt vmcnt(0) ; RF\n" \
  "s_mov_b32 s93, 0\n" \
  "s_branch .Lp_rf1_c5\n" \
  ".Lp_rf1_p6:\n" \
  "s_mov_b32 s96, s94\n" \
  "s_add_u32 s94, s51, 24576\n" \
  "buffer_load_dwordx4 v[36:39], v248, s[84:87], s94 offen sc1 ; RF\n" \
  "s_add_u32 s94, s51, 81920\n" \
  "buffer_load_dwordx4 v[56:59], v248, s[84:87], s94 offen sc1 ; RF\n" \
  "s_mov_b32 s92, 200000\n" \
  "s_getpc_b64 s[98:99]\n" \
  "s_waitcnt vmcnt(0) ; RF\n" \
  "s_mov_b32 s93, 0\n" \
  "s_branch .Lp_rf1_c6\n" \
  ".Lp_rf1_p7:\n" \
  "s_mov_b32 s96, s94\n" \
  "s_add_u32 s94, s51, 81920\n" \
  "buffer_load_dwordx4 v[56:59], v248, s[84:87], s94 offen sc1 ; RF\n" \
  "s_mov_b32 s92, 200000\n" \
  "s_getpc_b64 s[98:99]\n" \
  "s_waitcnt vmcnt(0) ; RF\n" \
  "s_mov_b32 s93, 0\n" \
  "s_branch .Lp_rf1_c7\n" \
  ".Lp_rf1_p8:\n" \
  "s_mov_b32 s96, s94\n" \
  "s_add_u32 s94, s51, 32768\n" \
  "buffer_load_dwordx4 v[40:43], v248, s[84:87], s94 offen sc1 ; RF\n" \
  "s_add_u32 s94, s51, 90112\n" \
  "buffer_load_dwordx4 v[60:63], v248, s[84:87], s94 offen sc1 ; RF\n" \
  "s_mov_b32 s92, 200000\n" \
  "s_getpc_b64 s[98:99]\n" \
  "s_waitcnt vmcnt(0) ; RF\n" \
  "s_mov_b32 s93, 0\n" \
  "s_branch .Lp_rf1_c8\n" \
  ".Lp_rf1_p9:\n" \
  "s_mov_b32 s96, s94\n" \
  "s_add_u32 s94, s51, 90112\n" \
  "buffer_load_dwordx4 v[60:63], v248, s[84:87], s94 offen sc1 ; RF\n" \
  "s_mov_b32 s92, 200000\n" \
  "s_getpc_b64 s[98:99]\n" \
  "s_waitcnt vmcnt(0) ; RF\n" \
  "s_mov_b32 s93, 0\n" \
  "s_branch .Lp_rf1_c9\n" \
  ".Lp_rf1_p10:\n" \
  "s_mov_b32 s96, s94\n" \
  "s_add_u32 s94, s51, 40960\n" \
  "buffer_load_dwordx4 v[44:47], v248, s[84:87], s94 offen sc1 ; RF\n" \
  "s_add_u32 s94, s51, 98304\n" \
  "buffer_load_dwordx4 v[64:67], v248, s[84:87], s94 offen sc1 ; RF\n" \
  "s_mov_b32 s92, 200000\n" \
  "s_getpc_b64 s[98:99]\n" \
  "s_waitcnt vmcnt(0) ; RF\n" \
  "s_mov_b32 s93, 0\n" \
  "s_branch .Lp_rf1_c10\n" \
  ".Lp_rf1_p11:\n" \
  "s_mov_b32 s96, s94\n" \
  "s_add_u32 s94, s51, 98304\n" \
  "buffer_load_dwordx4 v[64:67], v248, s[84:87], s94 offen sc1 ; RF\n" \
  "s_mov_b32 s92, 200000\n" \
  "s_getpc_b64 s[98:99]\n" \
  "s_waitcnt vmcnt(0) ; RF\n" \
  "s_mov_b32 s93, 0\n" \
  "s_branch .Lp_rf1_c11\n" \
  ".Lp_rf1_p12:\n" \
  "s_mov_b32 s96, s94\n" \
  "s_add_u32 s94, s51, 49152\n" \
  "buffer_load_dwordx4 v[48:51], v248, s[84:87], s94 offen sc1 ; RF\n" \
  "s_add_u32 s94, s51, 106496\n" \
  "buffer_load_dwordx4 v[68:71], v248, s[84:87], s94 offen sc1 ; RF\n" \
  "s_mov_b32 s92, 200000\n" \
  "s_getpc_b64 s[98:99]\n" \
  "s_waitcnt vmcnt(0) ; RF\n" \
  "s_mov_b32 s93, 0\n" \
  "s_branch .Lp_rf1_c12\n" \
  ".Lp_rf1_p13:\n" \
  "s_mov_b32 s96, s94\n" \
  "s_add_u32 s94, s51, 106496\n" \
  "buffer_load_dwordx4 v[68:71], v248, s[84:87], s94 offen sc1 ; RF\n" \
  "s_mov_b32 s92, 200000\n" \
  "s_getpc_b64 s[98:99]\n" \
  "s_waitcnt vmcnt(0) ; RF\n" \
  "s_mov_b32 s93, 0\n" \
  "s_branch .Lp_rf1_c13\n" \
  ".Lp_rf1_c0:\n" \
  "v_max_u32 v251, v16, v18\n" \
  "v_cmp_eq_u32 vcc, -1, v251\n" \
  "s_cbranch_vccz .Lp_rf1_c1\n" \
  "s_add_u32 s94, s51, 0\n" \
  "buffer_load_dwordx4 v[16:19], v248, s[84:87], s94 offen sc1 ; RF\n" \
  "s_mov_b32 s93, 1\n" \
  ".Lp_rf1_c1:\n" \
  "v_max_u32 v251, v20, v22\n" \
  "v_cmp_eq_u32 vcc, -1, v251\n" \
  "s_cbranch_vccz .Lp_rf1_c2\n" \
  "s_add_u32 s94, s51, 57344\n" \
  "buffer_load_dwordx4 v[20:23], v248, s[84:87], s94 offen sc1 ; RF\n" \
  "s_mov_b32 s93, 1\n" \
  ".Lp_rf1_c2:\n" \
  "v_max_u32 v251, v24, v26\n" \
  "v_cmp_eq_u32 vcc, -1, v251\n" \
  "s_cbranch_vccz .Lp_rf1_c3\n" \
  "s_add_u32 s94, s51, 8192\n" \
  "buffer_load_dwordx4 v[24:27], v248, s[84:87], s94 offen sc1 ; RF\n" \
  "s_mov_b32 s93, 1\n" \
  ".Lp_rf1_c3:\n" \
  "v_max_u32 v251, v28, v30\n" \
  "v_cmp_eq_u32 vcc, -1, v251\n" \
  "s_cbranch_vccz .Lp_rf1_c4\n" \
  "s_add_u32 s94, s51, 65536\n" \
  "buffer_load_dwordx4 v[28:31], v248, s[84:87], s94 offen sc1 ; RF\n" \
  "s_mov_b32 s93, 1\n" \
  ".Lp_rf1_c4:\n" \
  "v_max_u32 v251, v32, v34\n" \
  "v_cmp_eq_u32 vcc, -1, v251\n" \
  "s_cbranch_vccz .Lp_rf1_c5\n" \
  "s_add_u32 s94, s51, 16384\n" \
  "buffer_load_dwordx4 v[32:35], v248, s[84:87], s94 offen sc1 ; RF\n" \
  "s_mov_b32 s93, 1\n" \
  ".Lp_rf1_c5:\n" \
  "v_max_u32 v251, v52, v54\n" \
  "v_cmp_eq_u32 vcc, -1, v251\n" \
  "s_cbranch_vccz .Lp_rf1_c6\n" \
  "s_add_u32 s94, s51, 73728\n" \
  "buffer_load_dwordx4 v[52:55], v248, s[84:87], s94 offen sc1 ; RF\n" \
  "s_mov_b32 s93, 1\n" \
  ".Lp_rf1_c6:\n" \
  "v_max_u32 v251, v36, v38\n" \
  "v_cmp_eq_u32 vcc, -1, v251\n" \
  "s_cbranch_vccz .Lp_rf1_c7\n" \
  "s_add_u32 s94, s51, 24576\n" \
  "buffer_load_dwordx4 v[36:39], v248, s[84:87], s94 offen sc1 ; RF\n" \
  "s_mov_b32 s93, 1\n" \
  ".Lp_rf1_c7:\n" \
  "v_max_u32 v251, v56, v58\n" \
  "v_cmp_eq_u32 vcc, -1, v251\n" \
  "s_cbranch_vccz .Lp_rf1_c8\n" \
  "s_add_u32 s94, s51, 81920\n" \
  "buffer_load_dwordx4 v[56:59], v248, s[84:87], s94 offen sc1 ; RF\n" \
  "s_mov_b32 s93, 1\n" \
  ".Lp_rf1_c8:\n" \
  "v_max_u32 v251, v40, v42\n" \
  "v_cmp_eq_u32 vcc, -1, v251\n" \
  "s_cbranch_vccz .Lp_rf1_c9\n" \
  "s_add_u32 s94, s51, 32768\n" \
  "buffer_load_dwordx4 v[40:43], v248, s[84:87], s94 offen sc1 ; RF\n" \
  "s_mov_b32 s93, 1\n" \
  ".Lp_rf1_c9:\n" \
  "v_max_u32 v251, v60, v62\n" \
  "v_cmp_eq_u32 vcc, -1, v251\n" \
  "s_cbranch_vccz .Lp_rf1_c10\n" \
  "s_add_u32 s94, s51, 90112\n" \
  "buffer_load_dwordx4 v[60:63], v248, s[84:87], s94 offen sc1 ; RF\n" \
  "s_mov_b32 s93, 1\n" \
  ".Lp_rf1_c10:\n" \
  "v_max_u32 v251, v44, v46\n" \
  "v_cmp_eq_u32 vcc, -1, v251\n" \
  "s_cbranch_vccz .Lp_rf1_c11\n" \
  "s_add_u32 s94, s51, 40960\n" \
  "buffer_load_dwordx4 v[44:47], v248, s[84:87], s94 offen sc1 ; RF\n" \
  "s_mov_b32 s93, 1\n" \
  ".Lp_rf1_c11:\n" \
  "v_max_u32 v251, v64, v66\n" \
  "v_cmp_eq_u32 vcc, -1, v251\n" \
  "s_cbranch_vccz .Lp_rf1_c12\n" \
  "s_add_u32 s94, s51, 98304\n" \
  "buffer_load_dwordx4 v[64:67], v248, s[84:87], s94 offen sc1 ; RF\n" \
  "s_mov_b32 s93, 1\n" \
  ".Lp_rf1_c12:\n" \
  "v_max_u32 v251, v48, v50\n" \
  "v_cmp_eq_u32 vcc, -1, v251\n" \
  "s_cbranch_vccz .Lp_rf1_c13\n" \
  "s_add_u32 s94, s51, 49152\n" \
  "buffer_load_dwordx4 v[48:51], v248, s[84:87], s94 offen sc1 ; RF\n" \
  "s_mov_b32 s93, 1\n" \
  ".Lp_rf1_c13:\n" \
  "v_max_u32 v251, v68, v70\n" \
  "v_cmp_eq_u32 vcc, -1, v251\n" \
  "s_cbranch_vccz .Lp_rf1_c14\n" \
  "s_add_u32 s94, s51, 106496\n" \
  "buffer_load_dwordx4 v[68:71], v248, s[84:87], s94 offen sc1 ; RF\n" \
  "s_mov_b32 s93, 1\n" \
  ".Lp_rf1_c14:\n" \
  "s_cmp_eq_u32 s93, 0\n" \
  "s_cbranch_scc1 .Lp_rf1_x\n" \
  "s_sub_u32 s92, s92, 1\n" \
  "s_cbranch_scc1 .Lp_rf1_y\n" \
  "s_setpc_b64 s[98:99]\n" \
  ".Lp_rf1_y:\n" \
  "s_waitcnt vmcnt(0) ; RF\n" \
  ".Lp_rf1_x:\n" \
  "s_mov_b32 s94, s96\n" \
  "s_setpc_b64 s[90:91]\n" \
  ".Lp_rf2_p0:\n" \
  "s_mov_b32 s96, s94\n" \
  "s_add_u32 s94, s51, 114688\n" \
  "buffer_load_dwordx4 v[72:75], v248, s[84:87], s94 offen sc1 ; RF\n" \
  "s_add_u32 s94, s51, 172032\n" \
  "buffer_load_dwordx4 v[232:235], v248, s[84:87], s94 offen sc1 ; RF\n" \
  "s_mov_b32 s92, 200000\n" \
  "s_getpc_b64 s[98:99]\n" \
  "s_waitcnt vmcnt(0) ; RF\n" \
  "s_mov_b32 s93, 0\n" \
  "s_branch .Lp_rf2_c0\n" \
  ".Lp_rf2_p1:\n" \
  "s_mov_b32 s96, s94\n" \
  "s_add_u32 s94, s51, 172032\n" \
  "buffer_load_dwordx4 v[232:235], v248, s[84:87], s94 offen sc1 ; RF\n" \
  "s_mov_b32 s92, 200000\n" \
  "s_getpc_b64 s[98:99]\n" \
  "s_waitcnt vmcnt(0) ; RF\n" \
  "s_mov_b32 s93, 0\n" \
  "s_branch .Lp_rf2_c1\n" \
  ".Lp_rf2_p2:\n" \
  "s_mov_b32 s96, s94\n" \
  "s_add_u32 s94, s51, 122880\n" \
  "buffer_load_dwordx4 v[76:79], v248, s[84:87], s94 offen sc1 ; RF\n" \
  "s_add_u32 s94, s51, 180224\n" \
  "buffer_load_dwordx4 v[236:239], v248, s[84:87], s94 offen sc1 ; RF\n" \
  "s_mov_b32 s92, 200000\n" \
  "s_getpc_b64 s[98:99]\n" \
  "s_waitcnt vmcnt(0) ; RF\n" \
  "s_mov_b32 s93, 0\n" \
  "s_branch .Lp_rf2_c2\n" \
  ".Lp_rf2_p3:\n" \
  "s_mov_b32 s96, s94\n" \
  "s_add_u32 s94, s51, 180224\n" \
  "buffer_load_dwordx4 v[236:239], v248, s[84:87], s94 offen sc1 ; RF\n" \
  "s_mov_b32 s92, 200000\n" \
  "s_getpc_b64 s[98:99]\n" \
  "s_waitcnt vmcnt(0) ; RF\n" \
  "s_mov_b32 s93, 0\n" \
  "s_branch .Lp_rf2_c3\n" \
  ".Lp_rf2_p4:\n" \
  "s_mov_b32 s96, s94\n" \
  "s_add_u32 s94, s51, 131072\n" \
  "buffer_load_dwordx4 v[0:3], v248, s[84:87], s94 offen sc1 ; RF\n" \
  "s_add_u32 s94, s51, 188416\n" \
  "buffer_load_dwordx4 v[80:83], v248, s[84:87], s94 offen sc1 ; RF\n" \
  "s_mov_b32 s92, 200000\n" \
  "s_getpc_b64 s[98:99]\n" \
  "s_waitcnt vmcnt(0) ; RF\n" \
  "s_mov_b32 s93, 0\n" \
  "s_branch .Lp_rf2_c4\n" \
  ".Lp_rf2_p5:\n" \
  "s_mov_b32 s96, s94\n" \
  "s_add_u32 s94, s51, 188416\n" \
  "buffer_load_dwordx4 v[80:83], v248, s[84:87], s94 offen sc1 ; RF\n" \
  "s_mov_b32 s92, 200000\n" \
  "s_getpc_b64 s[98:99]\n" \
  "s_waitcnt vmcnt(0) ; RF\n" \
  "s_mov_b32 s93, 0\n" \
  "s_branch .Lp_rf2_c5\n" \
  ".Lp_rf2_p6:\n" \
  "s_mov_b32 s96, s94\n" \
  "s_add_u32 s94, s51, 139264\n" \
  "buffer_load_dwordx4 v[4:7], v248, s[84:87], s94 offen sc1 ; RF\n" \
  "s_add_u32 s94, s51, 196608\n" \
  "buffer_load_dwordx4 v[16:19], v248, s[84:87], s94 offen sc1 ; RF\n" \
  "s_mov_b32 s92, 200000\n" \
  "s_getpc_b64 s[98:99]\n" \
  "s_waitcnt vmcnt(0) ; RF\n" \
  "s_mov_b32 s93, 0\n" \
  "s_branch .Lp_rf2_c6\n" \
  ".Lp_rf2_p7:\n" \
  "s_mov_b32 s96, s94\n" \
  "s_add_u32 s94, s51, 196608\n" \
  "buffer_load_dwordx4 v[16:19], v248, s[84:87], s94 offen sc1 ; RF\n" \
  "s_mov_b32 s92, 200000\n" \
  "s_getpc_b64 s[98:99]\n" \
  "s_waitcnt vmcnt(0) ; RF\n" \
  "s_mov_b32 s93, 0\n" \
  "s_branch .Lp_rf2_c7\n" \
  ".Lp_rf2_p8:\n" \
  "s_mov_b32 s96, s94\n" \
  "s_add_u32 s94, s51, 147456\n" \
  "buffer_load_dwordx4 v[8:11], v248, s[84:87], s94 offen sc1 ; RF\n" \
  "s_add_u32 s94, s51, 204800\n" \
  "buffer_load_dwordx4 v[24:27], v248, s[84:87], s94 offen sc1 ; RF\n" \
  "s_mov_b32 s92, 200000\n" \
  "s_getpc_b64 s[98:99]\n" \
  "s_waitcnt vmcnt(0) ; RF\n" \
  "s_mov_b32 s93, 0\n" \
  "s_branch .Lp_rf2_c8\n" \
  ".Lp_rf2_p9:\n" \
  "s_mov_b32 s96, s94\n" \
  "s_add_u32 s94, s51, 204800\n" \
  "buffer_load_dwordx4 v[24:27], v248, s[84:87], s94 offen sc1 ; RF\n" \
  "s_mov_b32 s92, 200000\n" \
  "s_getpc_b64 s[98:99]\n" \
  "s_waitcnt vmcnt(0) ; RF\n" \
  "s_mov_b32 s93, 0\n" \
  "s_branch .Lp_rf2_c9\n" \
  ".Lp_rf2_p10:\n" \
  "s_mov_b32 s96, s94\n" \
  "s_add_u32 s94, s51, 155648\n" \
  "buffer_load_dwordx4 v[12:15], v248, s[84:87], s94 offen sc1 ; RF\n" \
  "s_add_u32 s94, s51, 212992\n" \
  "buffer_load_dwordx4 v[32:35], v248, s[84:87], s94 offen sc1 ; RF\n" \
  "s_mov_b32 s92, 200000\n" \
  "s_getpc_b64 s[98:99]\n" \
  "s_waitcnt vmcnt(0) ; RF\n" \
  "s_mov_b32 s93, 0\n" \
  "s_branch .Lp_rf2_c10\n" \
  ".Lp_rf2_p11:\n" \
  "s_mov_b32 s96, s94\n" \
  "s_add_u32 s94, s51, 212992\n" \
  "buffer_load_dwordx4 v[32:35], v248, s[84:87], s94 offen sc1 ; RF\n" \
  "s_mov_b32 s92, 200000\n" \
  "s_getpc_b64 s[98:99]\n" \
  "s_waitcnt vmcnt(0) ; RF\n" \
  "s_mov_b32 s93, 0\n" \
  "s_branch .Lp_rf2_c11\n" \
  ".Lp_rf2_p12:\n" \
  "s_mov_b32 s96, s94\n" \
  "s_add_u32 s94, s51, 163840\n" \
  "buffer_load_dwordx4 v[228:231], v248, s[84:87], s94 offen sc1 ; RF\n" \
  "s_add_u32 s94, s51, 221184\n" \
  "buffer_load_dwordx4 v[36:39], v248, s[84:87], s94 offen sc1 ; RF\n" \
  "s_mov_b32 s92, 200000\n" \
  "s_getpc_b64 s[98:99]\n" \
  "s_waitcnt vmcnt(0) ; RF\n" \
  "s_mov_b32 s93, 0\n" \
  "s_branch .Lp_rf2_c12\n" \
  ".Lp_rf2_p13:\n" \
  "s_mov_b32 s96, s94\n" \
  "s_add_u32 s94, s51, 221184\n" \
  "buffer_load_dwordx4 v[36:39], v248, s[84:87], s94 offen sc1 ; RF\n" \
  "s_mov_b32 s92, 200000\n" \
  "s_getpc_b64 s[98:99]\n" \
  "s_waitcnt vmcnt(0) ; RF\n" \
  "s_mov_b32 s93, 0\n" \
  "s_branch .Lp_rf2_c13\n" \
  ".Lp_rf2_c0:\n" \
  "v_max_u32 v251, v72, v74\n" \
  "v_cmp_eq_u32 vcc, -1, v251\n" \
  "s_cbranch_vccz .Lp_rf2_c1\n" \
  "s_add_u32 s94, s51, 114688\n" \
  "buffer_load_dwordx4 v[72:75], v248, s[84:87], s94 offen sc1 ; RF\n" \
  "s_mov_b32 s93, 1\n" \
  ".Lp_rf2_c1:\n" \
  "v_max_u32 v251, v232, v234\n" \
  "v_cmp_eq_u32 vcc, -1, v251\n" \
  "s_cbranch_vccz .Lp_rf2_c2\n" \
  "s_add_u32 s94, s51, 172032\n" \
  "buffer_load_dwordx4 v[232:235], v248, s[84:87], s94 offen sc1 ; RF\n" \
  "s_mov_b32 s93, 1\n" \
  ".Lp_rf2_c2:\n" \
  "v_max_u32 v251, v76, v78\n" \
  "v_cmp_eq_u32 vcc, -1, v251\n" \
  "s_cbranch_vccz .Lp_rf2_c3\n" \
  "s_add_u32 s94, s51, 122880\n" \
  "buffer_load_dwordx4 v[76:79], v248, s[84:87], s94 offen sc1 ; RF\n" \
  "s_mov_b32 s93, 1\n" \
  ".Lp_rf2_c3:\n" \
  "v_max_u32 v251, v236, v238\n" \
  "v_cmp_eq_u32 vcc, -1, v251\n" \
  "s_cbranch_vccz .Lp_rf2_c4\n" \
  "s_add_u32 s94, s51, 180224\n" \
  "buffer_load_dwordx4 v[236:239], v248, s[84:87], s94 offen sc1 ; RF\n" \
  "s_mov_b32 s93, 1\n" \
  ".Lp_rf2_c4:\n" \
  "v_max_u32 v251, v0, v2\n" \
  "v_cmp_eq_u32 vcc, -1, v251\n" \
  "s_cbranch_vccz .Lp_rf2_c5\n" \
  "s_add_u32 s94, s51, 131072\n" \
  "buffer_load_dwordx4 v[0:3], v248, s[84:87], s94 offen sc1 ; RF\n" \
  "s_mov_b32 s93, 1\n" \
  ".Lp_rf2_c5:\n" \
  "v_max_u32 v251, v80, v82\n" \
  "v_cmp_eq_u32 vcc, -1, v251\n" \
  "s_cbranch_vccz .Lp_rf2_c6\n" \
  "s_add_u32 s94, s51, 188416\n" \
  "buffer_load_dwordx4 v[80:83], v248, s[84:87], s94 offen sc1 ; RF\n" \
  "s_mov_b32 s93, 1\n" \
  ".Lp_rf2_c6:\n" \
  "v_max_u32 v251, v4, v6\n" \
  "v_cmp_eq_u32 vcc, -1, v251\n" \
  "s_cbranch_vccz .Lp_rf2_c7\n" \
  "s_add_u32 s94, s51, 139264\n" \
  "buffer_load_dwordx4 v[4:7], v248, s[84:87], s94 offen sc1 ; RF\n" \
  "s_mov_b32 s93, 1\n" \
  ".Lp_rf2_c7:\n" \
  "v_max_u32 v251, v16, v18\n" \
  "v_cmp_eq_u32 vcc, -1, v251\n" \
  "s_cbranch_vccz .Lp_rf2_c8\n" \
  "s_add_u32 s94, s51, 196608\n" \
  "buffer_load_dwordx4 v[16:19], v248, s[84:87], s94 offen sc1 ; RF\n" \
  "s_mov_b32 s93, 1\n" \
  ".Lp_rf2_c8:\n" \
  "v_max_u32 v251, v8, v10\n" \
  "v_cmp_eq_u32 vcc, -1, v251\n" \
  "s_cbranch_vccz .Lp_rf2_c9\n" \
  "s_add_u32 s94, s51, 147456\n" \
  "buffer_load_dwordx4 v[8:11], v248, s[84:87], s94 offen sc1 ; RF\n" \
  "s_mov_b32 s93, 1\n" \
  ".Lp_rf2_c9:\n" \
  "v_max_u32 v251, v24, v26\n" \
  "v_cmp_eq_u32 vcc, -1, v251\n" \
  "s_cbranch_vccz .Lp_rf2_c10\n" \
  "s_add_u32 s94, s51, 204800\n" \
  "buffer_load_dwordx4 v[24:27], v248, s[84:87], s94 offen sc1 ; RF\n" \
  "s_mov_b32 s93, 1\n" \
  ".Lp_rf2_c10:\n" \
  "v_max_u32 v251, v12, v14\n" \
  "v_cmp_eq_u32 vcc, -1, v251\n" \
  "s_cbranch_vccz .Lp_rf2_c11\n" \
  "s_add_u32 s94, s51, 155648\n" \
  "buffer_load_dwordx4 v[12:15], v248, s[84:87], s94 offen sc1 ; RF\n" \
  "s_mov_b32 s93, 1\n" \
  ".Lp_rf2_c11:\n" \
  "v_max_u32 v251, v32, v34\n" \
  "v_cmp_eq_u32 vcc, -1, v251\n" \
  "s_cbranch_vccz .Lp_rf2_c12\n" \
  "s_add_u32 s94, s51, 212992\n" \
  "buffer_load_dwordx4 v[32:35], v248, s[84:87], s94 offen sc1 ; RF\n" \
  "s_mov_b32 s93, 1\n" \
  ".Lp_rf2_c12:\n" \
  "v_max_u32 v251, v228, v230\n" \
  "v_cmp_eq_u32 vcc, -1, v251\n" \
  "s_cbranch_vccz .Lp_rf2_c13\n" \
  "s_add_u32 s94, s51, 163840\n" \
  "buffer_load_dwordx4 v[228:231], v248, s[84:87], s94 offen sc1 ; RF\n" \
  "s_mov_b32 s93, 1\n" \
  ".Lp_rf2_c13:\n" \
  "v_max_u32 v251, v36, v38\n" \
  "v_cmp_eq_u32 vcc, -1, v251\n" \
  "s_cbranch_vccz .Lp_rf2_c14\n" \
  "s_add_u32 s94, s51, 221184\n" \
  "buffer_load_dwordx4 v[36:39], v248, s[84:87], s94 offen sc1 ; RF\n" \
  "s_mov_b32 s93, 1\n" \
  ".Lp_rf2_c14:\n" \
  "s_cmp_eq_u32 s93, 0\n" \
  "s_cbranch_scc1 .Lp_rf2_x\n" \
  "s_sub_u32 s92, s92, 1\n" \
  "s_cbranch_scc1 .Lp_rf2_y\n" \
  "s_setpc_b64 s[98:99]\n" \
  ".Lp_rf2_y:\n" \
  "s_waitcnt vmcnt(0) ; RF\n" \
  ".Lp_rf2_x:\n" \
  "s_mov_b32 s94, s96\n" \
  "s_setpc_b64 s[90:91]\n" \
  ".Lfb_last:\n" \
  ".Lp_end:\n"
#define POST_CLOB "v0", "v1", "v2", "v3", "v4", "v5", "v6", "v7", "v8", "v9", "v10", "v11", "v12", "v13", "v14", "v15", "v16", "v17", "v18", "v19", "v20", "v21", "v22", "v23", "v24", "v25", "v26", "v27", "v28", "v29", "v30", "v31", "v32", "v33", "v34", "v35", "v36", "v37", "v38", "v39", "v40", "v41", "v42", "v43", "v44", "v45", "v46", "v47", "v48", "v49", "v50", "v51", "v52", "v53", "v54", "v55", "v56", "v57", "v58", "v59", "v60", "v61", "v62", "v63", "v64", "v65", "v66", "v67", "v68", "v69", "v70", "v71", "v72", "v73", "v74", "v75", "v76", "v77", "v78", "v79", "v80", "v82", "v83", "v84", "v85", "v86", "v87", "v88", "v89", "v90", "v91", "v92", "v93", "v94", "v95", "v96", "v97", "v98", "v99", "v100", "v101", "v102", "v103", "v104", "v105", "v106", "v107", "v108", "v109", "v110", "v111", "v112", "v113", "v114", "v115", "v116", "v117", "v118", "v119", "v120", "v121", "v122", "v123", "v124", "v125", "v126", "v127", "v128", "v129", "v130", "v131", "v132", "v133", "v134", "v135", "v136", "v137", "v138", "v139", "v140", "v141", "v142", "v143", "v144", "v145", "v146", "v147", "v148", "v149", "v150", "v151", "v152", "v153", "v154", "v155", "v156", "v157", "v158", "v159", "v196", "v197", "v198", "v199", "v200", "v201", "v202", "v203", "v204", "v205", "v206", "v207", "v208", "v209", "v210", "v211", "v212", "v213", "v214", "v215", "v216", "v217", "v218", "v219", "v220", "v221", "v222", "v223", "v224", "v225", "v226", "v227", "v228", "v229", "v230", "v231", "v232", "v233", "v234", "v235", "v236", "v237", "v238", "v239", "v240", "v241", "v242", "v243", "v244", "v245", "v246", "v247", "v249", "v250", "v251", "v254", "v255", "s60", "s61", "s62", "s63", "s64", "s65", "s66", "s67", "s70", "s71", "s72", "s73", "s74", "s75", "s76", "s77", "s88", "s89", "s90", "s91", "s92", "s93", "s94", "s95", "s96", "s97", "s98", "s99"
#define DPJ 8
constexpr int h_iss(int j) { return DEP + j + (j > DPJ) < 11 ? DEP + j + (j > DPJ) : 11; }
constexpr int wq_lo(int t) { return WQ == 1 ? (t <= 9 ? 0 : t == 10 ? 4 : 8) : WQ == 2 ? (t <= 8 ? 0 : t == 9 ? 2 : t == 10 ? 4 : 8) : (t <= 8 ? 0 : t == 9 ? 4 : 8); }
constexpr int h_ops(int t) { return WQ && t > 8 ? 3 * NC + 6 * (8 - NC) + 2 * wq_lo(t) : t <= NC ? 3 * t : t <= 10 ? 3 * NC + 6 * (t - NC) : 3 * NC + 6 * (10 - NC) + 4; }
static_assert(DEP >= 2 && DEP <= 5 && NC <= DEP, "slot re-use");
#define W_CLOB "v32", "v33", "v34", "v35", "v36", "v37", "v38", "v39", "v40", "v41", "v42", "v43", "v44", "v45", "v46", "v47", "v48", "v49", "v50", "v51", "v52", "v53", "v54", "v55", "v56", "v57", "v58", "v59", "v60", "v61", "v62", "v63", "s98", "s99"

#define QE_CLOB "v0", "v1", "v2", "v3", "v4", "v5", "v6", "v7", "v8", "v9", "v10", "v11", "v12", "v13", "v14", "v15", "v64", "v65", "v66", "v67", "v68", "v69", "v70", "v71", "v72", "v73", "v74", "v75", "v76", "v77", "v78", "v79", "v80", "v81", "v82", "v83", "v84", "v85", "v86", "v87", "v88", "v89", "v90", "v91", "v92", "v93", "v94", "v95", "v96", "v97", "v98", "v99", "v100", "v101", "v102", "v103", "v104", "v105", "v106", "v107", "v108", "v109", "v110", "v111", "v112", "v113", "v114", "v115", "v116", "v117", "v118", "v119", "v120", "v121", "v122", "v123", "v124", "v125", "v126", "v127", "v204", "v205", "v206", "v207", "v208", "v209", "v210"

#define KVE_CLOB "v0", "v1", "v2", "v3", "v4", "v5", "v6", "v7", "v8", "v9", "v10", "v11", "v12", "v13", "v14", "v15", "v16", "v17", "v18", "v19", "v20", "v21", "v22", "v23", "v24", "v25", "v26", "v27", "v28", "v29", "v30", "v31", "v192", "v193", "v194", "v195", "v196", "v197", "v198", "v199", "v200", "v201", "v202", "v203", "v204", "v205", "v206", "v207", "v208", "v209", "v210", "v211", "s93", "s94", "s95", "s96", "s97"

struct Unit { u32 m, off, dst; };
DI Unit unit(u32 U, u32 ln) {
  const u32 g = U & 3;
  u32 src;
  u32 rlo, rhi, pitch = 1024, fstr = 1024, col, dst, ex = 0;
  if (U < 1536) {
    const u32 m = U >> 9, kb = (U >> 2) & 15, cs = (U >> 6) & 7;
    rlo = m == 0 ? 32 * kb + 16 * (g >> 1) + 4 * (g & 1) : 64 * (kb >> 1) + 16 * g + 8 * (kb & 1);
    rhi = rlo + (m == 0 ? 8 : 4);
    src = m;
    col = cs * 128;
    dst = WS_WOT + m * (512u << 10) + (cs * 16 + ((kb - 2 * cs - (m == 2 ? 2 * ROT3 : 0)) & 15)) * 4096;
  } else if (U < 1536 + 1792) {
    const u32 t = (U - 1536) >> 2, kb = t & 15, sl = t >> 4;
    rlo = 64 * (kb >> 1) + 16 * g + 8 * (kb & 1);
    rhi = rlo + 4;
    src = 3; pitch = 3584; fstr = 16384;
    col = sl * 128;
    dst = WS_W3T + 64 * sl * 1024;
    ex = ((kb - (((4 * sl + (ln >> 4)) / 7) & 14)) & 15) * 1024;
  } else {
    const u32 t = (U - 1536 - 1792) >> 2, cs = t & 7, kb = t >> 3;
    const bool sg = kb >= 48;
    const u32 Tt = sg ? 7 * (2 * (kb - 48) + (g >> 1)) + 6 : 7 * (kb & 15) + 2 * (kb >> 4);
    const u32 T1 = sg ? Tt : Tt + 1, g0 = sg ? 2 * (g & 1) : g, g1 = sg ? g0 + 1 : g;
    rlo = 64 * (Tt >> 2) + 16 * g0 + 4 * (Tt & 3);
    rhi = 64 * (T1 >> 2) + 16 * g1 + 4 * (T1 & 3);
    src = 4;
    col = cs * 128;
    dst = WS_W4T + (cs * 56 + kb) * 4096;
  }
  return Unit{src, ((ln >> 5 ? rhi : rlo) + ((ln >> 3) & 3)) * pitch + col + (ln & 7) * 16, dst + g * 256 + (ln >> 4) * fstr + (ln & 15) * 16 + ex};
}

__global__ void __launch_bounds__(512, 2)
fused_block(const char* __restrict__ px, const char* __restrict__ pwqkv, const char* __restrict__ pwo, const char* __restrict__ pw1,
            const char* __restrict__ pw2, const char* __restrict__ pw3, const char* __restrict__ pw4, char* __restrict__ pout, char* __restrict__ pws) {
  __shared__ __attribute__((aligned(16))) char lds_[163840];
  LAS char* lds = (LAS char*)lds_;
  asm volatile(".Lfb_first:" ::"s"(lds) : "memory");
  const u32 lane = __builtin_amdgcn_mbcnt_hi(~0u, __builtin_amdgcn_mbcnt_lo(~0u, 0u));
  const u32 w = rfl(threadIdx.x >> 6);
  const u32 b = blockIdx.x, xcd = b & 7, jj = b >> 3;
  const u32 rb = 8 * (xcd >> 1) + (jj >> 2), hq = 4 * (xcd & 1) + ((jj + HQR) & 3);
  STAMP(0);
  STAMPR(62);
  const srd_t sws = __builtin_amdgcn_make_buffer_rsrc(pws, 0, WS_END, 0x00020000);
  const u64 ep = *(const __attribute__((address_space(4))) u64*)(pws + WS_EPOCH);
  const u32 h = lane >> 5, r31 = lane & 31;
  const u32 rot = ROT == 0 ? 0 : ROT == 1 ? (rb + 2 * hq) & 7 : ROT == 2 ? ((rb + hq) & 1) * 4 : ROT == 4 ? (hq == 3 ? rb & 7 : ((rb + hq) & 1) * 4) : ROT == 5 ? (hq == 3 ? (rb & 3) * 2 : ((rb + hq) & 1) * 4) : ((rb + hq) & 3) * 2;
  u64 tag;
  u32x4 td4, to4;
  const char* tsrc[4];
  const u32 kh = w & 1, hl = w >> 1, hd = 4 * hq + hl;
  u32 kvoff = WS_K + lane * 16, vvoff = WS_V + h * 256 + (lane & 15) * 16;
  const u64 vmask = 0x0000FFFF0000FFFFull;
  const u32 hbase = hd * 65536;
  const u32 wv0 = (lane >> 3) * 3072 + (((lane & 7) ^ ((lane >> 2) & 4)) * 16);
  const u32 xv0 = (lane >> 3) * 1024 + (((lane & 7) ^ (lane >> 4)) * 16);
  const u32 kcol = 1024 + ((hq ^ KXR) - hq) * 128, vcol = 2048 + ((hq ^ VXR) - hq) * 128;
  u32 xo0 = (64 * rb + 8 * w) * 1024 + rot * 128, ko0 = 8 * w * 3072 + hq * 128 + rot * (64 * 3072) + kcol, vo0 = ko0 - kcol + vcol;
  u32 lx0 = w * 1024, lk0 = l_k(0) + w * 1024, lv0 = l_k(0) + 8192 + w * 1024, xvf = xv0 ^ (64 * (w & 1)), wvf = wv0;
  asm volatile("" : "+s"(xo0), "+s"(ko0), "+s"(vo0), "+s"(lx0), "+s"(lk0), "+s"(lv0));
  asm volatile("" : "+v"(xvf), "+v"(wvf));
  u64 ax = (u64)px, aw = (u64)pwqkv;
  asm volatile("" : "+s"(ax), "+s"(aw) : "s"(pwo), "s"(pw1), "s"(pw2), "s"(pw3), "s"(pw4), "s"(pout), "s"(pws));
  const srd_t sx = __builtin_amdgcn_make_buffer_rsrc((void*)ax, 0, 2048u * 1024u, 0x00020000);
  const srd_t sw = __builtin_amdgcn_make_buffer_rsrc((void*)aw, 0, 512u * 3072u, 0x00020000);
  static_assert(NC == 1, "");
  dma16(sx, xvf, xo0, lx0);
  dma16(sw, wvf, ko0, lk0);
  dma16(sw, wvf, vo0, lv0);
  STAMP(1);

  const u32 rt = w & 1, ch = (w >> 1) & 1;
#define LANEC \
    const u32 one2 = (((lane >> 2) ^ (lane >> 4)) & 1) ? 0u : 0x3F803F80u; \
    const u32x4 sel4 = {one2, one2, one2, one2}; \
    const u32 xl = ((32 * rt + r31) * 128 + ((h ^ ((r31 >> 1) & 7)) * 16)) ^ (HX ? 32 * ch + 64 * rt : 0); \
    const u32 wl = (8 * h + ((lane >> 2) & 3)) * 128 + ((ch ^ ((lane >> 3) & 1)) * 64) + ((lane >> 4) & 1) * 32 + (lane & 3) * 8
  if (w < 4) {
    u32 touch = 0;
    u64 tp;
    if (w == 0 && !TOP) {
      u64 pc;
      u32 len;
      u32 off;
      asm volatile("s_getpc_b64 %0\n.Lfb_here:\n\ts_mov_b32 %1, .Lfb_last-.Lfb_first\n\ts_mov_b32 %2, .Lfb_here-.Lfb_first" : "=s"(pc), "=s"(len), "=s"(off));
      touch = *(const GAS u32*)(pc - off + min(((jj % TCH) * 64 + lane) * TST, len));
    } else if (w <= 1) {
      touch = __builtin_amdgcn_raw_buffer_load_b32(sws, NTL & 1 ? 0 : min(lane, 26u) << 20, 0, 0);
    } else {
      const u32 m = (lane >> 2) & 7, o = (lane & 3) << 19;
      const char* p = m == 0 ? pwo : m == 1 ? pw1 : m == 2 ? pw2 : m == 3 ? pw3 : m == 4 ? pw4 : pout;
      touch = *(const GAS u32*)(NTL & 2 ? px : NTL & 4 && w == 3 ? px : p + (m < 3 ? 0 : o));
    }
#define TOUCH_IO
    __builtin_assume(b < 256 && w < 4);
    const Unit un = unit(b * 4 + w, lane);
    f32x16 accK, accV, accQ;
    u32x4 tu;
    tu[0] = un.off;
    f32x32 xf0, xf1, xf2, xf3;
    f32x4 sx4, ck, cv, cq, qa4, qb4;
    LANEC;
    asm volatile("" ::"s"(ep));
    const u32 tile = 2 * rb + rt, hp = 2 * hq + ch, hpk = hp ^ (2 * KXR), hpv = hp ^ (2 * VXR);
    u32x4 mf = {xl, xl ^ 32, xl ^ 64, xl ^ 96};
    float rstd = __uint_as_float(wl + 131072);
    const u32 wp = wl + 65536 + 2048 * ch + 4096 * rt;
    u32x4 wb = {wp, wp ^ 2048, wp ^ 4096, wp ^ 6144};
    u32 ex = L_EX + w * 1024 + lane * 4;
    {
      const u32 pi = (r31 & 19) | ((r31 & 4) << 1) | ((r31 & 8) >> 1);
      const u32 ko = WS_K + (2 * hpk * 64 + tile) * 1024 + h * 512 + pi * 16;
      const u32 vo = WS_V + ((2 * hpv + (r31 >> 4)) * 64 + tile) * 1024 + h * 256 + (r31 & 15) * 16;
      asm volatile("s_nop 1\n" P1_ASM
                   : "={v[64:95]}"(xf0), "={v[96:127]}"(xf1), "={v[128:159]}"(xf2), "={v[160:191]}"(xf3), "+{v[216:219]}"(mf), "={v222}"(rstd), "+{v[224:227]}"(tu),
                     "+{v[232:235]}"(wb), "+{v236}"(ex) TOUCH_IO POST_STV
                   : "{v[212:215]}"(sel4), "{v228}"(ko), "{v229}"(vo), "{v230}"(L_RST + w * 128 + r31 * 4), "{v231}"(L_RST + w * 128 + h * 16),
                     "{s[84:87]}"(sws), [p0] "s"(un.m ? pw1 : pwo)
                   : "memory", W_CLOB, KVE_CLOB, "v220", "v221", "v223", "v237");
    }
    auto csfrag = [&](f32x4 c) {
      const float t = c[0], hi = __uint_as_float(__float_as_uint(t) & 0xFFFF0000u);
      return u32x4{pk_bf16(hi, t - hi), 0, 0, 0};
    };
    STAMP(5);
#define Q_IN "{v[64:95]}"(xf0), "{v[96:127]}"(xf1), "{v[128:159]}"(xf2), "{v[160:191]}"(xf3), "{v[212:215]}"(sel4)
#define Q_IO , "+{v[232:235]}"(wb)
    bar();
    STAMP(6);
    asm volatile(Q0_ASM : "={v[0:15]}"(accQ), "={v[204:207]}"(cq) Q_IO : Q_IN : "memory", W_CLOB);
    STAMP(7);
    wait_vm<0>();
    asm volatile("" : "+v"(touch));
    tag = ep + 1;
    {
      u32 old;
      asm volatile("s_mov_b64 exec, 1\n\tds_add_rtn_u32 %0, %1, %2\n\ts_mov_b64 exec, -1\n\ts_waitcnt lgkmcnt(0)" : "=v"(old) : "v"(L_CNT), "v"(1u) : "memory");
      if (rfl(old) == 3 && lane == 0) __hip_atomic_store((GAS u64*)(pws + WS_KVFLAG + (hq * 4 + (rb >> 3)) * 128 + (rb & 7) * 8), tag, RLX_AGENT);
    }
    STAMP(19);
    asm volatile("" : "+{v[224:227]}"(tu));
    lds_w128(lds, L_TSC + w * 1024 + lane * 16, tu);
    bar();
    asm volatile(Q1_ASM : "+{v[0:15]}"(accQ), "+{v[204:207]}"(cq) Q_IO : Q_IN : "memory", W_CLOB);
    accQ = mfma32(csfrag(cq), mf, accQ);
    {
      const float qs = rstd * (0.25f * 1.4426950408889634f);
      u32x4 a, c;
      for (int i = 0; i < 4; i++) { a[i] = pk_bf16(accQ[2 * i] * qs, accQ[2 * i + 1] * qs); c[i] = pk_bf16(accQ[8 + 2 * i] * qs, accQ[9 + 2 * i] * qs); }
      lds_w128(lds, L_QX + w * 2048 + lane * 16, a);
      lds_w128(lds, L_QX + w * 2048 + 1024 + lane * 16, c);
    }
    td4[0] = un.dst;
    STAMP(8);
  } else {
    const u32 hw = w - 4;
    const u32 wv1 = wv0 + 8 * 3072, ws_ = 16 * hw * 3072 + hq * 128;
    auto w_issue = [&](int j, u32 col, u32 l) {
      const u32 wo = ws_ + ((j + rot) & 7) * (64 * 3072) + col;
      dma16(sw, wv0, wo, l + hw * 2048);
      dma16(sw, wv1, wo, l + hw * 2048 + 1024);
    };
    const u32 xs = (64 * rb + 16 * hw) * 1024, xv1 = (xv0 ^ 64) + 8192;
    auto item = [&](int t) {
      if (t < 8) {
        const u32 xo = xs + ((t + rot) & 7) * 128;
        dma16(sx, xv0, xo, 8192 * t + hw * 2048);
        dma16(sx, xv1, xo, 8192 * t + hw * 2048 + 1024);
        w_issue(t, kcol, l_k(t)); w_issue(t, vcol, l_k(t) + 8192);
      } else if (WQ) {
        for (int j = wq_lo(t); j < wq_lo(t + 1); j++) w_issue(j, 0, l_q(j));
      } else {
        for (int j = 3 * (t - 8); j < 3 * (t - 8) + 3 && j < 8; j++) w_issue(j ^ (QS == 1 ? 4 : 0), 0, l_q(j ^ (QS == 1 ? 4 : 0)));
      }
    };
#define unit_e(n) do {                                             \
      u32 bb = b * 4 + hw;                                           \
      asm volatile("" : "+s"(bb));                                   \
      __builtin_assume(bb < 1024);                                   \
      const Unit un = unit(((n) + 1) * 1024 + bb, lane);             \
      const char* p_ = un.m == 2 ? pw2 : un.m == 3 ? pw3 : pw4;      \
      u32 o_ = un.off, d_ = un.dst;                                  \
      asm volatile("" : "+s"(p_));                                   \
      asm volatile("" : "+v"(o_), "+v"(d_));                         \
      tsrc[(n) & 3] = p_; to4[(n) & 3] = o_; td4[(n) & 3] = d_;      \
    } while (0)
#pragma unroll
    for (int t = NC; t < DEP; t++) item(t);
    asm volatile("" ::"s"(ep));
    STAMP(16);
#pragma unroll
    for (int j = 0; j < 11 - DEP; j++) {
      asm volatile("s_waitcnt vmcnt(%0)" ::"n"(h_ops(DEP + j) - h_ops(j + 1)) : "memory");
      bar();
      if (j == 4) STAMP(3);
      if (FLG == 2 && j == 6) asm volatile("ds_write_b128 %0, %1" ::"v"(L_CNT), "v"(u32x4{0, 0, 0, 0}) : "memory");
      item(DEP + j);
      if (UDMA && j >= UJ && j < UJ + 4) unit_e(j - UJ);
    }
    STAMP(5);
#pragma unroll
    for (int j = 11 - DEP; j < 8; j++) {
      asm volatile("s_waitcnt vmcnt(%0)" ::"n"(h_ops(11) - h_ops(j + 1)) : "memory");
      bar();
    }
    STAMP(4);
#define UNITS() _Pragma("unroll") for (int n = 0; n < 4; n++) {   \
      asm volatile("s_mov_b32 m0, %0\n\ts_nop 0\n\tglobal_load_lds_dwordx4 %1, %2" UNT ::"s"(L_UN + hw * 4096 + n * 1024), "v"(to4[n]), "s"(tsrc[n]) : "memory"); \
    }
#pragma unroll
    for (int n = 0; n < UA; n++) if (DPJ < 8 || UJ + n >= 11 - DEP) unit_e(n);
    if (ULATE == 0) { UNITS(); }
    wait_vm<ULATE ? 8 : 12>(); bar();
    STAMP(6);
#pragma unroll
    for (int n = UA; n < 4; n++) if (DPJ < 8 || UJ + n >= 11 - DEP) unit_e(n);
    if (ULATE == 1) { UNITS(); }
    wait_vm<ULATE == 2 ? 0 : 4>(); bar();
    if (ULATE == 2) { UNITS(); }
    STAMP(7);
    tag = ep + 1;
    if (FLG == 1 && w == 4 && lane == 0) __hip_atomic_store((GAS u64*)(pws + WS_KVFLAG + (hq * 4 + (rb >> 3)) * 128 + (rb & 7) * 8), tag, RLX_AGENT);
    STAMP(8);
  }
  const f32x16 zero16 = {};
  u32 svoff = lane * 16, slast = WS_O + (b * 61 + 60) * 1024;
  f32x16 o;
  u32x4 q;
  f32x32 w4a;
  w4a[0] = __uint_as_float(L_TSC + w * 1024 + lane * 16);
  w4a[1] = __uint_as_float((UDMA && w >= 4 ? L_UN - 16384 + w * 4096 : L_TSC + w * 1024) + ((lane >> 2) & 3) * 128 + (lane & 3) * 32 + (lane >> 4) * 8);
  if (!UDMA) for (int n = 0; n < 4; n++) w4a[4 + 4 * n] = __uint_as_float(to4[n]);
  if (!ACKT) STAMP(10);
  const u32 tg = (u32)tag;
  u32 pr0;
  u32 hho, lim, oadr;
  u64 okm;
#define OK_OUT , "={s[88:89]}"(okm)
  asm volatile(ATT_ASM
               : "={v[0:15]}"(o), "={v81}"(pr0), "={v[228:231]}"(q), "+{v[160:163]}"(td4), "+{v[164:195]}"(w4a), "+{v240}"(kvoff), "+{v241}"(vvoff), "+{v248}"(svoff), "+{s74}"(slast),
                 "={v251}"(oadr), "={v252}"(hho), "={v253}"(lim) OK_OUT POST_STV
               : "{s[84:87]}"(sws), "{s68}"(tg), "{s100}"(WS_KVFLAG + (hq ^ VXR) * (FLG ? 512 : 1024)), "{s69}"((u32)(tag >> 32)), "{s70}"(hbase), "{s71}"((16 * ((xcd >> 1) & G0M) + kh) * 1024 - 16384),
                 "{s72}"(WS_KVFLAG + (hq ^ KXR) * (FLG ? 512 : 1024)), "{s73}"(WS_O + (b * 61 + w * 8) * 1024), "{s75}"(WS_WOT + ((jj & 7) * 64 + w * 8) * 1024),
                 "{s[38:39]}"(px), "{s[36:37]}"(pout), "{s78}"(b * 8), "{s79}"(L_QX + (hl >> 1) * 4096 + (hl & 1) * 1024), "{s56}"(w)
               : "memory", "scc", "vcc", ATT_CLOB);
  u32 p40, p41, p42, p43, p44, p45, p46, p47, p48, p49, p50, p51, p57, p58, p59;
  u64 p52, p54;
  asm volatile("" : "={s40}"(p40), "={s41}"(p41), "={s42}"(p42), "={s43}"(p43), "={s44}"(p44), "={s45}"(p45), "={s46}"(p46), "={s47}"(p47), "={s48}"(p48), "={s49}"(p49),
               "={s50}"(p50), "={s51}"(p51), "={s[52:53]}"(p52), "={s[54:55]}"(p54), "={s57}"(p57), "={s58}"(p58), "={s59}"(p59));
  STAMP(12);
  {
    float l = o[8];
    if (__builtin_expect(okm != ~0ull, 0)) {
      u32x4 k1 = {0, 0, 0, 0}, v1 = {0x3F803F80u, 0x3F803F80u, 0x3F803F80u, 0x3F803F80u}, v2 = v1;
      float m = -3.0e38f;
      o = zero16;
#pragma unroll 1
      for (u32 t = 0; t < 64; t++) {
        ring_issue(k1, v1, v2, sws, kvoff, vvoff, hbase + t * 1024, vmask);
        ring_wait(0, k1, v1, v2);
        f32x16 s = mfma32(k1, q, zero16);
        float mx = m;
        for (int i = 0; i < 16; i++) mx = fmaxf(mx, s[i]);
        mx = swap32_max(mx);
        const float al = __builtin_amdgcn_exp2f(m - mx);
        m = mx;
        for (int i = 0; i < 16; i++) { s[i] -= mx; o[i] *= al; }
        o = smf(v1, exp_lo(s), o);
        o = smf(v2, exp_hi(s), o);
      }
      l = o[8];
      const float il = __builtin_amdgcn_rcpf(l);
      u32x4 ov;
      for (int i = 0; i < 4; i++) ov[i] = pk_bf16(o[2 * i] * il, o[2 * i + 1] * il);
      st_c(sws, oadr, clampS(ov));
    }
  }

  STAMP(13);
  asm volatile(POST_ASM
               : "+{v252}"(hho), "+{v253}"(lim), "+{v[160:163]}"(td4), "+{v[164:195]}"(w4a), "+{v81}"(pr0) POST_STV
               : "{v248}"(svoff), "{s68}"(tg), "{s[84:87]}"(sws), "{s40}"(p40), "{s41}"(p41), "{s42}"(p42), "{s43}"(p43), "{s44}"(p44), "{s45}"(p45), "{s46}"(p46), "{s47}"(p47),
                 "{s48}"(p48), "{s49}"(p49), "{s50}"(p50), "{s51}"(p51), "{s[52:53]}"(p52), "{s[54:55]}"(p54), "{s56}"(w), "{s57}"(p57), "{s58}"(p58), "{s59}"(p59)
               : "memory", "scc", "vcc", POST_CLOB);
  STAMP(56);
  STAMP(14);
  STAMPR(63);
}

extern "C" void kernel_launch(void* const* d_in, const int* in_sizes, int n_in, void* d_out, int out_size, void* d_ws, size_t ws_size,
                              hipStream_t stream) {
  fused_block<<<256, 512, 0, stream>>>((const char*)d_in[0], (const char*)d_in[1], (const char*)d_in[2], (const char*)d_in[4], (const char*)d_in[5],
                                       (const char*)d_in[6], (const char*)d_in[7], (char*)d_out, (char*)d_ws + WSOFF);
}
```
